```python
import torch

def _softmax(x, axis=-1):
    return torch.softmax(x, dim=axis)
def _gelu(x):
    return torch.nn.functional.gelu(x, approximate="tanh")
def _silu(x):
    return torch.nn.functional.silu(x)
def _sigmoid(x):
    return torch.sigmoid(x)
def _layernorm(x, axis=-1, eps=1e-5):
    m = x.mean(dim=axis, keepdim=True)
    v = x.var(dim=axis, keepdim=True, unbiased=False)
    return (x - m) / torch.sqrt(v + eps)
def _scatter_add(tgt, idx, src, axis=0):
    return tgt.clone().index_add_(axis, idx, src)
def _segment_sum(src, seg, n):
    out = torch.zeros((n,) + src.shape[1:], dtype=src.dtype)
    return out.index_add_(0, seg, src)
def _pad(x, pw):
    flat = tuple(v for pair in reversed(pw) for v in pair)
    return torch.nn.functional.pad(x, flat)

kernel_name = 'gated_mix__sliding_block_cached__sliding_block_cached__decode'

def setup_inputs():

    _w = (lambda a: (lambda t: t.to(torch.bfloat16) if t.is_floating_point() else t)(torch.as_tensor(a)))
    def _A_setup():
        B, Sq, D = 1, 1, 512
        Hq, Hkv, Sk, Dh = 32, 8, 32768, 128
        assert Hq % Hkv == 0, (Hq, Hkv)
        return {
            "x": _w((lambda *s: torch.randn(*s, dtype=torch.bfloat16) * 0.05)(B, Sq, D)),
            "k_cache": _w((lambda *s: torch.randn(*s, dtype=torch.bfloat16) * 6.4)(B, Hkv, Sk, Dh)),
            "v_cache": _w((lambda *s: torch.randn(*s, dtype=torch.bfloat16) * 0.05)(B, Hkv, Sk, Dh)),
            "wq": _w((lambda *s: torch.randn(*s, dtype=torch.bfloat16) * 6.4)(D, Hq * Dh) * (D ** -0.5)),
            "wo": _w((lambda *s: torch.randn(*s, dtype=torch.bfloat16) * 0.05)(Hq * Dh, D) * ((Hq * Dh) ** -0.5)),
        }
    _A_in = _A_setup()

    def _B_setup():
        B, Sq, D = 1, 1, 512
        Hq, Hkv, Sk, Dh = 32, 8, 32768, 128
        assert Hq % Hkv == 0, (Hq, Hkv)
        return {
            "x": _w((lambda *s: torch.randn(*s, dtype=torch.bfloat16) * 0.05)(B, Sq, D)),
            "k_cache": _w((lambda *s: torch.randn(*s, dtype=torch.bfloat16) * 6.4)(B, Hkv, Sk, Dh)),
            "v_cache": _w((lambda *s: torch.randn(*s, dtype=torch.bfloat16) * 0.05)(B, Hkv, Sk, Dh)),
            "wq": _w((lambda *s: torch.randn(*s, dtype=torch.bfloat16) * 6.4)(D, Hq * Dh) * (D ** -0.5)),
            "wo": _w((lambda *s: torch.randn(*s, dtype=torch.bfloat16) * 0.05)(Hq * Dh, D) * ((Hq * Dh) ** -0.5)),
        }
    _B_in = _B_setup()

    return {
        "A_x": _A_in["x"],
        "A_k_cache": _A_in["k_cache"],
        "A_v_cache": _A_in["v_cache"],
        "A_wq": _A_in["wq"],
        "A_wo": _A_in["wo"],
        "B_x": _B_in["x"],
        "B_k_cache": _B_in["k_cache"],
        "B_v_cache": _B_in["v_cache"],
        "B_wq": _B_in["wq"],
        "B_wo": _B_in["wo"],
        "gate_w": _w((lambda *s: torch.randn(*s, dtype=torch.bfloat16) * 0.05)(512) * 16.0),
    }

def reference(A_x, A_k_cache, A_v_cache, A_wq, A_wo, B_x, B_k_cache, B_v_cache, B_wq, B_wo, gate_w):
    def _A_ref(x, k_cache, v_cache, wq, wo):
        B, Sq, D = x.shape
        Hq, Dh = 32, 128
        Hkv, Sk = k_cache.shape[1], k_cache.shape[2]
        W = min(4096, max(256, Sk // 4))
        rep = Hq // Hkv
        q = (lambda x, *d: x.permute(*d))((x @ wq).reshape(B, Sq, Hq, Dh), 0, 2, 1, 3)
        k = (lambda x, n, axis: torch.repeat_interleave(x, n, dim=axis))(k_cache[:, :, Sk - W:, :], rep, axis=1)
        v = (lambda x, n, axis: torch.repeat_interleave(x, n, dim=axis))(v_cache[:, :, Sk - W:, :], rep, axis=1)
        scores = torch.einsum("bhqd,bhkd->bhqk", q, k) * (Dh ** -0.5)
        p = _softmax(scores, axis=-1)
        o = torch.einsum("bhqk,bhkd->bhqd", p, v)
        return (lambda x, *d: x.permute(*d))(o, 0, 2, 1, 3).reshape(B, Sq, Hq * Dh) @ wo

    def _B_ref(x, k_cache, v_cache, wq, wo):
        B, Sq, D = x.shape
        Hq, Dh = 32, 128
        Hkv, Sk = k_cache.shape[1], k_cache.shape[2]
        W = min(4096, max(256, Sk // 4))
        rep = Hq // Hkv
        q = (lambda x, *d: x.permute(*d))((x @ wq).reshape(B, Sq, Hq, Dh), 0, 2, 1, 3)
        k = (lambda x, n, axis: torch.repeat_interleave(x, n, dim=axis))(k_cache[:, :, Sk - W:, :], rep, axis=1)
        v = (lambda x, n, axis: torch.repeat_interleave(x, n, dim=axis))(v_cache[:, :, Sk - W:, :], rep, axis=1)
        scores = torch.einsum("bhqd,bhkd->bhqk", q, k) * (Dh ** -0.5)
        p = _softmax(scores, axis=-1)
        o = torch.einsum("bhqk,bhkd->bhqd", p, v)
        return (lambda x, *d: x.permute(*d))(o, 0, 2, 1, 3).reshape(B, Sq, Hq * Dh) @ wo

    x = A_x
    g = _sigmoid((x * gate_w).sum(-1, keepdims=True))
    a = _A_ref(x, A_k_cache, A_v_cache, A_wq, A_wo)
    b = _B_ref(x, B_k_cache, B_v_cache, B_wq, B_wo)
    return g * a + (1 - g) * b
```

```cpp
#include <hip/hip_runtime.h>
#include <hip/hip_bf16.h>

typedef unsigned short u16;
typedef unsigned v4u __attribute__((ext_vector_type(4)));
typedef float f32x4 __attribute__((ext_vector_type(4)));
typedef __bf16 bf16x2 __attribute__((ext_vector_type(2)));

constexpr int D = 512, HQ = 32, HKV = 8, SK = 32768, DH = 128, W = 4096, NQ = HQ * DH;
constexpr int NSPLIT = 16, KEYS = W / NSPLIT;

__device__ __forceinline__ float bf2f(unsigned b) { return __builtin_bit_cast(float, b << 16); }
__device__ __forceinline__ float bflo(unsigned w) { return __builtin_bit_cast(float, w << 16); }
__device__ __forceinline__ float bfhi(unsigned w) { return __builtin_bit_cast(float, w & 0xffff0000u); }
__device__ __forceinline__ unsigned f2bf(float f) { unsigned u = __builtin_bit_cast(unsigned, f); return (u + 0x7fffu + ((u >> 16) & 1u)) >> 16; }
__device__ __forceinline__ float rbf(float f) { return bf2f(f2bf(f)); }
__device__ __forceinline__ float dot2(unsigned k, unsigned q, float acc) {
  return __builtin_amdgcn_fdot2_f32_bf16(__builtin_bit_cast(bf16x2, k), __builtin_bit_cast(bf16x2, q), acc, false);
}

__global__ void __launch_bounds__(256) k_qproj(const u16* x, const u16* wqA, const u16* wqB, u16* q) {
  const int br = blockIdx.x >> 7, cb = blockIdx.x & 127;
  const u16* wq = br ? wqB : wqA;
  const int tid = threadIdx.x, cq = tid & 3, r0 = tid >> 2;
  float acc[8] = {0, 0, 0, 0, 0, 0, 0, 0};
  __shared__ float red[64][33];
#pragma unroll
  for (int i = 0; i < 8; ++i) {
    const int r = r0 + 64 * i;
    const v4u w = *(const v4u*)(wq + (size_t)r * NQ + cb * 32 + cq * 8);
    const float xv = bf2f(x[r]);
#pragma unroll
    for (int j = 0; j < 4; ++j) { acc[2 * j] += xv * bflo(w[j]); acc[2 * j + 1] += xv * bfhi(w[j]); }
  }
#pragma unroll
  for (int j = 0; j < 8; ++j) red[r0][cq * 8 + j] = acc[j];
  __syncthreads();
  if (tid < 32) { float s = 0; for (int r = 0; r < 64; ++r) s += red[r][tid]; q[br * NQ + cb * 32 + tid] = (u16)f2bf(s); }
}

__global__ void __launch_bounds__(256) k_attn(const u16* q, const u16* kA, const u16* vA, const u16* kB, const u16* vB, float* pm, float* pl, float* po) {
  const int g = blockIdx.x >> 4, s = blockIdx.x & 15, br = g >> 3, kvh = g & 7;
  const u16* kp = (br ? kB : kA) + ((size_t)kvh * SK + (SK - W) + s * KEYS) * DH;
  const u16* vp = (br ? vB : vA) + ((size_t)kvh * SK + (SK - W) + s * KEYS) * DH;
  const int tid = threadIdx.x, lane = tid & 63, wave = tid >> 6, c = lane & 15, r = lane >> 4;
  __shared__ float sc[4][KEYS];
  __shared__ float red[4][4][DH];
  __shared__ float sm[4], sl[4];
  v4u qv[4];
#pragma unroll
  for (int h = 0; h < 4; ++h) qv[h] = *(const v4u*)(q + br * NQ + (kvh * 4 + h) * DH + c * 8);
  for (int i = 0; i < 16; ++i) {
    const int key = wave * 64 + i * 4 + r;
    const v4u kv = *(const v4u*)(kp + (size_t)key * DH + c * 8);
#pragma unroll
    for (int h = 0; h < 4; ++h) {
      float d = 0;
#pragma unroll
      for (int j = 0; j < 4; ++j) d = dot2(kv[j], qv[h][j], d);
      d += __shfl_xor(d, 1); d += __shfl_xor(d, 2); d += __shfl_xor(d, 4); d += __shfl_xor(d, 8);
      if (c == 0) sc[h][key] = d * 0.08838834764831845f;
    }
  }
  __syncthreads();
  {
    const int h = wave; float v[4]; float m = -INFINITY;
#pragma unroll
    for (int i = 0; i < 4; ++i) { v[i] = sc[h][lane + 64 * i]; m = fmaxf(m, v[i]); }
    for (int o = 32; o; o >>= 1) m = fmaxf(m, __shfl_xor(m, o));
    float l = 0;
#pragma unroll
    for (int i = 0; i < 4; ++i) { v[i] = __expf(v[i] - m); l += v[i]; sc[h][lane + 64 * i] = v[i]; }
    for (int o = 32; o; o >>= 1) l += __shfl_xor(l, o);
    if (lane == 0) { sm[h] = m; sl[h] = l; }
  }
  __syncthreads();
  float acc[4][8];
#pragma unroll
  for (int h = 0; h < 4; ++h)
#pragma unroll
    for (int j = 0; j < 8; ++j) acc[h][j] = 0;
  for (int i = 0; i < 16; ++i) {
    const int key = wave * 64 + i * 4 + r;
    const v4u vv = *(const v4u*)(vp + (size_t)key * DH + c * 8);
#pragma unroll
    for (int h = 0; h < 4; ++h) {
      const float p = sc[h][key];
#pragma unroll
      for (int j = 0; j < 4; ++j) { acc[h][2 * j] += p * bflo(vv[j]); acc[h][2 * j + 1] += p * bfhi(vv[j]); }
    }
  }
#pragma unroll
  for (int h = 0; h < 4; ++h)
#pragma unroll
    for (int j = 0; j < 8; ++j) { float a = acc[h][j]; a += __shfl_xor(a, 16); a += __shfl_xor(a, 32); if (r == 0) red[wave][h][c * 8 + j] = a; }
  __syncthreads();
  for (int e = tid; e < 4 * DH; e += 256) {
    const int h = e >> 7, d = e & 127;
    po[((size_t)(g * 4 + h) * NSPLIT + s) * DH + d] = red[0][h][d] + red[1][h][d] + red[2][h][d] + red[3][h][d];
  }
  if (tid < 4) { pm[(g * 4 + tid) * NSPLIT + s] = sm[tid]; pl[(g * 4 + tid) * NSPLIT + s] = sl[tid]; }
}

__global__ void __launch_bounds__(256) k_wo(const float* pm, const float* pl, const float* po, const u16* woA, const u16* woB, float* part) {
  const int br = blockIdx.x >> 7, rb = blockIdx.x & 127, hq = rb >> 2, d0 = (rb & 3) * 32;
  const u16* wo = (br ? woB : woA) + (size_t)rb * 32 * D;
  const int tid = threadIdx.x;
  __shared__ float ov[32];
  __shared__ float red[4][D];
  const int hh = br * HQ + hq;
  if (tid < 32) {
    float M = -INFINITY;
    for (int s = 0; s < NSPLIT; ++s) M = fmaxf(M, pm[hh * NSPLIT + s]);
    float L = 0, o = 0;
    for (int s = 0; s < NSPLIT; ++s) { const float w = __expf(pm[hh * NSPLIT + s] - M); L += w * pl[hh * NSPLIT + s]; o += w * po[((size_t)hh * NSPLIT + s) * DH + d0 + tid]; }
    ov[tid] = rbf(o / L);
  }
  __syncthreads();
  const int wave = tid >> 6, lane = tid & 63;
  float acc[8] = {0, 0, 0, 0, 0, 0, 0, 0};
#pragma unroll
  for (int i = 0; i < 8; ++i) {
    const int r = wave * 8 + i;
    const v4u w = *(const v4u*)(wo + (size_t)r * D + lane * 8);
    const float o = ov[r];
#pragma unroll
    for (int j = 0; j < 4; ++j) { acc[2 * j] += o * bflo(w[j]); acc[2 * j + 1] += o * bfhi(w[j]); }
  }
#pragma unroll
  for (int j = 0; j < 8; ++j) red[wave][lane * 8 + j] = acc[j];
  __syncthreads();
  for (int e = tid; e < D; e += 256) part[(size_t)blockIdx.x * D + e] = red[0][e] + red[1][e] + red[2][e] + red[3][e];
}

__global__ void __launch_bounds__(256) k_final(const float* part, const u16* x, const u16* gw, u16* out) {
  const int tid = threadIdx.x, c = tid & 63, rq = tid >> 6;
  const int col = blockIdx.x * 64 + c;
  __shared__ float red[2][4][64];
  __shared__ float gs[4];
  float a = 0, b = 0;
  for (int i = rq; i < 128; i += 4) { a += part[(size_t)i * D + col]; b += part[(size_t)(128 + i) * D + col]; }
  red[0][rq][c] = a; red[1][rq][c] = b;
  float gsum = 0;
  for (int i = tid; i < D; i += 256) gsum += rbf(bf2f(x[i]) * bf2f(gw[i]));
  for (int o = 32; o; o >>= 1) gsum += __shfl_xor(gsum, o);
  if ((tid & 63) == 0) gs[tid >> 6] = gsum;
  __syncthreads();
  if (tid < 64) {
    const float A = rbf(red[0][0][c] + red[0][1][c] + red[0][2][c] + red[0][3][c]);
    const float B = rbf(red[1][0][c] + red[1][1][c] + red[1][2][c] + red[1][3][c]);
    const float z = rbf(gs[0] + gs[1] + gs[2] + gs[3]);
    const float g = rbf(1.f / (1.f + __expf(-z)));
    const float g1 = rbf(1.f - g);
    out[col] = (u16)f2bf(rbf(g * A) + rbf(g1 * B));
  }
}

extern "C" void kernel_launch(void* const* d_in, const int* in_sizes, int n_in, void* d_out, int out_size, void* d_ws, size_t ws_size, hipStream_t stream) {
  const u16* Ax = (const u16*)d_in[0]; const u16* Ak = (const u16*)d_in[1]; const u16* Av = (const u16*)d_in[2]; const u16* Awq = (const u16*)d_in[3]; const u16* Awo = (const u16*)d_in[4];
  const u16* Bk = (const u16*)d_in[6]; const u16* Bv = (const u16*)d_in[7]; const u16* Bwq = (const u16*)d_in[8]; const u16* Bwo = (const u16*)d_in[9]; const u16* gw = (const u16*)d_in[10];
  char* ws = (char*)d_ws;
  u16* q = (u16*)ws;
  float* pm = (float*)(ws + 65536);
  float* pl = (float*)(ws + 65536 * 2);
  float* po = (float*)(ws + 65536 * 3);
  float* part = (float*)(ws + 65536 * 3 + 524288);
  k_qproj<<<256, 256, 0, stream>>>(Ax, Awq, Bwq, q);
  k_attn<<<256, 256, 0, stream>>>(q, Ak, Av, Bk, Bv, pm, pl, po);
  k_wo<<<256, 256, 0, stream>>>(pm, pl, po, Awo, Bwo, part);
  k_final<<<8, 256, 0, stream>>>(part, Ax, gw, (u16*)d_out);
}
```

```cpp
#include <hip/hip_runtime.h>
#include <hip/hip_bf16.h>

typedef unsigned short u16;
typedef unsigned v4u __attribute__((ext_vector_type(4)));
typedef unsigned v2u __attribute__((ext_vector_type(2)));
typedef float f32x4 __attribute__((ext_vector_type(4)));
typedef short s16x4 __attribute__((ext_vector_type(4)));
typedef short s16x8 __attribute__((ext_vector_type(8)));
typedef __bf16 bf16x2 __attribute__((ext_vector_type(2)));
typedef __bf16 bf16x8 __attribute__((ext_vector_type(8)));
#define GAS __attribute__((address_space(1)))
#define LAS __attribute__((address_space(3)))
#define RLX_AGENT __ATOMIC_RELAXED, __HIP_MEMORY_SCOPE_AGENT

constexpr int DM = 512, SK = 32768, DH = 128, WIN = 4096, NQ = 4096;
constexpr int NWG = 256, NTHR = 512;
constexpr int L_K = 0, L_WO = 0, L_V = 65536, L_P = 131072, P_STR = 576, L_Q = L_P + 4 * P_STR, Q_STR = 320, L_QP = L_Q + 4 * Q_STR,
              L_MX = L_QP + 1024, L_LS = L_MX + 64, L_O = L_LS + 64, L_RED = L_O + 64, L_CNT = L_RED + 512, L_END = L_CNT + 64;
enum { C_QP = L_CNT, C_QRDY = L_CNT + 4, C_KL = L_CNT + 8, C_MX = L_CNT + 12, C_QK = L_CNT + 16, C_TAG = L_CNT + 20 };
constexpr unsigned W_E = 0, W_Q = 4096, W_ML = W_Q + 16 * 8192, W_O = W_ML + 16384, W_F = W_O + (1u << 20), W_END = W_F + (1u << 20);
constexpr unsigned SPIN_CAP = 1u << 20;

#define STAMP(i) do { } while (0)

struct Params { const u16* x; const u16* gw; const u16* wq[2]; const u16* k[2]; const u16* v[2]; const u16* wo[2]; u16* out; unsigned char* ws; };

__device__ __forceinline__ float bflo(unsigned w) { return __builtin_bit_cast(float, w << 16); }
__device__ __forceinline__ float bfhi(unsigned w) { return __builtin_bit_cast(float, w & 0xffff0000u); }
typedef float f32x2 __attribute__((ext_vector_type(2)));
__device__ __forceinline__ unsigned cvtpk(float lo, float hi) { return __builtin_bit_cast(unsigned, __builtin_convertvector(f32x2{lo, hi}, bf16x2)); }
__device__ __forceinline__ float rbf(float f) { return bflo(cvtpk(f, f)); }
__device__ __forceinline__ float dot2(unsigned a, unsigned b, float acc) { return __builtin_amdgcn_fdot2_f32_bf16(__builtin_bit_cast(bf16x2, a), __builtin_bit_cast(bf16x2, b), acc, false); }
template <int CTRL> __device__ __forceinline__ float dpp(float x) { return __builtin_bit_cast(float, __builtin_amdgcn_mov_dpp(__builtin_bit_cast(int, x), CTRL, 0xf, 0xf, true)); }
__device__ __forceinline__ float u2f(unsigned u) { return __builtin_bit_cast(float, u); }
__device__ __forceinline__ unsigned f2u(float f) { return __builtin_bit_cast(unsigned, f); }
__device__ __forceinline__ float rs32(float a, float b) { auto r = __builtin_amdgcn_permlane32_swap(f2u(a), f2u(b), false, false); return u2f(r[0]) + u2f(r[1]); }
__device__ __forceinline__ float rs16(float a, float b) { auto r = __builtin_amdgcn_permlane16_swap(f2u(a), f2u(b), false, false); return u2f(r[0]) + u2f(r[1]); }
__device__ __forceinline__ float xrow_max(float x) {
  auto s = __builtin_amdgcn_permlane16_swap(f2u(x), f2u(x), false, false); x = fmaxf(u2f(s[0]), u2f(s[1]));
  auto t = __builtin_amdgcn_permlane32_swap(f2u(x), f2u(x), false, false); return fmaxf(u2f(t[0]), u2f(t[1]));
}
__device__ __forceinline__ float xrow_sum(float x) { x = rs16(x, x); return rs32(x, x); }
__device__ __forceinline__ f32x4 mfma(v4u a, v4u b, f32x4 c) { return __builtin_amdgcn_mfma_f32_16x16x32_bf16(__builtin_bit_cast(bf16x8, a), __builtin_bit_cast(bf16x8, b), c, 0, 0, 0); }
__device__ __forceinline__ v4u tr_pair(const LAS unsigned char* a0, int off2) {
  const s16x4 lo = __builtin_amdgcn_ds_read_tr16_b64_v4i16((LAS s16x4*)a0), hi = __builtin_amdgcn_ds_read_tr16_b64_v4i16((LAS s16x4*)(a0 + off2));
  return __builtin_bit_cast(v4u, __builtin_shufflevector(lo, hi, 0, 1, 2, 3, 4, 5, 6, 7));
}
template <int STRIDE> __device__ __forceinline__ void dma_run(const void* sbase, unsigned voff, unsigned ldst, unsigned n) {
  asm volatile("s_mov_b32 m0, %3\n\ts_nop 0\n"
               "1:\n\tglobal_load_lds_dwordx4 %0, %2 nt\n\tv_add_u32 %0, %4, %0\n\ts_add_u32 m0, m0, %4\n\ts_sub_u32 %1, %1, 1\n\ts_cmp_lg_u32 %1, 0\n\ts_cbranch_scc1 1b"
               : "+v"(voff), "+s"(n) : "s"(sbase), "s"(ldst), "n"(STRIDE) : "memory", "scc");
}
#define WAIT_VM(n) asm volatile("s_waitcnt vmcnt(" #n ")" ::: "memory")
#define WAIT_LGKM() asm volatile("s_waitcnt lgkmcnt(0)" ::: "memory")
#define BARRIER() asm volatile("s_waitcnt lgkmcnt(0)\n\ts_barrier" ::: "memory")
__device__ __forceinline__ unsigned lds_add(unsigned addr) {
  unsigned old, one = 1u;
  asm volatile("s_waitcnt lgkmcnt(0)\n\ts_mov_b64 exec, 1\n\tds_add_rtn_u32 %0, %1, %2\n\ts_waitcnt lgkmcnt(0)\n\ts_mov_b64 exec, -1" : "=&v"(old) : "v"(addr), "v"(one) : "memory");
  return __builtin_amdgcn_readfirstlane(old);
}
__device__ __forceinline__ void lds_wait(const LAS unsigned char* L, unsigned addr, unsigned want) {
#pragma clang loop unroll(disable)
  for (unsigned i = 0; i < SPIN_CAP; ++i) { if (__builtin_amdgcn_readfirstlane(*(volatile LAS unsigned*)(L + addr)) >= want) break; __builtin_amdgcn_s_sleep(1); }
  asm volatile("" ::: "memory");
}
#define RELOAD() asm volatile("" ::: "memory")
__device__ __forceinline__ void st_granule(unsigned char* ws, unsigned off, float v, unsigned tag) {
  __hip_atomic_store((GAS unsigned long long*)(ws + off), ((unsigned long long)tag << 32) | f2u(v), RLX_AGENT);
}

__global__ void __launch_bounds__(NTHR, 2) fused_decode(Params p) {
  __shared__ __attribute__((aligned(16))) unsigned char lds_raw[L_END];
  LAS unsigned char* L = (LAS unsigned char*)lds_raw;
  const int tid = threadIdx.x, lane = tid & 63, wave = __builtin_amdgcn_readfirstlane(tid >> 6);
  const int b = blockIdx.x, g = b & 15, s = b >> 4, br = g >> 3, kvh = g & 7;
  const int lr = lane >> 4, lc = lane & 15;
  const __amdgpu_buffer_rsrc_t wsr = __builtin_amdgcn_make_buffer_rsrc(p.ws, 0, W_END, 0x00020000);
  unsigned tag;
  float gate = 0.f;

  if (wave < 4) {
    __builtin_amdgcn_s_setprio(2);
    if (wave == 0) STAMP(0);
    const int kh = s >> 3, cb = s & 7, row0 = 256 * kh + 64 * wave + 8 * (lane >> 3);
    const GAS v4u* wp = (const GAS v4u*)(p.wq[br] + (size_t)row0 * NQ + kvh * 512 + 64 * cb + 8 * (lane & 7));
    const v4u xv = *(const GAS v4u*)(p.x + row0);
    v4u w[8];
#pragma unroll
    for (int i = 0; i < 8; ++i) w[i] = __builtin_nontemporal_load(wp + (size_t)i * (NQ / 8));
    RELOAD();
    unsigned ep = __hip_atomic_load((GAS unsigned*)(p.ws + W_E), RLX_AGENT);
    v4u gx = *(const GAS v4u*)(p.x + 8 * lane), gg = *(const GAS v4u*)(p.gw + 8 * lane);
    BARRIER();
    asm volatile("" : "+v"(w[0]), "+v"(w[1]));
    float acc[8];
#pragma unroll
    for (int k = 0; k < 8; ++k) acc[k] = 0.f;
#pragma unroll
    for (int i = 0; i < 8; i += 2)
#pragma unroll
      for (int d = 0; d < 4; ++d) {
        acc[2 * d] = dot2(__builtin_amdgcn_perm(w[i + 1][d], w[i][d], 0x05040100u), xv[i >> 1], acc[2 * d]);
        acc[2 * d + 1] = dot2(__builtin_amdgcn_perm(w[i + 1][d], w[i][d], 0x07060302u), xv[i >> 1], acc[2 * d + 1]);
      }
    if (wave == 0) STAMP(1);
    asm volatile("" : "+v"(ep));
    tag = ep + 1u;
    if (wave == 0 && lane == 0) *(LAS unsigned*)(L + C_TAG) = tag;
    {
      const float t0 = rs32(acc[0], acc[4]), t1 = rs32(acc[1], acc[5]), t2 = rs32(acc[2], acc[6]), t3 = rs32(acc[3], acc[7]);
      const float u0 = rs16(t0, t2), u1 = rs16(t1, t3);
      const float a0 = u0 + dpp<0x128>(u0), a1 = u1 + dpp<0x128>(u1);
      const int col = 8 * (lane & 7) + ((lane >> 3) & 1) + 2 * ((lane >> 4) & 1) + 4 * (lane >> 5);
      ((LAS float*)(L + L_QP))[wave * 64 + col] = (lane & 8) ? a1 : a0;
    }
    if (lds_add(C_QP) == 3) {
      const LAS float* qp = (const LAS float*)(L + L_QP);
      st_granule(p.ws, W_Q + (g * 2 + kh) * 4096 + (64 * cb + lane) * 8, (qp[lane] + qp[64 + lane]) + (qp[128 + lane] + qp[192 + lane]), tag);
      STAMP(2);
    }
    if (wave == 0) {
      float z = 0.f;
#pragma unroll
      for (int d = 0; d < 4; ++d) z += rbf(bflo(gx[d]) * bflo(gg[d])) + rbf(bfhi(gx[d]) * bfhi(gg[d]));
      z += dpp<0xB1>(z); z += dpp<0x4E>(z); z += dpp<0x124>(z); z += dpp<0x128>(z); z = xrow_sum(z);
      gate = rbf(__builtin_amdgcn_rcpf(1.f + __expf(-rbf(z))));
    }
    {
      const int off = W_Q + g * 8192 + (128 * wave + 2 * lane) * 8;
      v4u a0, a1;
#pragma clang loop unroll(disable)
      for (unsigned i = 0; i < SPIN_CAP; ++i) {
        RELOAD();
        a0 = __builtin_amdgcn_raw_buffer_load_b128(wsr, off, 0, 16); a1 = __builtin_amdgcn_raw_buffer_load_b128(wsr, off + 4096, 0, 16);
        if (__all(a0[1] == tag && a0[3] == tag && a1[1] == tag && a1[3] == tag)) break;
      }
      *(LAS unsigned*)(L + L_Q + wave * Q_STR + 4 * lane) = cvtpk(u2f(a0[0]) + u2f(a1[0]), u2f(a0[2]) + u2f(a1[2]));
      lds_add(C_QRDY);
      if (wave == 0) STAMP(3);
    }
    lds_wait(L, C_QRDY, 4); lds_wait(L, C_KL, 4);
    if (wave == 0) STAMP(5);
    f32x4 sc[4];
    {
      v4u qf[4];
#pragma unroll
      for (int ks = 0; ks < 4; ++ks) qf[ks] = *(const LAS v4u*)(L + L_Q + (lane & 3) * Q_STR + 64 * ks + 16 * lr);
      const unsigned ka = L_K + (64 * wave + lc) * 256 + ((lr ^ lc) << 4);
#pragma unroll
      for (int t = 0; t < 4; ++t) {
        sc[t] = f32x4{0.f, 0.f, 0.f, 0.f};
#pragma unroll
        for (int ks = 0; ks < 4; ++ks) sc[t] = mfma(*(const LAS v4u*)(L + (ka ^ (64 * ks)) + 4096 * t), qf[ks], sc[t]);
      }
    }
    float m = fmaxf(fmaxf(sc[0][0], sc[0][1]), fmaxf(sc[0][2], sc[0][3]));
#pragma unroll
    for (int t = 1; t < 4; ++t) m = fmaxf(m, fmaxf(fmaxf(sc[t][0], sc[t][1]), fmaxf(sc[t][2], sc[t][3])));
    m = xrow_max(m);
    if (lane < 4) ((LAS float*)(L + L_MX))[lane * 4 + wave] = m;
    lds_add(C_MX); lds_wait(L, C_MX, 4);
    constexpr float C2 = 0.08838834764831845f * 1.4426950408889634f;
    const f32x4 mx = *(const LAS f32x4*)(L + L_MX + 16 * (lane & 3));
    const float mc = fmaxf(fmaxf(mx[0], mx[1]), fmaxf(mx[2], mx[3])) * C2;
    float ls = 0.f;
#pragma unroll
    for (int t = 0; t < 4; ++t) {
      float e[4];
#pragma unroll
      for (int r = 0; r < 4; ++r) { e[r] = __builtin_amdgcn_exp2f(fmaf(sc[t][r], C2, -mc)); ls += e[r]; }
      if ((lane & 12) == 0) *(LAS v2u*)(L + L_P + (lane & 3) * P_STR + (64 * wave + 16 * t + 4 * lr) * 2) = v2u{cvtpk(e[0], e[1]), cvtpk(e[2], e[3])};
    }
    ls = xrow_sum(ls);
    if (lane < 4) ((LAS float*)(L + L_LS))[lane * 4 + wave] = ls;
    if (lds_add(C_QK) == 3) {
      if (lane < 4) {
        const f32x4 l4 = *(const LAS f32x4*)(L + L_LS + 16 * lane);
        __builtin_amdgcn_raw_buffer_store_b128(v4u{f2u(mc), tag, f2u((l4[0] + l4[1]) + (l4[2] + l4[3])), tag}, wsr, W_ML + ((g * 16 + s) * 4 + lane) * 16, 0, 16);
      }
      STAMP(6);
    }
  } else {
    const int j = wave - 4;
    if (j == 0 && lane < 5) *(LAS unsigned*)(L + L_CNT + 4 * lane) = 0u;
    const size_t kvoff = ((size_t)kvh * SK + (SK - WIN) + s * 256) * DH;
    const unsigned char* kb = (const unsigned char*)(p.k[br] + kvoff) + j * 1024;
    const unsigned char* vb = (const unsigned char*)(p.v[br] + kvoff) + j * 1024;
    const unsigned char* wb = (const unsigned char*)(p.wo[br] + ((size_t)kvh * 512 + s * 32) * DM) + j * 2048;
    const unsigned ko = lr * 256 + ((lc ^ (4 * j + lr)) << 4);
    const unsigned vo = lr * 256 + ((lc ^ ((lr | ((j & 2) << 1)) << 1)) << 4);
    BARRIER();
    dma_run<4096>(kb, ko, L_K + j * 1024, 16);
    dma_run<4096>(vb, vo, L_V + j * 1024, 8);
    WAIT_VM(8);
    lds_add(C_KL);
    if (j == 0) STAMP(4);
    dma_run<4096>(vb, vo + 32768, L_V + 32768 + j * 1024, 8);
    if (j == 0) STAMP(7);
    lds_wait(L, C_QK, 4);
    dma_run<8192>(wb, (unsigned)((lane ^ (4 * j)) << 4), L_WO + j * 2048, 4);
    dma_run<8192>(wb + 1024, (unsigned)((lane ^ (4 * j + 2)) << 4), L_WO + j * 2048 + 1024, 4);
    WAIT_VM(8);
    if (j == 0) STAMP(8);
  }
  BARRIER();
  tag = *(const LAS unsigned*)(L + C_TAG);
  {
    const int q4 = lc >> 2, p4 = lane & 3;
    const LAS unsigned char* va = L + L_V + (8 * lr + q4) * 256 + (((2 * wave + (p4 >> 1)) ^ ((q4 | ((lr & 1) << 2)) << 1)) << 4) + 8 * (p4 & 1);
    const LAS unsigned char* pa = L + L_P + p4 * P_STR + 16 * lr;
    f32x4 o = {0.f, 0.f, 0.f, 0.f};
#pragma unroll
    for (int ks = 0; ks < 8; ++ks) o = mfma(tr_pair(va + 8192 * ks, 1024), *(const LAS v4u*)(pa + 64 * ks), o);
    const float ov = q4 == 0 ? o[0] : q4 == 1 ? o[1] : q4 == 2 ? o[2] : o[3];
    st_granule(p.ws, W_O + ((g * 16 + 4 * p4 + (wave >> 1)) * 16 + s) * 256 + (16 * (wave & 1) + 4 * lr + q4) * 8, ov, tag);
    if (wave == 0) STAMP(9);
  }
  if (wave == 0) {
    const int off = W_O + (g * 16 + s) * 4096 + lane * 16, moff = W_ML + ((g * 16 + lr) * 4 + (s >> 2)) * 16;
    v4u d[4], ml[4];
#pragma clang loop unroll(disable)
    for (unsigned it = 0; it < SPIN_CAP; ++it) {
      RELOAD();
      bool ok = true;
#pragma unroll
      for (int i = 0; i < 4; ++i) { d[i] = __builtin_amdgcn_raw_buffer_load_b128(wsr, off + i * 1024, 0, 16); ml[i] = __builtin_amdgcn_raw_buffer_load_b128(wsr, moff + i * 256, 0, 16); }
#pragma unroll
      for (int i = 0; i < 4; ++i) ok &= d[i][1] == tag && d[i][3] == tag && ml[i][1] == tag && ml[i][3] == tag;
      if (__all(ok)) break;
    }
    const float M = xrow_max(fmaxf(fmaxf(u2f(ml[0][0]), u2f(ml[1][0])), fmaxf(u2f(ml[2][0]), u2f(ml[3][0]))));
    float Ls = 0.f, o0 = 0.f, o1 = 0.f;
#pragma unroll
    for (int i = 0; i < 4; ++i) { const float wgt = __builtin_amdgcn_exp2f(u2f(ml[i][0]) - M); Ls = fmaf(wgt, u2f(ml[i][2]), Ls); o0 = fmaf(wgt, u2f(d[i][0]), o0); o1 = fmaf(wgt, u2f(d[i][2]), o1); }
    Ls = xrow_sum(Ls); o0 = xrow_sum(o0); o1 = xrow_sum(o1);
    const float inv = __builtin_amdgcn_rcpf(Ls);
    if (lane < 16) *(LAS unsigned*)(L + L_O + 16 * ((lane >> 1) & 3) + 8 * (lane >> 3) + 4 * (lane & 1)) = cvtpk(o0 * inv, o1 * inv);
    STAMP(10);
  }
  if (wave >= 4) WAIT_VM(0);
  BARRIER();
  if (wave == 0) STAMP(11);
  {
    const int q4 = lc >> 2, p4 = lane & 3;
    const v4u of = *(const LAS v4u*)(L + L_O + 16 * lr);
    const unsigned wa = L_WO + (4 * lr + q4) * 1024 + (((8 * wave + (p4 >> 1)) ^ ((q4 | ((lr & 1) << 2)) << 1)) << 4) + 8 * (p4 & 1);
    f32x4 o = {0.f, 0.f, 0.f, 0.f};
#pragma unroll
    for (int t = 0; t < 4; ++t) { const v4u z = {0u, 0u, 0u, 0u}; o = mfma(tr_pair(L + (wa ^ (32 * t)), 16384), p4 == t ? of : z, o); }
    const float ov = q4 == 0 ? o[0] : q4 == 1 ? o[1] : q4 == 2 ? o[2] : o[3];
    st_granule(p.ws, W_F + ((4 * wave + p4) * 256 + br * 128 + kvh * 16 + s) * 128 + (4 * lr + q4) * 8, ov, tag);
    if (wave == 0) STAMP(12);
  }
  if (b < 32) {
    const int off = W_F + b * 32768 + wave * 4096 + lane * 16;
    v4u d[4];
#pragma clang loop unroll(disable)
    for (unsigned it = 0; it < SPIN_CAP; ++it) {
      RELOAD();
      bool ok = true;
#pragma unroll
      for (int i = 0; i < 4; ++i) d[i] = __builtin_amdgcn_raw_buffer_load_b128(wsr, off + i * 1024, 0, 16);
#pragma unroll
      for (int i = 0; i < 4; ++i) ok &= d[i][1] == tag && d[i][3] == tag;
      if (__all(ok)) break;
    }
    float t = rs32((u2f(d[0][0]) + u2f(d[1][0])) + (u2f(d[2][0]) + u2f(d[3][0])), (u2f(d[0][2]) + u2f(d[1][2])) + (u2f(d[2][2]) + u2f(d[3][2])));
    t = rs16(t, t); t += dpp<0x128>(t);
    if ((lane & 24) == 0) ((LAS float*)(L + L_RED))[wave * 16 + 2 * (lane & 7) + (lane >> 5)] = t;
    if (wave == 0) STAMP(13);
    BARRIER();
    if (wave == 0) {
      if (lane < 16) {
        const LAS float* r = (const LAS float*)(L + L_RED) + lane;
        const float A = rbf((r[0] + r[16]) + (r[32] + r[48])), B = rbf((r[64] + r[80]) + (r[96] + r[112]));
        p.out[16 * b + lane] = (u16)(cvtpk(rbf(gate * A) + rbf(rbf(1.f - gate) * B), 0.f) & 0xffffu);
      }
      if (b == 0 && lane == 0) __hip_atomic_store((GAS unsigned*)(p.ws + W_E), tag, RLX_AGENT);
      STAMP(14);
    }
  }
}

extern "C" void kernel_launch(void* const* d_in, const int* in_sizes, int n_in, void* d_out, int out_size, void* d_ws, size_t ws_size, hipStream_t stream) {
  Params p;
  p.x = (const u16*)d_in[0]; p.gw = (const u16*)d_in[10];
  p.wq[0] = (const u16*)d_in[3]; p.wq[1] = (const u16*)d_in[8];
  p.k[0] = (const u16*)d_in[1]; p.k[1] = (const u16*)d_in[6];
  p.v[0] = (const u16*)d_in[2]; p.v[1] = (const u16*)d_in[7];
  p.wo[0] = (const u16*)d_in[4]; p.wo[1] = (const u16*)d_in[9];
  p.out = (u16*)d_out; p.ws = (unsigned char*)d_ws;
  fused_decode<<<NWG, NTHR, 0, stream>>>(p);
}
```

```cpp
#define AMP_STAMP 1
#include <hip/hip_runtime.h>
#include <hip/hip_bf16.h>

typedef unsigned short u16;
typedef unsigned v4u __attribute__((ext_vector_type(4)));
typedef unsigned v2u __attribute__((ext_vector_type(2)));
typedef float f32x4 __attribute__((ext_vector_type(4)));
typedef short s16x4 __attribute__((ext_vector_type(4)));
typedef short s16x8 __attribute__((ext_vector_type(8)));
typedef __bf16 bf16x2 __attribute__((ext_vector_type(2)));
typedef __bf16 bf16x8 __attribute__((ext_vector_type(8)));
#define GAS __attribute__((address_space(1)))
#define LAS __attribute__((address_space(3)))
#define RLX_AGENT __ATOMIC_RELAXED, __HIP_MEMORY_SCOPE_AGENT

constexpr int DM = 512, SK = 32768, DH = 128, WIN = 4096, NQ = 4096;
constexpr int NWG = 256, NTHR = 512;
constexpr int L_K = 0, L_WO = 0, L_V = 65536, L_P = 131072, P_STR = 576, L_Q = L_P + 4 * P_STR, Q_STR = 320, L_QP = L_Q + 4 * Q_STR,
              L_MX = L_QP + 1024, L_LS = L_MX + 64, L_O = L_LS + 64, L_RED = L_O + 64, L_CNT = L_RED + 512, L_END = L_CNT + 64;
enum { C_QP = L_CNT, C_QRDY = L_CNT + 4, C_KL = L_CNT + 8, C_MX = L_CNT + 12, C_QK = L_CNT + 16, C_TAG = L_CNT + 20 };
constexpr unsigned W_E = 0, W_Q = 4096, W_ML = W_Q + 16 * 8192, W_O = W_ML + 16384, W_F = W_O + (1u << 20), W_END = W_F + (1u << 20);
constexpr unsigned SPIN_CAP = 1u << 20;

#define STAMP(i) do { if ((i) == 0 || (i) == AMP_STAMP) { if (lane == 0) *(volatile LAS unsigned long long*)(L + L_CNT + 32 + ((i) == 0 ? 0 : 8)) = __builtin_amdgcn_s_memrealtime(); } } while (0)

struct Params { const u16* x; const u16* gw; const u16* wq[2]; const u16* k[2]; const u16* v[2]; const u16* wo[2]; u16* out; unsigned char* ws; };

__device__ __forceinline__ float bflo(unsigned w) { return __builtin_bit_cast(float, w << 16); }
__device__ __forceinline__ float bfhi(unsigned w) { return __builtin_bit_cast(float, w & 0xffff0000u); }
typedef float f32x2 __attribute__((ext_vector_type(2)));
__device__ __forceinline__ unsigned cvtpk(float lo, float hi) { return __builtin_bit_cast(unsigned, __builtin_convertvector(f32x2{lo, hi}, bf16x2)); }
__device__ __forceinline__ float rbf(float f) { return bflo(cvtpk(f, f)); }
__device__ __forceinline__ float dot2(unsigned a, unsigned b, float acc) { return __builtin_amdgcn_fdot2_f32_bf16(__builtin_bit_cast(bf16x2, a), __builtin_bit_cast(bf16x2, b), acc, false); }
template <int CTRL> __device__ __forceinline__ float dpp(float x) { return __builtin_bit_cast(float, __builtin_amdgcn_mov_dpp(__builtin_bit_cast(int, x), CTRL, 0xf, 0xf, true)); }
__device__ __forceinline__ float u2f(unsigned u) { return __builtin_bit_cast(float, u); }
__device__ __forceinline__ unsigned f2u(float f) { return __builtin_bit_cast(unsigned, f); }
__device__ __forceinline__ float rs32(float a, float b) { auto r = __builtin_amdgcn_permlane32_swap(f2u(a), f2u(b), false, false); return u2f(r[0]) + u2f(r[1]); }
__device__ __forceinline__ float rs16(float a, float b) { auto r = __builtin_amdgcn_permlane16_swap(f2u(a), f2u(b), false, false); return u2f(r[0]) + u2f(r[1]); }
__device__ __forceinline__ float xrow_max(float x) {
  auto s = __builtin_amdgcn_permlane16_swap(f2u(x), f2u(x), false, false); x = fmaxf(u2f(s[0]), u2f(s[1]));
  auto t = __builtin_amdgcn_permlane32_swap(f2u(x), f2u(x), false, false); return fmaxf(u2f(t[0]), u2f(t[1]));
}
__device__ __forceinline__ float xrow_sum(float x) { x = rs16(x, x); return rs32(x, x); }
__device__ __forceinline__ f32x4 mfma(v4u a, v4u b, f32x4 c) { return __builtin_amdgcn_mfma_f32_16x16x32_bf16(__builtin_bit_cast(bf16x8, a), __builtin_bit_cast(bf16x8, b), c, 0, 0, 0); }
__device__ __forceinline__ v4u tr_pair(const LAS unsigned char* a0, int off2) {
  const s16x4 lo = __builtin_amdgcn_ds_read_tr16_b64_v4i16((LAS s16x4*)a0), hi = __builtin_amdgcn_ds_read_tr16_b64_v4i16((LAS s16x4*)(a0 + off2));
  return __builtin_bit_cast(v4u, __builtin_shufflevector(lo, hi, 0, 1, 2, 3, 4, 5, 6, 7));
}
template <int STRIDE> __device__ __forceinline__ void dma_run(const void* sbase, unsigned voff, unsigned ldst, unsigned n) {
  asm volatile("s_mov_b32 m0, %3\n\ts_nop 0\n"
               "1:\n\tglobal_load_lds_dwordx4 %0, %2 nt\n\tv_add_u32 %0, %4, %0\n\ts_add_u32 m0, m0, %4\n\ts_sub_u32 %1, %1, 1\n\ts_cmp_lg_u32 %1, 0\n\ts_cbranch_scc1 1b"
               : "+v"(voff), "+s"(n) : "s"(sbase), "s"(ldst), "n"(STRIDE) : "memory", "scc");
}
#define WAIT_VM(n) asm volatile("s_waitcnt vmcnt(" #n ")" ::: "memory")
#define WAIT_LGKM() asm volatile("s_waitcnt lgkmcnt(0)" ::: "memory")
#define BARRIER() asm volatile("s_waitcnt lgkmcnt(0)\n\ts_barrier" ::: "memory")
__device__ __forceinline__ unsigned lds_add(unsigned addr) {
  unsigned old, one = 1u;
  asm volatile("s_waitcnt lgkmcnt(0)\n\ts_mov_b64 exec, 1\n\tds_add_rtn_u32 %0, %1, %2\n\ts_waitcnt lgkmcnt(0)\n\ts_mov_b64 exec, -1" : "=&v"(old) : "v"(addr), "v"(one) : "memory");
  return __builtin_amdgcn_readfirstlane(old);
}
__device__ __forceinline__ void lds_wait(const LAS unsigned char* L, unsigned addr, unsigned want) {
#pragma clang loop unroll(disable)
  for (unsigned i = 0; i < SPIN_CAP; ++i) { if (__builtin_amdgcn_readfirstlane(*(volatile LAS unsigned*)(L + addr)) >= want) break; __builtin_amdgcn_s_sleep(1); }
  asm volatile("" ::: "memory");
}
#define RELOAD() asm volatile("" ::: "memory")
__device__ __forceinline__ void st_granule(unsigned char* ws, unsigned off, float v, unsigned tag) {
  __hip_atomic_store((GAS unsigned long long*)(ws + off), ((unsigned long long)tag << 32) | f2u(v), RLX_AGENT);
}

__global__ void __launch_bounds__(NTHR, 2) fused_decode(Params p) {
  __shared__ __attribute__((aligned(16))) unsigned char lds_raw[L_END];
  LAS unsigned char* L = (LAS unsigned char*)lds_raw;
  const int tid = threadIdx.x, lane = tid & 63, wave = __builtin_amdgcn_readfirstlane(tid >> 6);
  const int b = blockIdx.x, g = b & 15, s = b >> 4, br = g >> 3, kvh = g & 7;
  const int lr = lane >> 4, lc = lane & 15;
  const __amdgpu_buffer_rsrc_t wsr = __builtin_amdgcn_make_buffer_rsrc(p.ws, 0, W_END, 0x00020000);
  unsigned tag;
  float gate = 0.f;

  if (wave < 4) {
    __builtin_amdgcn_s_setprio(2);
    if (wave == 0) STAMP(0);
    const int kh = s >> 3, cb = s & 7, row0 = 256 * kh + 64 * wave + 8 * (lane >> 3);
    const GAS v4u* wp = (const GAS v4u*)(p.wq[br] + (size_t)row0 * NQ + kvh * 512 + 64 * cb + 8 * (lane & 7));
    const v4u xv = *(const GAS v4u*)(p.x + row0);
    v4u w[8];
#pragma unroll
    for (int i = 0; i < 8; ++i) w[i] = __builtin_nontemporal_load(wp + (size_t)i * (NQ / 8));
    RELOAD();
    unsigned ep = __hip_atomic_load((GAS unsigned*)(p.ws + W_E), RLX_AGENT);
    v4u gx = *(const GAS v4u*)(p.x + 8 * lane), gg = *(const GAS v4u*)(p.gw + 8 * lane);
    BARRIER();
    asm volatile("" : "+v"(w[0]), "+v"(w[1]));
    float acc[8];
#pragma unroll
    for (int k = 0; k < 8; ++k) acc[k] = 0.f;
#pragma unroll
    for (int i = 0; i < 8; i += 2)
#pragma unroll
      for (int d = 0; d < 4; ++d) {
        acc[2 * d] = dot2(__builtin_amdgcn_perm(w[i + 1][d], w[i][d], 0x05040100u), xv[i >> 1], acc[2 * d]);
        acc[2 * d + 1] = dot2(__builtin_amdgcn_perm(w[i + 1][d], w[i][d], 0x07060302u), xv[i >> 1], acc[2 * d + 1]);
      }
    if (wave == 0) STAMP(1);
    asm volatile("" : "+v"(ep));
    tag = ep + 1u;
    if (wave == 0 && lane == 0) *(LAS unsigned*)(L + C_TAG) = tag;
    {
      const float t0 = rs32(acc[0], acc[4]), t1 = rs32(acc[1], acc[5]), t2 = rs32(acc[2], acc[6]), t3 = rs32(acc[3], acc[7]);
      const float u0 = rs16(t0, t2), u1 = rs16(t1, t3);
      const float a0 = u0 + dpp<0x128>(u0), a1 = u1 + dpp<0x128>(u1);
      const int col = 8 * (lane & 7) + ((lane >> 3) & 1) + 2 * ((lane >> 4) & 1) + 4 * (lane >> 5);
      ((LAS float*)(L + L_QP))[wave * 64 + col] = (lane & 8) ? a1 : a0;
    }
    if (lds_add(C_QP) == 3) {
      const LAS float* qp = (const LAS float*)(L + L_QP);
      st_granule(p.ws, W_Q + (g * 2 + kh) * 4096 + (64 * cb + lane) * 8, (qp[lane] + qp[64 + lane]) + (qp[128 + lane] + qp[192 + lane]), tag);
      STAMP(2);
    }
    if (wave == 0) {
      float z = 0.f;
#pragma unroll
      for (int d = 0; d < 4; ++d) z += rbf(bflo(gx[d]) * bflo(gg[d])) + rbf(bfhi(gx[d]) * bfhi(gg[d]));
      z += dpp<0xB1>(z); z += dpp<0x4E>(z); z += dpp<0x124>(z); z += dpp<0x128>(z); z = xrow_sum(z);
      gate = rbf(__builtin_amdgcn_rcpf(1.f + __expf(-rbf(z))));
    }
    {
      const int off = W_Q + g * 8192 + (128 * wave + 2 * lane) * 8;
      v4u a0, a1;
#pragma clang loop unroll(disable)
      for (unsigned i = 0; i < SPIN_CAP; ++i) {
        RELOAD();
        a0 = __builtin_amdgcn_raw_buffer_load_b128(wsr, off, 0, 16); a1 = __builtin_amdgcn_raw_buffer_load_b128(wsr, off + 4096, 0, 16);
        if (__all(a0[1] == tag && a0[3] == tag && a1[1] == tag && a1[3] == tag)) break;
      }
      *(LAS unsigned*)(L + L_Q + wave * Q_STR + 4 * lane) = cvtpk(u2f(a0[0]) + u2f(a1[0]), u2f(a0[2]) + u2f(a1[2]));
      lds_add(C_QRDY);
      if (wave == 0) STAMP(3);
    }
    lds_wait(L, C_QRDY, 4); lds_wait(L, C_KL, 4);
    if (wave == 0) STAMP(5);
    f32x4 sc[4];
    {
      v4u qf[4];
#pragma unroll
      for (int ks = 0; ks < 4; ++ks) qf[ks] = *(const LAS v4u*)(L + L_Q + (lane & 3) * Q_STR + 64 * ks + 16 * lr);
      const unsigned ka = L_K + (64 * wave + lc) * 256 + ((lr ^ lc) << 4);
#pragma unroll
      for (int t = 0; t < 4; ++t) {
        sc[t] = f32x4{0.f, 0.f, 0.f, 0.f};
#pragma unroll
        for (int ks = 0; ks < 4; ++ks) sc[t] = mfma(*(const LAS v4u*)(L + (ka ^ (64 * ks)) + 4096 * t), qf[ks], sc[t]);
      }
    }
    float m = fmaxf(fmaxf(sc[0][0], sc[0][1]), fmaxf(sc[0][2], sc[0][3]));
#pragma unroll
    for (int t = 1; t < 4; ++t) m = fmaxf(m, fmaxf(fmaxf(sc[t][0], sc[t][1]), fmaxf(sc[t][2], sc[t][3])));
    m = xrow_max(m);
    if (lane < 4) ((LAS float*)(L + L_MX))[lane * 4 + wave] = m;
    lds_add(C_MX); lds_wait(L, C_MX, 4);
    constexpr float C2 = 0.08838834764831845f * 1.4426950408889634f;
    const f32x4 mx = *(const LAS f32x4*)(L + L_MX + 16 * (lane & 3));
    const float mc = fmaxf(fmaxf(mx[0], mx[1]), fmaxf(mx[2], mx[3])) * C2;
    float ls = 0.f;
#pragma unroll
    for (int t = 0; t < 4; ++t) {
      float e[4];
#pragma unroll
      for (int r = 0; r < 4; ++r) { e[r] = __builtin_amdgcn_exp2f(fmaf(sc[t][r], C2, -mc)); ls += e[r]; }
      if ((lane & 12) == 0) *(LAS v2u*)(L + L_P + (lane & 3) * P_STR + (64 * wave + 16 * t + 4 * lr) * 2) = v2u{cvtpk(e[0], e[1]), cvtpk(e[2], e[3])};
    }
    ls = xrow_sum(ls);
    if (lane < 4) ((LAS float*)(L + L_LS))[lane * 4 + wave] = ls;
    if (lds_add(C_QK) == 3) {
      if (lane < 4) {
        const f32x4 l4 = *(const LAS f32x4*)(L + L_LS + 16 * lane);
        __builtin_amdgcn_raw_buffer_store_b128(v4u{f2u(mc), tag, f2u((l4[0] + l4[1]) + (l4[2] + l4[3])), tag}, wsr, W_ML + ((g * 16 + s) * 4 + lane) * 16, 0, 16);
      }
      STAMP(6);
    }
  } else {
    const int j = wave - 4;
    if (j == 0 && lane < 5) *(LAS unsigned*)(L + L_CNT + 4 * lane) = 0u;
    const size_t kvoff = ((size_t)kvh * SK + (SK - WIN) + s * 256) * DH;
    const unsigned char* kb = (const unsigned char*)(p.k[br] + kvoff) + j * 1024;
    const unsigned char* vb = (const unsigned char*)(p.v[br] + kvoff) + j * 1024;
    const unsigned char* wb = (const unsigned char*)(p.wo[br] + ((size_t)kvh * 512 + s * 32) * DM) + j * 2048;
    const unsigned ko = lr * 256 + ((lc ^ (4 * j + lr)) << 4);
    const unsigned vo = lr * 256 + ((lc ^ ((lr | ((j & 2) << 1)) << 1)) << 4);
    BARRIER();
    dma_run<4096>(kb, ko, L_K + j * 1024, 16);
    dma_run<4096>(vb, vo, L_V + j * 1024, 8);
    WAIT_VM(8);
    lds_add(C_KL);
    if (j == 0) STAMP(4);
    dma_run<4096>(vb, vo + 32768, L_V + 32768 + j * 1024, 8);
    if (j == 0) STAMP(7);
    lds_wait(L, C_QK, 4);
    dma_run<8192>(wb, (unsigned)((lane ^ (4 * j)) << 4), L_WO + j * 2048, 4);
    dma_run<8192>(wb + 1024, (unsigned)((lane ^ (4 * j + 2)) << 4), L_WO + j * 2048 + 1024, 4);
    WAIT_VM(8);
    if (j == 0) STAMP(8);
  }
  BARRIER();
  tag = *(const LAS unsigned*)(L + C_TAG);
  {
    const int q4 = lc >> 2, p4 = lane & 3;
    const LAS unsigned char* va = L + L_V + (8 * lr + q4) * 256 + (((2 * wave + (p4 >> 1)) ^ ((q4 | ((lr & 1) << 2)) << 1)) << 4) + 8 * (p4 & 1);
    const LAS unsigned char* pa = L + L_P + p4 * P_STR + 16 * lr;
    f32x4 o = {0.f, 0.f, 0.f, 0.f};
#pragma unroll
    for (int ks = 0; ks < 8; ++ks) o = mfma(tr_pair(va + 8192 * ks, 1024), *(const LAS v4u*)(pa + 64 * ks), o);
    const float ov = q4 == 0 ? o[0] : q4 == 1 ? o[1] : q4 == 2 ? o[2] : o[3];
    st_granule(p.ws, W_O + ((g * 16 + 4 * p4 + (wave >> 1)) * 16 + s) * 256 + (16 * (wave & 1) + 4 * lr + q4) * 8, ov, tag);
    if (wave == 0) STAMP(9);
  }
  if (wave == 0) {
    const int off = W_O + (g * 16 + s) * 4096 + lane * 16, moff = W_ML + ((g * 16 + lr) * 4 + (s >> 2)) * 16;
    v4u d[4], ml[4];
#pragma clang loop unroll(disable)
    for (unsigned it = 0; it < SPIN_CAP; ++it) {
      RELOAD();
      bool ok = true;
#pragma unroll
      for (int i = 0; i < 4; ++i) { d[i] = __builtin_amdgcn_raw_buffer_load_b128(wsr, off + i * 1024, 0, 16); ml[i] = __builtin_amdgcn_raw_buffer_load_b128(wsr, moff + i * 256, 0, 16); }
#pragma unroll
      for (int i = 0; i < 4; ++i) ok &= d[i][1] == tag && d[i][3] == tag && ml[i][1] == tag && ml[i][3] == tag;
      if (__all(ok)) break;
    }
    const float M = xrow_max(fmaxf(fmaxf(u2f(ml[0][0]), u2f(ml[1][0])), fmaxf(u2f(ml[2][0]), u2f(ml[3][0]))));
    float Ls = 0.f, o0 = 0.f, o1 = 0.f;
#pragma unroll
    for (int i = 0; i < 4; ++i) { const float wgt = __builtin_amdgcn_exp2f(u2f(ml[i][0]) - M); Ls = fmaf(wgt, u2f(ml[i][2]), Ls); o0 = fmaf(wgt, u2f(d[i][0]), o0); o1 = fmaf(wgt, u2f(d[i][2]), o1); }
    Ls = xrow_sum(Ls); o0 = xrow_sum(o0); o1 = xrow_sum(o1);
    const float inv = __builtin_amdgcn_rcpf(Ls);
    if (lane < 16) *(LAS unsigned*)(L + L_O + 16 * ((lane >> 1) & 3) + 8 * (lane >> 3) + 4 * (lane & 1)) = cvtpk(o0 * inv, o1 * inv);
    STAMP(10);
  }
  if (wave >= 4) WAIT_VM(0);
  BARRIER();
  if (wave == 0) STAMP(11);
  {
    const int q4 = lc >> 2, p4 = lane & 3;
    const v4u of = *(const LAS v4u*)(L + L_O + 16 * lr);
    const unsigned wa = L_WO + (4 * lr + q4) * 1024 + (((8 * wave + (p4 >> 1)) ^ ((q4 | ((lr & 1) << 2)) << 1)) << 4) + 8 * (p4 & 1);
    f32x4 o = {0.f, 0.f, 0.f, 0.f};
#pragma unroll
    for (int t = 0; t < 4; ++t) { const v4u z = {0u, 0u, 0u, 0u}; o = mfma(tr_pair(L + (wa ^ (32 * t)), 16384), p4 == t ? of : z, o); }
    const float ov = q4 == 0 ? o[0] : q4 == 1 ? o[1] : q4 == 2 ? o[2] : o[3];
    st_granule(p.ws, W_F + ((4 * wave + p4) * 256 + br * 128 + kvh * 16 + s) * 128 + (4 * lr + q4) * 8, ov, tag);
    if (wave == 0) STAMP(12);
  }
  if (b < 32) {
    const int off = W_F + b * 32768 + wave * 4096 + lane * 16;
    v4u d[4];
#pragma clang loop unroll(disable)
    for (unsigned it = 0; it < SPIN_CAP; ++it) {
      RELOAD();
      bool ok = true;
#pragma unroll
      for (int i = 0; i < 4; ++i) d[i] = __builtin_amdgcn_raw_buffer_load_b128(wsr, off + i * 1024, 0, 16);
#pragma unroll
      for (int i = 0; i < 4; ++i) ok &= d[i][1] == tag && d[i][3] == tag;
      if (__all(ok)) break;
    }
    float t = rs32((u2f(d[0][0]) + u2f(d[1][0])) + (u2f(d[2][0]) + u2f(d[3][0])), (u2f(d[0][2]) + u2f(d[1][2])) + (u2f(d[2][2]) + u2f(d[3][2])));
    t = rs16(t, t); t += dpp<0x128>(t);
    if ((lane & 24) == 0) ((LAS float*)(L + L_RED))[wave * 16 + 2 * (lane & 7) + (lane >> 5)] = t;
    if (wave == 0) STAMP(13);
    BARRIER();
    if (wave == 0) {
      if (lane < 16) {
        const LAS float* r = (const LAS float*)(L + L_RED) + lane;
        const float A = rbf((r[0] + r[16]) + (r[32] + r[48])), B = rbf((r[64] + r[80]) + (r[96] + r[112]));
        p.out[16 * b + lane] = (u16)(cvtpk(rbf(gate * A) + rbf(rbf(1.f - gate) * B), 0.f) & 0xffffu);
      }
      if (b == 0 && lane == 0) __hip_atomic_store((GAS unsigned*)(p.ws + W_E), tag, RLX_AGENT);
      STAMP(14);
      if (b == 0) { WAIT_LGKM(); const unsigned long long t0 = *(volatile LAS unsigned long long*)(L + L_CNT + 32), t1 = *(volatile LAS unsigned long long*)(L + L_CNT + 40);
        const unsigned long long until = __builtin_amdgcn_s_memrealtime() + 10ull * (t1 - t0);
        while (__builtin_amdgcn_s_memrealtime() < until) __builtin_amdgcn_s_sleep(1); }
    }
  }
}

extern "C" void kernel_launch(void* const* d_in, const int* in_sizes, int n_in, void* d_out, int out_size, void* d_ws, size_t ws_size, hipStream_t stream) {
  Params p;
  p.x = (const u16*)d_in[0]; p.gw = (const u16*)d_in[10];
  p.wq[0] = (const u16*)d_in[3]; p.wq[1] = (const u16*)d_in[8];
  p.k[0] = (const u16*)d_in[1]; p.k[1] = (const u16*)d_in[6];
  p.v[0] = (const u16*)d_in[2]; p.v[1] = (const u16*)d_in[7];
  p.wo[0] = (const u16*)d_in[4]; p.wo[1] = (const u16*)d_in[9];
  p.out = (u16*)d_out; p.ws = (unsigned char*)d_ws;
  fused_decode<<<NWG, NTHR, 0, stream>>>(p);
}
```

```cpp
#include <hip/hip_runtime.h>
#include <hip/hip_bf16.h>

typedef unsigned short u16;
typedef unsigned v4u __attribute__((ext_vector_type(4)));
typedef unsigned v2u __attribute__((ext_vector_type(2)));
typedef float f32x4 __attribute__((ext_vector_type(4)));
typedef short s16x4 __attribute__((ext_vector_type(4)));
typedef short s16x8 __attribute__((ext_vector_type(8)));
typedef __bf16 bf16x2 __attribute__((ext_vector_type(2)));
typedef __bf16 bf16x8 __attribute__((ext_vector_type(8)));
#define GAS __attribute__((address_space(1)))
#define LAS __attribute__((address_space(3)))
#define RLX_AGENT __ATOMIC_RELAXED, __HIP_MEMORY_SCOPE_AGENT

constexpr int DM = 512, SK = 32768, DH = 128, WIN = 4096, NQ = 4096;
constexpr int NWG = 256, NTHR = 512;
constexpr int L_K = 0, L_WO = 0, L_V = 65536, L_P = 131072, P_STR = 576, L_Q = L_P + 4 * P_STR, Q_STR = 320, L_QP = L_Q + 4 * Q_STR,
              L_MX = L_QP + 1024, L_LS = L_MX + 64, L_O = L_LS + 64, L_RED = L_O + 64, L_CNT = L_RED + 512, L_END = L_CNT + 64;
enum { C_QP = L_CNT, C_QRDY = L_CNT + 4, C_KL = L_CNT + 8, C_MX = L_CNT + 12, C_QK = L_CNT + 16, C_TAG = L_CNT + 20 };
constexpr unsigned W_E = 0, W_Q = 4096, W_ML = W_Q + 16 * 8192, W_O = W_ML + 16384, W_F = W_O + (1u << 20), W_END = W_F + (1u << 20);
constexpr unsigned SPIN_CAP = 1u << 20;

#define STAMP(i) do { } while (0)

struct Params { const u16* x; const u16* gw; const u16* wq[2]; const u16* k[2]; const u16* v[2]; const u16* wo[2]; u16* out; unsigned char* ws; };

__device__ __forceinline__ float bflo(unsigned w) { return __builtin_bit_cast(float, w << 16); }
__device__ __forceinline__ float bfhi(unsigned w) { return __builtin_bit_cast(float, w & 0xffff0000u); }
typedef float f32x2 __attribute__((ext_vector_type(2)));
__device__ __forceinline__ unsigned cvtpk(float lo, float hi) { return __builtin_bit_cast(unsigned, __builtin_convertvector(f32x2{lo, hi}, bf16x2)); }
__device__ __forceinline__ float rbf(float f) { return bflo(cvtpk(f, f)); }
__device__ __forceinline__ float dot2(unsigned a, unsigned b, float acc) { return __builtin_amdgcn_fdot2_f32_bf16(__builtin_bit_cast(bf16x2, a), __builtin_bit_cast(bf16x2, b), acc, false); }
template <int CTRL> __device__ __forceinline__ float dpp(float x) { return __builtin_bit_cast(float, __builtin_amdgcn_mov_dpp(__builtin_bit_cast(int, x), CTRL, 0xf, 0xf, true)); }
__device__ __forceinline__ float u2f(unsigned u) { return __builtin_bit_cast(float, u); }
__device__ __forceinline__ unsigned f2u(float f) { return __builtin_bit_cast(unsigned, f); }
__device__ __forceinline__ float rs32(float a, float b) { auto r = __builtin_amdgcn_permlane32_swap(f2u(a), f2u(b), false, false); return u2f(r[0]) + u2f(r[1]); }
__device__ __forceinline__ float rs16(float a, float b) { auto r = __builtin_amdgcn_permlane16_swap(f2u(a), f2u(b), false, false); return u2f(r[0]) + u2f(r[1]); }
__device__ __forceinline__ float xrow_max(float x) {
  auto s = __builtin_amdgcn_permlane16_swap(f2u(x), f2u(x), false, false); x = fmaxf(u2f(s[0]), u2f(s[1]));
  auto t = __builtin_amdgcn_permlane32_swap(f2u(x), f2u(x), false, false); return fmaxf(u2f(t[0]), u2f(t[1]));
}
__device__ __forceinline__ float xrow_sum(float x) { x = rs16(x, x); return rs32(x, x); }
__device__ __forceinline__ f32x4 mfma(v4u a, v4u b, f32x4 c) { return __builtin_amdgcn_mfma_f32_16x16x32_bf16(__builtin_bit_cast(bf16x8, a), __builtin_bit_cast(bf16x8, b), c, 0, 0, 0); }
__device__ __forceinline__ v4u tr_pair(const LAS unsigned char* a0, int off2) {
  const s16x4 lo = __builtin_amdgcn_ds_read_tr16_b64_v4i16((LAS s16x4*)a0), hi = __builtin_amdgcn_ds_read_tr16_b64_v4i16((LAS s16x4*)(a0 + off2));
  return __builtin_bit_cast(v4u, __builtin_shufflevector(lo, hi, 0, 1, 2, 3, 4, 5, 6, 7));
}
template <int STRIDE> __device__ __forceinline__ void dma_run(const void* sbase, unsigned voff, unsigned ldst, unsigned n) {
  asm volatile("s_mov_b32 m0, %3\n\ts_nop 0\n"
               "1:\n\tglobal_load_lds_dwordx4 %0, %2 nt\n\tv_add_u32 %0, %4, %0\n\ts_add_u32 m0, m0, %4\n\ts_sub_u32 %1, %1, 1\n\ts_cmp_lg_u32 %1, 0\n\ts_cbranch_scc1 1b"
               : "+v"(voff), "+s"(n) : "s"(sbase), "s"(ldst), "n"(STRIDE) : "memory", "scc");
}
#define WAIT_VM(n) asm volatile("s_waitcnt vmcnt(" #n ")" ::: "memory")
#define WAIT_LGKM() asm volatile("s_waitcnt lgkmcnt(0)" ::: "memory")
#define BARRIER() asm volatile("s_waitcnt lgkmcnt(0)\n\ts_barrier" ::: "memory")
__device__ __forceinline__ unsigned lds_add(unsigned addr) {
  unsigned old, one = 1u;
  asm volatile("s_waitcnt lgkmcnt(0)\n\ts_mov_b64 exec, 1\n\tds_add_rtn_u32 %0, %1, %2\n\ts_waitcnt lgkmcnt(0)\n\ts_mov_b64 exec, -1" : "=&v"(old) : "v"(addr), "v"(one) : "memory");
  return __builtin_amdgcn_readfirstlane(old);
}
__device__ __forceinline__ void lds_wait(const LAS unsigned char* L, unsigned addr, unsigned want) {
#pragma clang loop unroll(disable)
  for (unsigned i = 0; i < SPIN_CAP; ++i) { if (__builtin_amdgcn_readfirstlane(*(volatile LAS unsigned*)(L + addr)) >= want) break; __builtin_amdgcn_s_sleep(1); }
  asm volatile("" ::: "memory");
}
#define RELOAD() asm volatile("" ::: "memory")
__device__ __forceinline__ void st_granule(unsigned char* ws, unsigned off, float v, unsigned tag) {
  __hip_atomic_store((GAS unsigned long long*)(ws + off), ((unsigned long long)tag << 32) | f2u(v), RLX_AGENT);
}

__global__ void __launch_bounds__(NTHR, 2) fused_decode(Params p) {
  __shared__ __attribute__((aligned(16))) unsigned char lds_raw[L_END];
  unsigned long long t_entry;
  asm volatile("s_memrealtime %0\n\ts_waitcnt lgkmcnt(0)" : "=s"(t_entry) :: "memory");
  LAS unsigned char* L = (LAS unsigned char*)lds_raw;
  const int tid = threadIdx.x, lane = tid & 63, wave = __builtin_amdgcn_readfirstlane(tid >> 6);
  const int b = blockIdx.x, g = b & 15, s = b >> 4, br = g >> 3, kvh = g & 7;
  const int lr = lane >> 4, lc = lane & 15;
  const __amdgpu_buffer_rsrc_t wsr = __builtin_amdgcn_make_buffer_rsrc(p.ws, 0, W_END, 0x00020000);
  unsigned tag;
  float gate = 0.f;

  if (wave < 4) {
    __builtin_amdgcn_s_setprio(2);
    if (wave == 0) STAMP(0);
    const int kh = s >> 3, cb = s & 7, row0 = 256 * kh + 64 * wave + 8 * (lane >> 3);
    const GAS v4u* wp = (const GAS v4u*)(p.wq[br] + (size_t)row0 * NQ + kvh * 512 + 64 * cb + 8 * (lane & 7));
    const v4u xv = *(const GAS v4u*)(p.x + row0);
    v4u w[8];
#pragma unroll
    for (int i = 0; i < 8; ++i) w[i] = __builtin_nontemporal_load(wp + (size_t)i * (NQ / 8));
    RELOAD();
    unsigned ep = __hip_atomic_load((GAS unsigned*)(p.ws + W_E), RLX_AGENT);
    v4u gx = *(const GAS v4u*)(p.x + 8 * lane), gg = *(const GAS v4u*)(p.gw + 8 * lane);
    BARRIER();
    asm volatile("" : "+v"(w[0]), "+v"(w[1]));
    float acc[8];
#pragma unroll
    for (int k = 0; k < 8; ++k) acc[k] = 0.f;
#pragma unroll
    for (int i = 0; i < 8; i += 2)
#pragma unroll
      for (int d = 0; d < 4; ++d) {
        acc[2 * d] = dot2(__builtin_amdgcn_perm(w[i + 1][d], w[i][d], 0x05040100u), xv[i >> 1], acc[2 * d]);
        acc[2 * d + 1] = dot2(__builtin_amdgcn_perm(w[i + 1][d], w[i][d], 0x07060302u), xv[i >> 1], acc[2 * d + 1]);
      }
    if (wave == 0) STAMP(1);
    asm volatile("" : "+v"(ep));
    tag = ep + 1u;
    if (wave == 0 && lane == 0) *(LAS unsigned*)(L + C_TAG) = tag;
    {
      const float t0 = rs32(acc[0], acc[4]), t1 = rs32(acc[1], acc[5]), t2 = rs32(acc[2], acc[6]), t3 = rs32(acc[3], acc[7]);
      const float u0 = rs16(t0, t2), u1 = rs16(t1, t3);
      const float a0 = u0 + dpp<0x128>(u0), a1 = u1 + dpp<0x128>(u1);
      const int col = 8 * (lane & 7) + ((lane >> 3) & 1) + 2 * ((lane >> 4) & 1) + 4 * (lane >> 5);
      ((LAS float*)(L + L_QP))[wave * 64 + col] = (lane & 8) ? a1 : a0;
    }
    if (lds_add(C_QP) == 3) {
      const LAS float* qp = (const LAS float*)(L + L_QP);
      st_granule(p.ws, W_Q + (g * 2 + kh) * 4096 + (64 * cb + lane) * 8, (qp[lane] + qp[64 + lane]) + (qp[128 + lane] + qp[192 + lane]), tag);
      STAMP(2);
    }
    if (wave == 0) {
      float z = 0.f;
#pragma unroll
      for (int d = 0; d < 4; ++d) z += rbf(bflo(gx[d]) * bflo(gg[d])) + rbf(bfhi(gx[d]) * bfhi(gg[d]));
      z += dpp<0xB1>(z); z += dpp<0x4E>(z); z += dpp<0x124>(z); z += dpp<0x128>(z); z = xrow_sum(z);
      gate = rbf(__builtin_amdgcn_rcpf(1.f + __expf(-rbf(z))));
    }
    {
      const int off = W_Q + g * 8192 + (128 * wave + 2 * lane) * 8;
      v4u a0, a1;
#pragma clang loop unroll(disable)
      for (unsigned i = 0; i < SPIN_CAP; ++i) {
        RELOAD();
        a0 = __builtin_amdgcn_raw_buffer_load_b128(wsr, off, 0, 16); a1 = __builtin_amdgcn_raw_buffer_load_b128(wsr, off + 4096, 0, 16);
        if (__all(a0[1] == tag && a0[3] == tag && a1[1] == tag && a1[3] == tag)) break;
      }
      *(LAS unsigned*)(L + L_Q + wave * Q_STR + 4 * lane) = cvtpk(u2f(a0[0]) + u2f(a1[0]), u2f(a0[2]) + u2f(a1[2]));
      lds_add(C_QRDY);
      if (wave == 0) STAMP(3);
    }
    lds_wait(L, C_QRDY, 4); lds_wait(L, C_KL, 4);
    if (wave == 0) STAMP(5);
    f32x4 sc[4];
    {
      v4u qf[4];
#pragma unroll
      for (int ks = 0; ks < 4; ++ks) qf[ks] = *(const LAS v4u*)(L + L_Q + (lane & 3) * Q_STR + 64 * ks + 16 * lr);
      const unsigned ka = L_K + (64 * wave + lc) * 256 + ((lr ^ lc) << 4);
#pragma unroll
      for (int t = 0; t < 4; ++t) {
        sc[t] = f32x4{0.f, 0.f, 0.f, 0.f};
#pragma unroll
        for (int ks = 0; ks < 4; ++ks) sc[t] = mfma(*(const LAS v4u*)(L + (ka ^ (64 * ks)) + 4096 * t), qf[ks], sc[t]);
      }
    }
    float m = fmaxf(fmaxf(sc[0][0], sc[0][1]), fmaxf(sc[0][2], sc[0][3]));
#pragma unroll
    for (int t = 1; t < 4; ++t) m = fmaxf(m, fmaxf(fmaxf(sc[t][0], sc[t][1]), fmaxf(sc[t][2], sc[t][3])));
    m = xrow_max(m);
    if (lane < 4) ((LAS float*)(L + L_MX))[lane * 4 + wave] = m;
    lds_add(C_MX); lds_wait(L, C_MX, 4);
    constexpr float C2 = 0.08838834764831845f * 1.4426950408889634f;
    const f32x4 mx = *(const LAS f32x4*)(L + L_MX + 16 * (lane & 3));
    const float mc = fmaxf(fmaxf(mx[0], mx[1]), fmaxf(mx[2], mx[3])) * C2;
    float ls = 0.f;
#pragma unroll
    for (int t = 0; t < 4; ++t) {
      float e[4];
#pragma unroll
      for (int r = 0; r < 4; ++r) { e[r] = __builtin_amdgcn_exp2f(fmaf(sc[t][r], C2, -mc)); ls += e[r]; }
      if ((lane & 12) == 0) *(LAS v2u*)(L + L_P + (lane & 3) * P_STR + (64 * wave + 16 * t + 4 * lr) * 2) = v2u{cvtpk(e[0], e[1]), cvtpk(e[2], e[3])};
    }
    ls = xrow_sum(ls);
    if (lane < 4) ((LAS float*)(L + L_LS))[lane * 4 + wave] = ls;
    if (lds_add(C_QK) == 3) {
      if (lane < 4) {
        const f32x4 l4 = *(const LAS f32x4*)(L + L_LS + 16 * lane);
        __builtin_amdgcn_raw_buffer_store_b128(v4u{f2u(mc), tag, f2u((l4[0] + l4[1]) + (l4[2] + l4[3])), tag}, wsr, W_ML + ((g * 16 + s) * 4 + lane) * 16, 0, 16);
      }
      STAMP(6);
    }
  } else {
    const int j = wave - 4;
    if (j == 0 && lane < 5) *(LAS unsigned*)(L + L_CNT + 4 * lane) = 0u;
    const size_t kvoff = ((size_t)kvh * SK + (SK - WIN) + s * 256) * DH;
    const unsigned char* kb = (const unsigned char*)(p.k[br] + kvoff) + j * 1024;
    const unsigned char* vb = (const unsigned char*)(p.v[br] + kvoff) + j * 1024;
    const unsigned char* wb = (const unsigned char*)(p.wo[br] + ((size_t)kvh * 512 + s * 32) * DM) + j * 2048;
    const unsigned ko = lr * 256 + ((lc ^ (4 * j + lr)) << 4);
    const unsigned vo = lr * 256 + ((lc ^ ((lr | ((j & 2) << 1)) << 1)) << 4);
    BARRIER();
    dma_run<4096>(kb, ko, L_K + j * 1024, 16);
    dma_run<4096>(vb, vo, L_V + j * 1024, 8);
    WAIT_VM(8);
    lds_add(C_KL);
    if (j == 0) STAMP(4);
    dma_run<4096>(vb, vo + 32768, L_V + 32768 + j * 1024, 8);
    if (j == 0) STAMP(7);
    lds_wait(L, C_QK, 4);
    dma_run<8192>(wb, (unsigned)((lane ^ (4 * j)) << 4), L_WO + j * 2048, 4);
    dma_run<8192>(wb + 1024, (unsigned)((lane ^ (4 * j + 2)) << 4), L_WO + j * 2048 + 1024, 4);
    WAIT_VM(8);
    if (j == 0) STAMP(8);
  }
  BARRIER();
  tag = *(const LAS unsigned*)(L + C_TAG);
  {
    const int q4 = lc >> 2, p4 = lane & 3;
    const LAS unsigned char* va = L + L_V + (8 * lr + q4) * 256 + (((2 * wave + (p4 >> 1)) ^ ((q4 | ((lr & 1) << 2)) << 1)) << 4) + 8 * (p4 & 1);
    const LAS unsigned char* pa = L + L_P + p4 * P_STR + 16 * lr;
    f32x4 o = {0.f, 0.f, 0.f, 0.f};
#pragma unroll
    for (int ks = 0; ks < 8; ++ks) o = mfma(tr_pair(va + 8192 * ks, 1024), *(const LAS v4u*)(pa + 64 * ks), o);
    const float ov = q4 == 0 ? o[0] : q4 == 1 ? o[1] : q4 == 2 ? o[2] : o[3];
    st_granule(p.ws, W_O + ((g * 16 + 4 * p4 + (wave >> 1)) * 16 + s) * 256 + (16 * (wave & 1) + 4 * lr + q4) * 8, ov, tag);
    if (wave == 0) STAMP(9);
  }
  if (wave == 0) {
    const int off = W_O + (g * 16 + s) * 4096 + lane * 16, moff = W_ML + ((g * 16 + lr) * 4 + (s >> 2)) * 16;
    v4u d[4], ml[4];
#pragma clang loop unroll(disable)
    for (unsigned it = 0; it < SPIN_CAP; ++it) {
      RELOAD();
      bool ok = true;
#pragma unroll
      for (int i = 0; i < 4; ++i) { d[i] = __builtin_amdgcn_raw_buffer_load_b128(wsr, off + i * 1024, 0, 16); ml[i] = __builtin_amdgcn_raw_buffer_load_b128(wsr, moff + i * 256, 0, 16); }
#pragma unroll
      for (int i = 0; i < 4; ++i) ok &= d[i][1] == tag && d[i][3] == tag && ml[i][1] == tag && ml[i][3] == tag;
      if (__all(ok)) break;
    }
    const float M = xrow_max(fmaxf(fmaxf(u2f(ml[0][0]), u2f(ml[1][0])), fmaxf(u2f(ml[2][0]), u2f(ml[3][0]))));
    float Ls = 0.f, o0 = 0.f, o1 = 0.f;
#pragma unroll
    for (int i = 0; i < 4; ++i) { const float wgt = __builtin_amdgcn_exp2f(u2f(ml[i][0]) - M); Ls = fmaf(wgt, u2f(ml[i][2]), Ls); o0 = fmaf(wgt, u2f(d[i][0]), o0); o1 = fmaf(wgt, u2f(d[i][2]), o1); }
    Ls = xrow_sum(Ls); o0 = xrow_sum(o0); o1 = xrow_sum(o1);
    const float inv = __builtin_amdgcn_rcpf(Ls);
    if (lane < 16) *(LAS unsigned*)(L + L_O + 16 * ((lane >> 1) & 3) + 8 * (lane >> 3) + 4 * (lane & 1)) = cvtpk(o0 * inv, o1 * inv);
    STAMP(10);
  }
  if (wave >= 4) WAIT_VM(0);
  BARRIER();
  if (wave == 0) STAMP(11);
  {
    const int q4 = lc >> 2, p4 = lane & 3;
    const v4u of = *(const LAS v4u*)(L + L_O + 16 * lr);
    const unsigned wa = L_WO + (4 * lr + q4) * 1024 + (((8 * wave + (p4 >> 1)) ^ ((q4 | ((lr & 1) << 2)) << 1)) << 4) + 8 * (p4 & 1);
    f32x4 o = {0.f, 0.f, 0.f, 0.f};
#pragma unroll
    for (int t = 0; t < 4; ++t) { const v4u z = {0u, 0u, 0u, 0u}; o = mfma(tr_pair(L + (wa ^ (32 * t)), 16384), p4 == t ? of : z, o); }
    const float ov = q4 == 0 ? o[0] : q4 == 1 ? o[1] : q4 == 2 ? o[2] : o[3];
    st_granule(p.ws, W_F + ((4 * wave + p4) * 256 + br * 128 + kvh * 16 + s) * 128 + (4 * lr + q4) * 8, ov, tag);
    if (wave == 0) STAMP(12);
  }
  if (b < 32) {
    const int off = W_F + b * 32768 + wave * 4096 + lane * 16;
    v4u d[4];
#pragma clang loop unroll(disable)
    for (unsigned it = 0; it < SPIN_CAP; ++it) {
      RELOAD();
      bool ok = true;
#pragma unroll
      for (int i = 0; i < 4; ++i) d[i] = __builtin_amdgcn_raw_buffer_load_b128(wsr, off + i * 1024, 0, 16);
#pragma unroll
      for (int i = 0; i < 4; ++i) ok &= d[i][1] == tag && d[i][3] == tag;
      if (__all(ok)) break;
    }
    float t = rs32((u2f(d[0][0]) + u2f(d[1][0])) + (u2f(d[2][0]) + u2f(d[3][0])), (u2f(d[0][2]) + u2f(d[1][2])) + (u2f(d[2][2]) + u2f(d[3][2])));
    t = rs16(t, t); t += dpp<0x128>(t);
    if ((lane & 24) == 0) ((LAS float*)(L + L_RED))[wave * 16 + 2 * (lane & 7) + (lane >> 5)] = t;
    if (wave == 0) STAMP(13);
    BARRIER();
    if (wave == 0) {
      if (lane < 16) {
        const LAS float* r = (const LAS float*)(L + L_RED) + lane;
        const float A = rbf((r[0] + r[16]) + (r[32] + r[48])), B = rbf((r[64] + r[80]) + (r[96] + r[112]));
        p.out[16 * b + lane] = (u16)(cvtpk(rbf(gate * A) + rbf(rbf(1.f - gate) * B), 0.f) & 0xffffu);
      }
      if (b == 0 && lane == 0) __hip_atomic_store((GAS unsigned*)(p.ws + W_E), tag, RLX_AGENT);
      STAMP(14);
      if (b == 0) { while (__builtin_amdgcn_s_memrealtime() < t_entry + 3000ull) __builtin_amdgcn_s_sleep(1); }
    }
  }
}

#define R4(x) x x x x
#define R16(x) R4(R4(x))
#define R256(x) R16(R16(x))
#define R4096(x) R16(R256(x))
__global__ void dead_k(float* o) { float a = o[threadIdx.x]; asm volatile(R4096("v_fma_f32 %0, %0, %0, %0\n\tv_fma_f32 %0, %0, %0, %0\n\t") : "+v"(a)); o[threadIdx.x] = a; }
void* keep_dead() { return (void*)dead_k; }
extern "C" void kernel_launch(void* const* d_in, const int* in_sizes, int n_in, void* d_out, int out_size, void* d_ws, size_t ws_size, hipStream_t stream) {
  Params p;
  p.x = (const u16*)d_in[0]; p.gw = (const u16*)d_in[10];
  p.wq[0] = (const u16*)d_in[3]; p.wq[1] = (const u16*)d_in[8];
  p.k[0] = (const u16*)d_in[1]; p.k[1] = (const u16*)d_in[6];
  p.v[0] = (const u16*)d_in[2]; p.v[1] = (const u16*)d_in[7];
  p.wo[0] = (const u16*)d_in[4]; p.wo[1] = (const u16*)d_in[9];
  p.out = (u16*)d_out; p.ws = (unsigned char*)d_ws;
  fused_decode<<<NWG, NTHR, 0, stream>>>(p);
}
```

```cpp
#include <hip/hip_runtime.h>
#include <hip/hip_bf16.h>

typedef unsigned short u16;
typedef unsigned v4u __attribute__((ext_vector_type(4)));
typedef unsigned v2u __attribute__((ext_vector_type(2)));
typedef float f32x2 __attribute__((ext_vector_type(2)));
typedef float f32x4 __attribute__((ext_vector_type(4)));
typedef short s16x4 __attribute__((ext_vector_type(4)));
typedef __bf16 bf16x2 __attribute__((ext_vector_type(2)));
typedef __bf16 bf16x8 __attribute__((ext_vector_type(8)));
#define GAS __attribute__((address_space(1)))
#define LAS __attribute__((address_space(3)))
#define RLX_AGENT __ATOMIC_RELAXED, __HIP_MEMORY_SCOPE_AGENT

constexpr int DM = 512, SK = 32768, DH = 128, WIN = 4096, NQ = 4096;
constexpr int NWG = 256, NCW = 8, NLW = 4, NTHR = 64 * (NCW + NLW + 1);
constexpr int L_K = 0, L_WO = 0, L_V = 65536, L_P = 131072, P_STR = 576, L_Q = L_P + 4 * P_STR, Q_STR = 320, L_QP = L_Q + 4 * Q_STR,
              L_MX = L_QP + 4096, L_LS = L_MX + 128, L_O = L_LS + 128, L_Z = L_O + 64, L_RED = L_Z + 64, L_CNT = L_RED + 512, L_DUM = L_CNT + 128, L_END = L_DUM + 512;
enum { C_FLAG = L_CNT, C_KL = L_CNT + 32, C_QP = L_CNT + 48, C_PF = L_CNT + 64, C_VH = L_CNT + 96, C_WO = L_CNT + 112 };
constexpr unsigned EMPTY = 0xFFFFFFFFu;
constexpr unsigned W_E = 0, W_Q = 4096, W_ML = W_Q + 16 * 16384, W_O = W_ML + 16384, W_F = W_O + (1u << 20), W_END = W_F + (1u << 20);
constexpr unsigned SPIN_CAP = 1u << 20;

#define XG 4
#define TLBPF 1
#define KWND 6
#define LWND 63
#define TUNEQ 44
#define TUNE2 12
#define WODELAY 4
#define KDELAY 8
#define CODEPF 8
#define CODEPF_TAIL 16
#define GAP1 8
#define GAP2 8

#define STAMPX(i) do { } while (0)
#define STAMP(i) do { } while (0)
#define LDS_EXTRA 0
#define STAMP0(i) do { if (wave == 0) STAMP(i); } while (0)

struct Params { const u16* x; const u16* gw; const u16* wq[2]; const u16* k[2]; const u16* v[2]; const u16* wo[2]; u16* out; unsigned char* ws; };

__device__ __forceinline__ float bflo(unsigned w) { return __builtin_bit_cast(float, w << 16); }
__device__ __forceinline__ float bfhi(unsigned w) { return __builtin_bit_cast(float, w & 0xffff0000u); }
__device__ __forceinline__ unsigned cvtpk(float lo, float hi) { return __builtin_bit_cast(unsigned, __builtin_convertvector(f32x2{lo, hi}, bf16x2)); }
__device__ __forceinline__ float rbf(float f) { return bflo(cvtpk(f, f)); }
__device__ __forceinline__ float dot2(unsigned a, unsigned b, float acc) { return __builtin_amdgcn_fdot2_f32_bf16(__builtin_bit_cast(bf16x2, a), __builtin_bit_cast(bf16x2, b), acc, false); }
template <int CTRL> __device__ __forceinline__ float dpp(float x) { return __builtin_bit_cast(float, __builtin_amdgcn_mov_dpp(__builtin_bit_cast(int, x), CTRL, 0xf, 0xf, true)); }
__device__ __forceinline__ float u2f(unsigned u) { return __builtin_bit_cast(float, u); }
__device__ __forceinline__ unsigned f2u(float f) { return __builtin_bit_cast(unsigned, f); }
__device__ __forceinline__ float rs32(float a, float b) { auto r = __builtin_amdgcn_permlane32_swap(f2u(a), f2u(b), false, false); return u2f(r[0]) + u2f(r[1]); }
__device__ __forceinline__ float rs16(float a, float b) { auto r = __builtin_amdgcn_permlane16_swap(f2u(a), f2u(b), false, false); return u2f(r[0]) + u2f(r[1]); }
__device__ __forceinline__ float xrow_max(float x) {
  auto s = __builtin_amdgcn_permlane16_swap(f2u(x), f2u(x), false, false); x = fmaxf(u2f(s[0]), u2f(s[1]));
  auto t = __builtin_amdgcn_permlane32_swap(f2u(x), f2u(x), false, false); return fmaxf(u2f(t[0]), u2f(t[1]));
}
__device__ __forceinline__ float xrow_sum(float x) { x = rs16(x, x); return rs32(x, x); }
__device__ __forceinline__ f32x4 mfma(v4u a, v4u b, f32x4 c) { return __builtin_amdgcn_mfma_f32_16x16x32_bf16(__builtin_bit_cast(bf16x8, a), __builtin_bit_cast(bf16x8, b), c, 0, 0, 0); }
__device__ __forceinline__ v4u tr_pair(const LAS unsigned char* a0, int off2) {
  const s16x4 lo = __builtin_amdgcn_ds_read_tr16_b64_v4i16((LAS s16x4*)a0), hi = __builtin_amdgcn_ds_read_tr16_b64_v4i16((LAS s16x4*)(a0 + off2));
  return __builtin_bit_cast(v4u, __builtin_shufflevector(lo, hi, 0, 1, 2, 3, 4, 5, 6, 7));
}
__device__ __forceinline__ float pick4(f32x4 o, bool b0, bool b1) { const float x = b0 ? o[1] : o[0], y = b0 ? o[3] : o[2]; return b1 ? y : x; }
template <int STRIDE, int WND> __device__ __forceinline__ void dma_run(const void* sbase, unsigned voff, unsigned ldst, unsigned n) {
  asm volatile("s_mov_b32 m0, %3\n\ts_nop 0\n"
               "1:\n\ts_waitcnt vmcnt(%5)\n\tglobal_load_lds_dwordx4 %0, %2 nt\n\tv_add_u32 %0, %4, %0\n\ts_add_u32 m0, m0, %4\n\ts_sub_u32 %1, %1, 1\n\ts_cmp_lg_u32 %1, 0\n\ts_cbranch_scc1 1b"
               : "+v"(voff), "+s"(n) : "s"(sbase), "s"(ldst), "n"(STRIDE), "n"(WND - 1) : "memory", "scc");
}
#define WAIT_VM(n) asm volatile("s_waitcnt vmcnt(" #n ")" ::: "memory")
#define WAIT_LGKM() asm volatile("s_waitcnt lgkmcnt(0)" ::: "memory")
#define BARRIER() asm volatile("s_waitcnt lgkmcnt(0)\n\ts_barrier" ::: "memory")
#define RELOAD() asm volatile("" ::: "memory")
__device__ __forceinline__ unsigned lds_add(unsigned addr) {
  unsigned old, one = 1u;
  asm volatile("s_waitcnt lgkmcnt(0)\n\ts_mov_b64 exec, 1\n\tds_add_rtn_u32 %0, %1, %2\n\ts_waitcnt lgkmcnt(0)\n\ts_mov_b64 exec, -1" : "=&v"(old) : "v"(addr), "v"(one) : "memory");
  return __builtin_amdgcn_readfirstlane(old);
}
__device__ __forceinline__ void st_granule(unsigned char* ws, unsigned off, float v, unsigned tag) {
  __hip_atomic_store((GAS unsigned long long*)(ws + off), ((unsigned long long)tag << 32) | f2u(v), RLX_AGENT);
}
__device__ __forceinline__ v4u ld_sc1(__amdgpu_buffer_rsrc_t r, int off) { return __builtin_amdgcn_raw_buffer_load_b128(r, off, 0, 16); }
__device__ __forceinline__ unsigned tag_bad(v4u d, unsigned tag) { return (d[1] ^ tag) | (d[3] ^ tag); }
#define PL4(a, b, c, d) "global_load_dwordx4 " a ", %[off], %[base] sc1\n\tglobal_load_dwordx4 " b ", %[off], %[base] offset:1024 sc1\n\t" \
                        "global_load_dwordx4 " c ", %[off], %[base] offset:2048 sc1\n\tglobal_load_dwordx4 " d ", %[off], %[base] offset:3072 sc1\n\t"
#define PL4A PL4("v[96:99]", "v[100:103]", "v[104:107]", "v[108:111]")
#define PL4B PL4("v[112:115]", "v[116:119]", "v[120:123]", "v[124:127]")
#define PCHK(a, b, c, d, e, f, g, h) "v_xor_b32 %[t0], %[tag], " a "\n\tv_xor_b32 %[t1], %[tag], " b "\n\tv_xor_b32 %[t2], %[tag], " c "\n\tv_or3_b32 %[t0], %[t0], %[t1], %[t2]\n\t" \
  "v_xor_b32 %[t1], %[tag], " d "\n\tv_xor_b32 %[t2], %[tag], " e "\n\tv_or3_b32 %[t0], %[t0], %[t1], %[t2]\n\t" \
  "v_xor_b32 %[t1], %[tag], " f "\n\tv_xor_b32 %[t2], %[tag], " g "\n\tv_or3_b32 %[t0], %[t0], %[t1], %[t2]\n\t" \
  "v_xor_b32 %[t1], %[tag], " h "\n\tv_or_b32 %[t0], %[t0], %[t1]\n\tv_cmp_ne_u32 vcc, 0, %[t0]\n\t"
template <int GAP> __device__ __forceinline__ void poll4(float (&x)[4], float (&y)[4], const unsigned char* base, unsigned off, unsigned tag) {
  unsigned t0, t1, t2, cnt = SPIN_CAP;
  asm volatile(PL4A "s_sleep %[gap]\n"
               "1:\n\t" PL4B "s_waitcnt vmcnt(4)\n\t" PCHK("v97", "v99", "v101", "v103", "v105", "v107", "v109", "v111") "s_cbranch_vccz 2f\n\t"
               PL4A "s_waitcnt vmcnt(4)\n\t" PCHK("v113", "v115", "v117", "v119", "v121", "v123", "v125", "v127") "s_cbranch_vccz 3f\n\t"
               "s_sub_u32 %[cnt], %[cnt], 1\n\ts_cmp_lg_u32 %[cnt], 0\n\ts_cbranch_scc1 1b\n"
               "2:\n\tv_mov_b32 %[x0], v96\n\tv_mov_b32 %[y0], v98\n\tv_mov_b32 %[x1], v100\n\tv_mov_b32 %[y1], v102\n\tv_mov_b32 %[x2], v104\n\tv_mov_b32 %[y2], v106\n\tv_mov_b32 %[x3], v108\n\tv_mov_b32 %[y3], v110\n\ts_branch 4f\n"
               "3:\n\tv_mov_b32 %[x0], v112\n\tv_mov_b32 %[y0], v114\n\tv_mov_b32 %[x1], v116\n\tv_mov_b32 %[y1], v118\n\tv_mov_b32 %[x2], v120\n\tv_mov_b32 %[y2], v122\n\tv_mov_b32 %[x3], v124\n\tv_mov_b32 %[y3], v126\n"
               "4:"
               : [x0] "=&v"(x[0]), [y0] "=&v"(y[0]), [x1] "=&v"(x[1]), [y1] "=&v"(y[1]), [x2] "=&v"(x[2]), [y2] "=&v"(y[2]), [x3] "=&v"(x[3]), [y3] "=&v"(y[3]),
                 [t0] "=&v"(t0), [t1] "=&v"(t1), [t2] "=&v"(t2), [cnt] "+s"(cnt)
               : [off] "v"(off), [base] "s"(base), [tag] "s"(tag), [gap] "n"(GAP)
               : "memory", "vcc", "scc", "v96", "v97", "v98", "v99", "v100", "v101", "v102", "v103", "v104", "v105", "v106", "v107", "v108", "v109", "v110", "v111",
                 "v112", "v113", "v114", "v115", "v116", "v117", "v118", "v119", "v120", "v121", "v122", "v123", "v124", "v125", "v126", "v127");
}
__device__ __forceinline__ bool none(unsigned bad) { return __builtin_amdgcn_ballot_w64(bad != 0u) == 0ull; }

__global__ void __launch_bounds__(NTHR, 4) fused_decode(Params p) {
  __shared__ __attribute__((aligned(16))) unsigned char lds_raw[L_END + LDS_EXTRA];
  LAS unsigned char* L = (LAS unsigned char*)lds_raw;
  unsigned long long pc0;
  asm volatile("s_getpc_b64 %0" : "=s"(pc0));
  const int tid = threadIdx.x, lane = tid & 63, wave = __builtin_amdgcn_readfirstlane(tid >> 6);
  const int b = blockIdx.x, g = b & 15, s = b >> 4, br = g >> 3, kvh = g & 7;
  const int go = g ^ XG, kvo = kvh ^ XG;
  const int lr = lane >> 4, lc = lane & 15;
  const size_t kvoff = ((size_t)kvh * SK + (SK - WIN) + s * 256) * DH;
  const u16 *px = p.x, *pgw = p.gw, *pwq = p.wq[br], *pk = p.k[br], *pv = p.v[br], *pwo = p.wo[br];
  u16* pout = p.out; unsigned char* pws = p.ws;
  asm volatile("" : "+s"(px), "+s"(pgw), "+s"(pwq), "+s"(pk), "+s"(pv), "+s"(pwo), "+s"(pout), "+s"(pws));

  if (wave >= NCW && wave < NCW + NLW) {
    const int j = wave - NCW;
    if (j == 0) {
      if (lane < 32) { *(LAS unsigned*)(L + L_CNT + 4 * lane) = 0u; *(LAS unsigned*)(L + L_MX + 4 * lane) = EMPTY; *(LAS unsigned*)(L + L_O + 4 * lane) = lane < 16 ? EMPTY : 0u; }
    }
    const unsigned char* kb = (const unsigned char*)(pk + kvoff) + j * 1024;
    const unsigned char* vb = (const unsigned char*)(pv + kvoff) + j * 1024;
    const GAS v4u* wb = (const GAS v4u*)(pwo + ((size_t)kvo * 512 + s * 32 + j * 8) * DM) + lane;
    const unsigned ko = lr * 256 + ((lc ^ (4 * j + lr)) << 4);
    const unsigned vo = lr * 256 + ((lc ^ ((lr | ((j & 2) << 1)) << 1)) << 4);
    const unsigned ldum = L_DUM + 8 * lane;
    BARRIER();
    if (KDELAY) __builtin_amdgcn_s_sleep(KDELAY);
    dma_run<4096, KWND>(kb, ko, L_K + j * 1024, 16);
    dma_run<4096, LWND>(vb, vo, L_V + j * 1024, 8);
    WAIT_VM(8);
    *(volatile LAS unsigned*)(L + (lane == 0 ? C_KL + 4 * j : ldum)) = 1u;
    STAMPX(1);
    dma_run<4096, LWND>(vb, vo + 32768, L_V + 32768 + j * 1024, 8);
    v4u w[8];
#pragma unroll
    for (int i = 0; i < 8; ++i) w[i] = __builtin_nontemporal_load(wb + i * 64);
    WAIT_VM(16);
    *(volatile LAS unsigned*)(L + (lane == 0 ? C_VH + 4 * j : ldum)) = 1u;
    WAIT_VM(8);
    STAMPX(8);
    BARRIER();
    if (WODELAY) __builtin_amdgcn_s_sleep(WODELAY);
#pragma unroll
    for (int i = 0; i < 8; ++i) *(LAS v4u*)(L + L_WO + (8 * j + i) * 1024 + ((lane ^ (2 * i)) << 4)) = w[i];
    *(volatile LAS unsigned*)(L + (lane == 0 ? C_WO + 4 * j : ldum)) = 1u;
    STAMPX(7);
    return;
  }

  const __amdgpu_buffer_rsrc_t wsr = __builtin_amdgcn_make_buffer_rsrc(pws, 0, W_END, 0x00020000);
  if (wave == NCW + NLW) {
    unsigned c0 = 0, c1 = 0;
    if (CODEPF && b < CODEPF) {
      const GAS unsigned char* cp = (const GAS unsigned char*)(pc0 & ~127ull) + lane * 128;
      c0 = *(const GAS unsigned*)cp; if (lane < CODEPF_TAIL) c1 = *(const GAS unsigned*)(cp + 8192);
    }
    asm volatile("s_barrier" ::: "memory");
    unsigned ep;
    asm volatile("s_load_dword %0, %1, 0x0" : "=s"(ep) : "s"(pws + W_E) : "memory");
    asm volatile("" :: "v"(c0), "v"(c1));
    BARRIER();
    asm volatile("" : "+s"(ep));
    const unsigned tag = ep + 1u;
    __builtin_amdgcn_s_setprio(3);
    const int moff = W_ML + ((go * 16 + lr) * 4 + (s >> 2)) * 16;
    v4u ml[4];
#pragma clang loop unroll(disable)
    for (unsigned it = 0; it < SPIN_CAP; ++it) {
      RELOAD();
      unsigned bad = 0;
#pragma unroll
      for (int i = 0; i < 4; ++i) ml[i] = ld_sc1(wsr, moff + i * 256);
#pragma unroll
      for (int i = 0; i < 4; ++i) bad |= tag_bad(ml[i], tag);
      if (none(bad)) break;
    }
    const float M = xrow_max(fmaxf(fmaxf(u2f(ml[0][0]), u2f(ml[1][0])), fmaxf(u2f(ml[2][0]), u2f(ml[3][0]))));
    float wgt[4], Ls = 0.f;
#pragma unroll
    for (int i = 0; i < 4; ++i) { wgt[i] = __builtin_amdgcn_exp2f(u2f(ml[i][0]) - M); Ls = fmaf(wgt[i], u2f(ml[i][2]), Ls); }
    const float inv = __builtin_amdgcn_rcpf(xrow_sum(Ls));
#pragma unroll
    for (int i = 0; i < 4; ++i) wgt[i] *= inv;
    unsigned oaddr = lane < 16 ? L_O + 16 * ((lane >> 1) & 3) + 8 * (lane >> 3) + 4 * (lane & 1) : L_DUM + 8 * lane;
    asm volatile("" : "+v"(wgt[0]), "+v"(wgt[1]), "+v"(wgt[2]), "+v"(wgt[3]), "+v"(oaddr));
    float x[4], y[4];
    poll4<GAP1>(x, y, pws, W_O + (go * 16 + s) * 4096 + lane * 16, tag);
    float ov = rs32(fmaf(wgt[0], x[0], wgt[1] * x[1]) + fmaf(wgt[2], x[2], wgt[3] * x[3]), fmaf(wgt[0], y[0], wgt[1] * y[1]) + fmaf(wgt[2], y[2], wgt[3] * y[3]));
    ov = rs16(ov, ov);
    const auto sw = __builtin_amdgcn_permlane32_swap(f2u(ov), f2u(ov), false, false);
    *(LAS unsigned*)(L + oaddr) = cvtpk(u2f(sw[0]), u2f(sw[1]));
    STAMP(10);
    return;
  }

  const int cw = wave;
  __builtin_amdgcn_s_setprio(2);
  STAMP0(0);
  unsigned tag;
  float gate = 0.f;
  const int q4 = lc >> 2, p4 = lane & 3;
  const bool qb0 = (lane & 4) != 0, qb1 = (lane & 8) != 0;
  const unsigned dum = L_DUM + 8 * lane;
  {
    const int kq = s >> 2, hq = s & 3, row0 = 128 * kq + 16 * cw;
    const GAS v4u* wp = (const GAS v4u*)(pwq + (size_t)(row0 + lr) * NQ + kvo * 512 + 128 * hq + 8 * lc);
    v4u w[4];
#pragma unroll
    for (int i = 0; i < 4; ++i) w[i] = __builtin_nontemporal_load(wp + (size_t)i * (NQ / 2));
    typedef unsigned s8u __attribute__((ext_vector_type(8)));
    s8u xs;
    asm volatile("s_load_dwordx8 %0, %1, 0x0" : "=s"(xs) : "s"(px + row0) : "memory");
    const bool gw = wave == 0 && b < 32;
    const v4u gx = *(gw ? (const GAS v4u*)(px + 8 * lane) : wp), gg = *(gw ? (const GAS v4u*)(pgw + 8 * lane) : wp);
    RELOAD();
    unsigned tlb = 0;
    if (TLBPF) {
      const unsigned char *a0 = (const unsigned char*)(pk + kvoff), *a1 = (const unsigned char*)(pv + kvoff), *a2 = (const unsigned char*)(pwo + ((size_t)kvo * 512 + s * 32) * DM), *a3 = pws + W_F + b * 4096;
      asm volatile("" : "+s"(a0), "+s"(a1), "+s"(a2), "+s"(a3));
      const unsigned char* a = (wave & 2) ? ((wave & 1) ? a3 : a2) : ((wave & 1) ? a1 : a0);
      asm volatile("s_load_dword %0, %1, 0x0" : "=s"(tlb) : "s"(a) : "memory");
    }
    asm volatile("s_barrier" ::: "memory");
    unsigned ep;
    asm volatile("s_load_dword %0, %1, 0x0" : "=s"(ep) : "s"(pws + W_E) : "memory");
    asm volatile("s_waitcnt lgkmcnt(0)" : "+s"(xs), "+s"(ep), "+s"(tlb), "+v"(w[0]), "+v"(w[1]) :: "memory");
    tag = ep + 1u;
    const unsigned xd[4] = {lr & 2 ? xs[1] : xs[0], lr & 2 ? xs[3] : xs[2], lr & 2 ? xs[5] : xs[4], lr & 2 ? xs[7] : xs[6]};
    const unsigned xsel = lr & 1 ? 0x07060302u : 0x05040100u;
    float acc[8];
#pragma unroll
    for (int k = 0; k < 8; ++k) acc[k] = 0.f;
#pragma unroll
    for (int i = 0; i < 4; i += 2) {
      const unsigned xp = __builtin_amdgcn_perm(xd[i + 1], xd[i], xsel);
#pragma unroll
      for (int d = 0; d < 4; ++d) {
        acc[2 * d] = dot2(__builtin_amdgcn_perm(w[i + 1][d], w[i][d], 0x05040100u), xp, acc[2 * d]);
        acc[2 * d + 1] = dot2(__builtin_amdgcn_perm(w[i + 1][d], w[i][d], 0x07060302u), xp, acc[2 * d + 1]);
      }
    }
    const float t0 = rs32(acc[0], acc[4]), t1 = rs32(acc[1], acc[5]), t2 = rs32(acc[2], acc[6]), t3 = rs32(acc[3], acc[7]);
    const float u0 = rs16(t0, t2), u1 = rs16(t1, t3);
    *(LAS f32x2*)(L + L_QP + (cw * 128 + 8 * lc + 4 * (lane >> 5) + 2 * (lr & 1)) * 4) = f32x2{u0, u1};
    if (__builtin_expect(lds_add(C_QP) == NCW - 1, 1)) {
      STAMP(11);
      f32x2 qs = {0.f, 0.f};
#pragma unroll
      for (int i = 0; i < 8; i += 2) qs += *(const LAS f32x2*)(L + L_QP + (i * 128 + 2 * lane) * 4) + *(const LAS f32x2*)(L + L_QP + ((i + 1) * 128 + 2 * lane) * 4);
      __builtin_amdgcn_raw_buffer_store_b128(v4u{f2u(qs[0]), tag, f2u(qs[1]), tag}, wsr, W_Q + (go * 4 + kq) * 4096 + (128 * hq + 2 * lane) * 8, 0, 16);
      STAMP(2);
    }
    v4u gx_ = gx, gg_ = gg;
    asm volatile("" : "+v"(gx_), "+v"(gg_));
    if (__builtin_expect(gw, 1)) {
      float z = 0.f;
#pragma unroll
      for (int d = 0; d < 4; ++d) z += rbf(bflo(gx_[d]) * bflo(gg_[d])) + rbf(bfhi(gx_[d]) * bfhi(gg_[d]));
      z += dpp<0xB1>(z); z += dpp<0x4E>(z); z += dpp<0x124>(z); z += dpp<0x128>(z); z = xrow_sum(z);
      gate = rbf(__builtin_amdgcn_rcpf(1.f + __expf(-rbf(z))));
    }
  }
  {
    const int off = W_Q + g * 16384 + (lane >> 5) * 4096 + (64 * cw + 2 * (lane & 31)) * 8;
    v4u a, a2;
    if (TUNEQ) __builtin_amdgcn_s_sleep(TUNEQ);
#pragma clang loop unroll(disable)
    for (unsigned i = 0; i < SPIN_CAP; ++i) {
      RELOAD(); a = ld_sc1(wsr, off); a2 = ld_sc1(wsr, off + 8192);
      if (none(tag_bad(a, tag) | tag_bad(a2, tag))) break;
    }
    const float qv = rs32(u2f(a[0]) + u2f(a2[0]), u2f(a[2]) + u2f(a2[2]));
    *(LAS u16*)(L + L_Q + (cw >> 1) * Q_STR + ((cw & 1) * 64 + 2 * (lane & 31) + (lane >> 5)) * 2) = (u16)cvtpk(qv, qv);
    *(volatile LAS unsigned*)(L + (lane == 0 ? C_FLAG + 4 * cw : dum)) = 1u;
    STAMPX(3);
  }
  unsigned ka = L_K + (32 * cw + lc) * 256 + ((lr ^ lc) << 4), qa = L_Q + p4 * Q_STR + 16 * lr, pa = L_P + p4 * P_STR + 16 * lr;
  unsigned pw = (lane & 12) == 0 ? L_P + p4 * P_STR + (32 * cw + 4 * lr) * 2 : dum, pw2 = (lane & 12) == 0 ? pw + 32 : dum;
  unsigned va = L_V + (8 * lr + q4) * 256 + (((2 * cw + (p4 >> 1)) ^ ((q4 | ((lr & 1) << 2)) << 1)) << 4) + 8 * (p4 & 1);
  unsigned wa = L_WO + (4 * lr + q4) * 1024 + (((8 * cw + (p4 >> 1)) ^ ((q4 | ((lr & 1) << 2)) << 1)) << 4) + 8 * (p4 & 1);
  unsigned oo = W_O + ((g * 16 + 4 * p4 + (cw >> 1)) * 16 + s) * 256 + (16 * (cw & 1) + 4 * lr + q4) * 8;
  unsigned fo = W_F + ((4 * cw + p4) * 256 + go * 16 + s) * 128 + (4 * lr + q4) * 8;
  unsigned ba0 = p4 == 0 ? L_O + 16 * lr : L_Z, ba1 = p4 == 1 ? L_O + 16 * lr : L_Z, ba2 = p4 == 2 ? L_O + 16 * lr : L_Z, ba3 = p4 == 3 ? L_O + 16 * lr : L_Z;
  asm volatile("" : "+v"(ka), "+v"(qa), "+v"(pw), "+v"(pw2), "+v"(pa), "+v"(va), "+v"(wa), "+v"(oo), "+v"(fo), "+v"(ba0), "+v"(ba1), "+v"(ba2), "+v"(ba3));
#pragma clang loop unroll(disable)
  for (unsigned i = 0; i < SPIN_CAP; ++i) if (none(*(volatile LAS unsigned*)(L + C_FLAG + 4 * (lane < 12 ? lane : 0)) == 0u)) break;
  RELOAD();
  STAMP0(5);
  float mc;
  {
    v4u qf[4], kf[2][4];
#pragma unroll
    for (int ks = 0; ks < 4; ++ks) { kf[0][ks] = *(const LAS v4u*)(L + (ka ^ (64 * ks))); qf[ks] = *(const LAS v4u*)(L + qa + 64 * ks); kf[1][ks] = *(const LAS v4u*)(L + (ka ^ (64 * ks)) + 4096); }
    __builtin_amdgcn_sched_barrier(0);
    f32x4 sc[2] = {{0.f, 0.f, 0.f, 0.f}, {0.f, 0.f, 0.f, 0.f}};
#pragma unroll
    for (int ks = 0; ks < 4; ++ks) { sc[0] = mfma(kf[0][ks], qf[ks], sc[0]); sc[1] = mfma(kf[1][ks], qf[ks], sc[1]); }
    float m = fmaxf(fmaxf(fmaxf(sc[0][0], sc[0][1]), fmaxf(sc[0][2], sc[0][3])), fmaxf(fmaxf(sc[1][0], sc[1][1]), fmaxf(sc[1][2], sc[1][3])));
    m = xrow_max(m);
    *(volatile LAS float*)(L + (lane < 4 ? L_MX + (lane * 8 + cw) * 4 : dum)) = m;
    constexpr float C2 = 0.08838834764831845f * 1.4426950408889634f;
    v4u mx0, mx1;
#pragma clang loop unroll(disable)
    for (unsigned i = 0; i < SPIN_CAP; ++i) {
      mx0 = *(volatile LAS v4u*)(L + L_MX + 32 * p4); mx1 = *(volatile LAS v4u*)(L + L_MX + 32 * p4 + 16);
      const unsigned top = max(max(max(mx0[0], mx0[1]), max(mx0[2], mx0[3])), max(max(mx1[0], mx1[1]), max(mx1[2], mx1[3])));
      if (none(top == EMPTY)) break;
    }
    mc = fmaxf(fmaxf(fmaxf(u2f(mx0[0]), u2f(mx0[1])), fmaxf(u2f(mx0[2]), u2f(mx0[3]))), fmaxf(fmaxf(u2f(mx1[0]), u2f(mx1[1])), fmaxf(u2f(mx1[2]), u2f(mx1[3])))) * C2;
    float ls = 0.f;
    v2u pk[2];
#pragma unroll
    for (int t = 0; t < 2; ++t) {
      float e[4];
#pragma unroll
      for (int r = 0; r < 4; ++r) { e[r] = __builtin_amdgcn_exp2f(fmaf(sc[t][r], C2, -mc)); ls += e[r]; }
      pk[t] = v2u{cvtpk(e[0], e[1]), cvtpk(e[2], e[3])};
    }
    ls = xrow_sum(ls);
    *(LAS v2u*)(L + pw) = pk[0]; *(LAS v2u*)(L + pw2) = pk[1];
    *(LAS float*)(L + (lane < 4 ? L_LS + (lane * 8 + cw) * 4 : dum)) = ls;
    *(volatile LAS unsigned*)(L + (lane == 0 ? C_PF + 4 * cw : dum)) = 1u;
    STAMPX(6);
  }
  f32x4 o = {0.f, 0.f, 0.f, 0.f};
  v4u pf[8];
  {
#pragma clang loop unroll(disable)
    for (unsigned i = 0; i < SPIN_CAP; ++i) if (none(*(volatile LAS unsigned*)(L + C_PF + 4 * (lane < 12 ? lane : 0)) == 0u)) break;
    RELOAD();
    v4u vf[4];
#pragma unroll
    for (int ks = 0; ks < 4; ++ks) { vf[ks] = tr_pair(L + va + 8192 * ks, 1024); pf[ks] = *(const LAS v4u*)(L + pa + 64 * ks); }
#pragma unroll
    for (int ks = 4; ks < 8; ++ks) pf[ks] = *(const LAS v4u*)(L + pa + 64 * ks);
    if (__builtin_expect(cw == NCW - 1, 1)) {
      const f32x4 l0 = *(const LAS f32x4*)(L + L_LS + 32 * p4), l1 = *(const LAS f32x4*)(L + L_LS + 32 * p4 + 16);
      const v4u ml = {f2u(mc), tag, f2u(((l0[0] + l0[1]) + (l0[2] + l0[3])) + ((l1[0] + l1[1]) + (l1[2] + l1[3]))), tag};
      __builtin_amdgcn_raw_buffer_store_b128(ml, wsr, lane < 4 ? W_ML + ((g * 16 + s) * 4 + lane) * 16 : W_END, 0, 16);
    }
#pragma unroll
    for (int ks = 0; ks < 4; ++ks) o = mfma(vf[ks], pf[ks], o);
    asm volatile("" : "+v"(o), "+v"(pf[4]), "+v"(pf[5]), "+v"(pf[6]), "+v"(pf[7]));
  }
  BARRIER();
  {
    v4u vf[4];
#pragma unroll
    for (int ks = 0; ks < 4; ++ks) vf[ks] = tr_pair(L + va + 8192 * (ks + 4), 1024);
    __builtin_amdgcn_sched_barrier(0);
#pragma unroll
    for (int ks = 0; ks < 4; ++ks) o = mfma(vf[ks], pf[ks + 4], o);
    st_granule(pws, oo, pick4(o, qb0, qb1), tag);
    STAMPX(9);
  }
  {
#pragma clang loop unroll(disable)
    for (unsigned i = 0; i < SPIN_CAP; ++i) if (none(*(volatile LAS unsigned*)(L + C_WO + 4 * (lane & 3)) == 0u)) break;
    RELOAD();
    v4u wf[4];
#pragma unroll
    for (int t = 0; t < 4; ++t) wf[t] = tr_pair(L + (wa ^ (32 * t)), 16384);
    asm volatile("" : "+v"(wf[0]), "+v"(wf[1]), "+v"(wf[2]), "+v"(wf[3]));
    v4u b0, b1, b2, b3;
#pragma clang loop unroll(disable)
    for (unsigned i = 0; i < SPIN_CAP; ++i) {
      b0 = *(volatile LAS v4u*)(L + ba0); b1 = *(volatile LAS v4u*)(L + ba1); b2 = *(volatile LAS v4u*)(L + ba2); b3 = *(volatile LAS v4u*)(L + ba3);
      if (none(((b0[0] | b1[0]) | (b2[0] | b3[0])) == EMPTY)) break;
    }
    f32x4 r = {0.f, 0.f, 0.f, 0.f};
    r = mfma(wf[0], b0, r); r = mfma(wf[1], b1, r); r = mfma(wf[2], b2, r); r = mfma(wf[3], b3, r);
    st_granule(pws, fo, pick4(r, qb0, qb1), tag);
    STAMPX(12);
  }
  if (__builtin_expect(b < 32, 1)) {
    unsigned ra_ = (lane & 24) == 0 ? L_RED + ((2 * (lane & 7) + (lane >> 5)) * 8 + cw) * 4 : dum;
    float g1 = rbf(1.f - gate);
    asm volatile("" : "+v"(ra_), "+v"(g1));
    if (TUNE2) __builtin_amdgcn_s_sleep(TUNE2);
    float x[4], y[4];
    poll4<GAP2>(x, y, pws, W_F + b * 32768 + cw * 4096 + lane * 16, tag);
    float t = rs32((x[0] + x[1]) + (x[2] + x[3]), (y[0] + y[1]) + (y[2] + y[3]));
    t = rs16(t, t); t += dpp<0x128>(t);
    *(LAS float*)(L + ra_) = t;
    STAMPX(13);
    BARRIER();
    if (__builtin_expect(cw == 0, 1)) {
      const f32x4 ra = *(const LAS f32x4*)(L + L_RED + 32 * lc), rb = *(const LAS f32x4*)(L + L_RED + 32 * lc + 16);
      const float A = rbf((ra[0] + ra[1]) + (ra[2] + ra[3])), B = rbf((rb[0] + rb[1]) + (rb[2] + rb[3]));
      const u16 res = (u16)cvtpk(rbf(gate * A) + rbf(g1 * B), 0.f);
      __builtin_amdgcn_raw_buffer_store_b16(res, __builtin_amdgcn_make_buffer_rsrc(pout, 0, 2 * DM, 0x00020000), lane < 16 ? (16 * b + lane) * 2 : 2 * DM, 0, 0);
      __builtin_amdgcn_raw_buffer_store_b32(tag, wsr, (b | lane) == 0 ? W_E : W_END, 0, 16);
      STAMP(14);
    }
  }
}

extern "C" void kernel_launch(void* const* d_in, const int* in_sizes, int n_in, void* d_out, int out_size, void* d_ws, size_t ws_size, hipStream_t stream) {
  Params p;
  p.x = (const u16*)d_in[0]; p.gw = (const u16*)d_in[10];
  p.wq[0] = (const u16*)d_in[3]; p.wq[1] = (const u16*)d_in[8];
  p.k[0] = (const u16*)d_in[1]; p.k[1] = (const u16*)d_in[6];
  p.v[0] = (const u16*)d_in[2]; p.v[1] = (const u16*)d_in[7];
  p.wo[0] = (const u16*)d_in[4]; p.wo[1] = (const u16*)d_in[9];
  p.out = (u16*)d_out; p.ws = (unsigned char*)d_ws;
  fused_decode<<<NWG, NTHR, 0, stream>>>(p);
}
```

```cpp
#include <hip/hip_runtime.h>
#include <hip/hip_bf16.h>

typedef unsigned short u16;
typedef unsigned v4u __attribute__((ext_vector_type(4)));
typedef unsigned v2u __attribute__((ext_vector_type(2)));
typedef float f32x2 __attribute__((ext_vector_type(2)));
typedef float f32x4 __attribute__((ext_vector_type(4)));
typedef short s16x4 __attribute__((ext_vector_type(4)));
typedef __bf16 bf16x2 __attribute__((ext_vector_type(2)));
typedef __bf16 bf16x8 __attribute__((ext_vector_type(8)));
#define GAS __attribute__((address_space(1)))
#define LAS __attribute__((address_space(3)))
#define RLX_AGENT __ATOMIC_RELAXED, __HIP_MEMORY_SCOPE_AGENT

constexpr int DM = 512, SK = 32768, DH = 128, WIN = 4096, NQ = 4096;
constexpr int NWG = 256, NCW = 8, NLW = 4, NTHR = 64 * (NCW + NLW + 1);
constexpr int L_K = 0, L_WO = 0, L_V = 65536, L_P = 131072, P_STR = 576, L_Q = L_P + 4 * P_STR, Q_STR = 320, L_QP = L_Q + 4 * Q_STR,
              L_MX = L_QP + 4096, L_LS = L_MX + 128, L_O = L_LS + 128, L_Z = L_O + 64, L_RED = L_Z + 64, L_CNT = L_RED + 512, L_DUM = L_CNT + 256, L_END = L_DUM + 512;
enum { C_FLAG = L_CNT, C_KL = L_CNT + 32, C_QP = L_CNT + 48, C_PF = L_CNT + 64, C_VH = L_CNT + 96, C_WO = L_CNT + 112, C_PI = L_CNT + 128, C_TG = L_CNT + 160, C_TQ = L_CNT + 164, C_WT = L_CNT + 176, C_OP = L_CNT + 192, C_PC = L_CNT + 224 };
constexpr unsigned EMPTY = 0xFFFFFFFFu;
constexpr unsigned W_E = 0, W_Q = 4096, W_ML = W_Q + 16 * 16384, W_O = W_ML + 16384, W_F = W_O + (1u << 20), W_T = W_F + (1u << 20), W_END = W_T + 4096;
constexpr unsigned SPIN_CAP = 1u << 20;

#define XG 4
#define TLBPF 1
#define KWND 7
#define LWND 63
#define TUNEQ 44
#define TUNE2 12
#define WODELAY 4
#define KDELAY 8
#define CODEPF 8
#define CODEPF_TAIL 16
#define GAP1 8
#define GAP2 8

#define GATE1 0
#define GATE2 0
#define WOGATE 590
#define TSLEEP 40
#define KOPEN 0
#define V2EARLY 1
#define KPACE 1
#define TK0 100
#define DK 15
#define TV0 340
#define DV 15
#define VWND 8
#define TQ 380
#define TGMEAN 1
#define WOM 0
#define WW 7
#define ROT 3
#define ROTJ 2
#define ROTW 1
#define TGMAX 0
#define RA 0
#define RB 2
#define RC 8
#define RD 7
#define WOA 6
#define TUNE1 24
#define WOTRIG 0
#define STAMPX(i) do { } while (0)
#define STAMP(i) do { } while (0)
#define LDS_EXTRA 0
#define STAMP0(i) do { if (wave == 0) STAMP(i); } while (0)

struct Params { const u16* x; const u16* gw; const u16* wq[2]; const u16* k[2]; const u16* v[2]; const u16* wo[2]; u16* out; unsigned char* ws; };

__device__ __forceinline__ float bflo(unsigned w) { return __builtin_bit_cast(float, w << 16); }
__device__ __forceinline__ float bfhi(unsigned w) { return __builtin_bit_cast(float, w & 0xffff0000u); }
__device__ __forceinline__ unsigned cvtpk(float lo, float hi) { return __builtin_bit_cast(unsigned, __builtin_convertvector(f32x2{lo, hi}, bf16x2)); }
__device__ __forceinline__ float rbf(float f) { return bflo(cvtpk(f, f)); }
__device__ __forceinline__ float dot2(unsigned a, unsigned b, float acc) { return __builtin_amdgcn_fdot2_f32_bf16(__builtin_bit_cast(bf16x2, a), __builtin_bit_cast(bf16x2, b), acc, false); }
template <int CTRL> __device__ __forceinline__ float dpp(float x) { return __builtin_bit_cast(float, __builtin_amdgcn_mov_dpp(__builtin_bit_cast(int, x), CTRL, 0xf, 0xf, true)); }
__device__ __forceinline__ float u2f(unsigned u) { return __builtin_bit_cast(float, u); }
__device__ __forceinline__ unsigned f2u(float f) { return __builtin_bit_cast(unsigned, f); }
__device__ __forceinline__ float rs32(float a, float b) { auto r = __builtin_amdgcn_permlane32_swap(f2u(a), f2u(b), false, false); return u2f(r[0]) + u2f(r[1]); }
__device__ __forceinline__ float rs16(float a, float b) { auto r = __builtin_amdgcn_permlane16_swap(f2u(a), f2u(b), false, false); return u2f(r[0]) + u2f(r[1]); }
__device__ __forceinline__ float xrow_max(float x) {
  auto s = __builtin_amdgcn_permlane16_swap(f2u(x), f2u(x), false, false); x = fmaxf(u2f(s[0]), u2f(s[1]));
  auto t = __builtin_amdgcn_permlane32_swap(f2u(x), f2u(x), false, false); return fmaxf(u2f(t[0]), u2f(t[1]));
}
__device__ __forceinline__ float xrow_sum(float x) { x = rs16(x, x); return rs32(x, x); }
__device__ __forceinline__ f32x4 mfma(v4u a, v4u b, f32x4 c) { return __builtin_amdgcn_mfma_f32_16x16x32_bf16(__builtin_bit_cast(bf16x8, a), __builtin_bit_cast(bf16x8, b), c, 0, 0, 0); }
__device__ __forceinline__ v4u tr_pair(const LAS unsigned char* a0, int off2) {
  const s16x4 lo = __builtin_amdgcn_ds_read_tr16_b64_v4i16((LAS s16x4*)a0), hi = __builtin_amdgcn_ds_read_tr16_b64_v4i16((LAS s16x4*)(a0 + off2));
  return __builtin_bit_cast(v4u, __builtin_shufflevector(lo, hi, 0, 1, 2, 3, 4, 5, 6, 7));
}
__device__ __forceinline__ float pick4(f32x4 o, bool b0, bool b1) { const float x = b0 ? o[1] : o[0], y = b0 ? o[3] : o[2]; return b1 ? y : x; }
template <int STRIDE, int WND> __device__ __forceinline__ void dma_run(const void* sbase, unsigned voff, unsigned ldst, unsigned n) {
  asm volatile("s_mov_b32 m0, %3\n\ts_nop 0\n"
               "1:\n\ts_waitcnt vmcnt(%5)\n\tglobal_load_lds_dwordx4 %0, %2 nt\n\tv_add_u32 %0, %4, %0\n\ts_add_u32 m0, m0, %4\n\ts_sub_u32 %1, %1, 1\n\ts_cmp_lg_u32 %1, 0\n\ts_cbranch_scc1 1b"
               : "+&v"(voff), "+&s"(n) : "s"(sbase), "s"(ldst), "n"(STRIDE), "n"(WND - 1) : "memory", "scc");
}
template <int STRIDE, int WND, int OPEN> __device__ __forceinline__ void dma_run_open(const void* sbase, unsigned voff, unsigned ldst, unsigned n, unsigned faddr) {
  unsigned f, sf;
  asm volatile("s_mov_b32 m0, %5\n\ts_nop 0\n"
               "1:\n\tds_read_b32 %2, %9\n\ts_waitcnt vmcnt(%7)\n\tglobal_load_lds_dwordx4 %0, %4 nt\n\tv_add_u32 %0, %6, %0\n\ts_add_u32 m0, m0, %6\n\ts_sub_u32 %1, %1, 1\n\ts_cmp_eq_u32 %1, 0\n\ts_cbranch_scc1 3f\n\t"
               "s_waitcnt lgkmcnt(0)\n\tv_readfirstlane_b32 %3, %2\n\ts_cmp_lt_u32 %3, %8\n\ts_cbranch_scc1 1b\n"
               "2:\n\tglobal_load_lds_dwordx4 %0, %4 nt\n\tv_add_u32 %0, %6, %0\n\ts_add_u32 m0, m0, %6\n\ts_sub_u32 %1, %1, 1\n\ts_cmp_lg_u32 %1, 0\n\ts_cbranch_scc1 2b\n"
               "3:\n\ts_waitcnt lgkmcnt(0)"
               : "+&v"(voff), "+&s"(n), "=&v"(f), "=&s"(sf) : "s"(sbase), "s"(ldst), "n"(STRIDE), "n"(WND - 1), "n"(OPEN), "v"(faddr) : "memory", "scc");
}
template <int STRIDE, int WND, int DELTA> __device__ __forceinline__ void dma_run_paced(const void* sbase, unsigned& voff, unsigned ldst, unsigned n, unsigned taddr, unsigned tnext) {
  unsigned f, sf;
  asm volatile("s_mov_b32 m0, %6\n"
               "1:\n\ts_waitcnt vmcnt(%8)\n"
               "2:\n\tds_read_b32 %3, %10\n\ts_memrealtime s[60:61]\n\ts_waitcnt lgkmcnt(0)\n\tv_readfirstlane_b32 %4, %3\n\ts_sub_u32 %4, s60, %4\n\ts_cmp_ge_i32 %4, %2\n\ts_cbranch_scc1 3f\n\ts_sleep 1\n\ts_branch 2b\n"
               "3:\n\tglobal_load_lds_dwordx4 %0, %5 nt\n\tv_add_u32 %0, %7, %0\n\ts_add_u32 m0, m0, %7\n\ts_add_u32 %2, %2, %9\n\ts_sub_u32 %1, %1, 1\n\ts_cmp_lg_u32 %1, 0\n\ts_cbranch_scc1 1b"
               : "+&v"(voff), "+&s"(n), "+&s"(tnext), "=&v"(f), "=&s"(sf) : "s"(sbase), "s"(ldst), "n"(STRIDE), "n"(WND - 1), "n"(DELTA), "v"(taddr) : "memory", "scc", "s60", "s61");
}
template <int WND, int DELTA> __device__ __forceinline__ void dma_run_rot(const void* sbase, unsigned voff, unsigned ldst, unsigned p0, unsigned n, unsigned rot, unsigned taddr, unsigned tnext) {
  unsigned f, sf, vt;
  asm volatile("s_lshl_b32 %4, %0, 12\n\ts_add_u32 m0, %8, %4\n\ts_add_u32 %0, %0, %9\n"
               "1:\n\ts_waitcnt vmcnt(%10)\n"
               "2:\n\tds_read_b32 %3, %12\n\ts_memrealtime s[60:61]\n\ts_waitcnt lgkmcnt(0)\n\tv_readfirstlane_b32 %4, %3\n\ts_sub_u32 %4, s60, %4\n\ts_cmp_ge_i32 %4, %2\n\ts_cbranch_scc1 3f\n\ts_sleep 1\n\ts_branch 2b\n"
               "3:\n\ts_and_b32 %4, %0, 15\n\ts_lshl_b32 %4, %4, 12\n\tv_add_u32 %5, %4, %7\n\t"
               "global_load_lds_dwordx4 %5, %6 nt\n\ts_add_u32 m0, m0, 4096\n\ts_add_u32 %0, %0, 1\n\ts_add_u32 %2, %2, %11\n\ts_sub_u32 %1, %1, 1\n\ts_cmp_lg_u32 %1, 0\n\ts_cbranch_scc1 1b"
               : "+&s"(p0), "+&s"(n), "+&s"(tnext), "=&v"(f), "=&s"(sf), "=&v"(vt)
               : "s"(sbase), "v"(voff), "s"(ldst), "s"(rot), "n"(WND - 1), "n"(DELTA), "v"(taddr) : "memory", "scc", "s60", "s61");
}
__device__ __forceinline__ void wait_until(unsigned taddr, unsigned t) {
  unsigned f, sf;
  asm volatile("2:\n\tds_read_b32 %0, %2\n\ts_memrealtime s[60:61]\n\ts_waitcnt lgkmcnt(0)\n\tv_readfirstlane_b32 %1, %0\n\ts_sub_u32 %1, s60, %1\n\ts_cmp_ge_i32 %1, %3\n\ts_cbranch_scc1 3f\n\ts_sleep 1\n\ts_branch 2b\n3:"
               : "=&v"(f), "=&s"(sf) : "v"(taddr), "s"(t) : "memory", "scc", "s60", "s61");
}
#define WAIT_VM(n) asm volatile("s_waitcnt vmcnt(" #n ")" ::: "memory")
#define WAIT_LGKM() asm volatile("s_waitcnt lgkmcnt(0)" ::: "memory")
#define BARRIER() asm volatile("s_waitcnt lgkmcnt(0)\n\ts_barrier" ::: "memory")
#define RELOAD() asm volatile("" ::: "memory")
__device__ __forceinline__ unsigned lds_add(unsigned addr) {
  unsigned old, one = 1u;
  asm volatile("s_waitcnt lgkmcnt(0)\n\ts_mov_b64 exec, 1\n\tds_add_rtn_u32 %0, %1, %2\n\ts_waitcnt lgkmcnt(0)\n\ts_mov_b64 exec, -1" : "=&v"(old) : "v"(addr), "v"(one) : "memory");
  return __builtin_amdgcn_readfirstlane(old);
}
__device__ __forceinline__ void st_granule(unsigned char* ws, unsigned off, float v, unsigned tag) {
  __hip_atomic_store((GAS unsigned long long*)(ws + off), ((unsigned long long)tag << 32) | f2u(v), RLX_AGENT);
}
__device__ __forceinline__ v4u ld_sc1(__amdgpu_buffer_rsrc_t r, int off) { return __builtin_amdgcn_raw_buffer_load_b128(r, off, 0, 16); }
__device__ __forceinline__ unsigned tag_bad(v4u d, unsigned tag) { return (d[1] ^ tag) | (d[3] ^ tag); }
#define PL4(a, b, c, d) "global_load_dwordx4 " a ", %[off], %[base] sc1\n\tglobal_load_dwordx4 " b ", %[off], %[base] offset:1024 sc1\n\t" \
                        "global_load_dwordx4 " c ", %[off], %[base] offset:2048 sc1\n\tglobal_load_dwordx4 " d ", %[off], %[base] offset:3072 sc1\n\t"
#define PL4A PL4("v[96:99]", "v[100:103]", "v[104:107]", "v[108:111]")
#define PL4B PL4("v[112:115]", "v[116:119]", "v[120:123]", "v[124:127]")
#define PCHK(a, b, c, d, e, f, g, h) "v_xor_b32 %[t0], %[tag], " a "\n\tv_xor_b32 %[t1], %[tag], " b "\n\tv_xor_b32 %[t2], %[tag], " c "\n\tv_or3_b32 %[t0], %[t0], %[t1], %[t2]\n\t" \
  "v_xor_b32 %[t1], %[tag], " d "\n\tv_xor_b32 %[t2], %[tag], " e "\n\tv_or3_b32 %[t0], %[t0], %[t1], %[t2]\n\t" \
  "v_xor_b32 %[t1], %[tag], " f "\n\tv_xor_b32 %[t2], %[tag], " g "\n\tv_or3_b32 %[t0], %[t0], %[t1], %[t2]\n\t" \
  "v_xor_b32 %[t1], %[tag], " h "\n\tv_or_b32 %[t0], %[t0], %[t1]\n\tv_cmp_ne_u32 vcc, 0, %[t0]\n\t"
template <int GAP> __device__ __forceinline__ void poll4(float (&x)[4], float (&y)[4], const unsigned char* base, unsigned off, unsigned tag) {
  unsigned t0, t1, t2, cnt = SPIN_CAP;
  asm volatile(PL4A "s_sleep %[gap]\n"
               "1:\n\t" PL4B "s_waitcnt vmcnt(4)\n\t" PCHK("v97", "v99", "v101", "v103", "v105", "v107", "v109", "v111") "s_cbranch_vccz 2f\n\t"
               PL4A "s_waitcnt vmcnt(4)\n\t" PCHK("v113", "v115", "v117", "v119", "v121", "v123", "v125", "v127") "s_cbranch_vccz 3f\n\t"
               "s_sub_u32 %[cnt], %[cnt], 1\n\ts_cmp_lg_u32 %[cnt], 0\n\ts_cbranch_scc1 1b\n"
               "2:\n\tv_mov_b32 %[x0], v96\n\tv_mov_b32 %[y0], v98\n\tv_mov_b32 %[x1], v100\n\tv_mov_b32 %[y1], v102\n\tv_mov_b32 %[x2], v104\n\tv_mov_b32 %[y2], v106\n\tv_mov_b32 %[x3], v108\n\tv_mov_b32 %[y3], v110\n\ts_branch 4f\n"
               "3:\n\tv_mov_b32 %[x0], v112\n\tv_mov_b32 %[y0], v114\n\tv_mov_b32 %[x1], v116\n\tv_mov_b32 %[y1], v118\n\tv_mov_b32 %[x2], v120\n\tv_mov_b32 %[y2], v122\n\tv_mov_b32 %[x3], v124\n\tv_mov_b32 %[y3], v126\n"
               "4:"
               : [x0] "=&v"(x[0]), [y0] "=&v"(y[0]), [x1] "=&v"(x[1]), [y1] "=&v"(y[1]), [x2] "=&v"(x[2]), [y2] "=&v"(y[2]), [x3] "=&v"(x[3]), [y3] "=&v"(y[3]),
                 [t0] "=&v"(t0), [t1] "=&v"(t1), [t2] "=&v"(t2), [cnt] "+&s"(cnt)
               : [off] "v"(off), [base] "s"(base), [tag] "s"(tag), [gap] "n"(GAP)
               : "memory", "vcc", "scc", "v96", "v97", "v98", "v99", "v100", "v101", "v102", "v103", "v104", "v105", "v106", "v107", "v108", "v109", "v110", "v111",
                 "v112", "v113", "v114", "v115", "v116", "v117", "v118", "v119", "v120", "v121", "v122", "v123", "v124", "v125", "v126", "v127");
}
#define PL5(a, b, c, d, e) PL4(a, b, c, d) "global_load_dwordx4 " e ", %[off5], %[base] sc1\n\t"
#define PL5A PL5("v[88:91]", "v[92:95]", "v[96:99]", "v[100:103]", "v[104:107]")
#define PL5B PL5("v[108:111]", "v[112:115]", "v[116:119]", "v[120:123]", "v[124:127]")
#define PCHK5(a, b, c, d, e, f, g, h, i, j) "v_xor_b32 %[t0], %[tag], " a "\n\tv_xor_b32 %[t1], %[tag], " b "\n\tv_xor_b32 %[t2], %[tag], " c "\n\tv_or3_b32 %[t0], %[t0], %[t1], %[t2]\n\t" \
  "v_xor_b32 %[t1], %[tag], " d "\n\tv_xor_b32 %[t2], %[tag], " e "\n\tv_or3_b32 %[t0], %[t0], %[t1], %[t2]\n\t" \
  "v_xor_b32 %[t1], %[tag], " f "\n\tv_xor_b32 %[t2], %[tag], " g "\n\tv_or3_b32 %[t0], %[t0], %[t1], %[t2]\n\t" \
  "v_xor_b32 %[t1], %[tag], " h "\n\tv_xor_b32 %[t2], %[tag], " i "\n\tv_or3_b32 %[t0], %[t0], %[t1], %[t2]\n\t" \
  "v_xor_b32 %[t1], %[tag], " j "\n\tv_or_b32 %[t0], %[t0], %[t1]\n\tv_cmp_ne_u32 vcc, 0, %[t0]\n\t"
template <int GAP> __device__ __forceinline__ void poll5(float (&x)[4], float (&y)[4], float& m, float& l, const unsigned char* base, unsigned off, unsigned off5, unsigned tag) {
  unsigned t0, t1, t2, cnt = SPIN_CAP;
  asm volatile(PL5A "s_sleep %[gap]\n"
               "1:\n\t" PL5B "s_waitcnt vmcnt(5)\n\t" PCHK5("v89", "v91", "v93", "v95", "v97", "v99", "v101", "v103", "v105", "v107") "s_cbranch_vccz 2f\n\t"
               PL5A "s_waitcnt vmcnt(5)\n\t" PCHK5("v109", "v111", "v113", "v115", "v117", "v119", "v121", "v123", "v125", "v127") "s_cbranch_vccz 3f\n\t"
               "s_sub_u32 %[cnt], %[cnt], 1\n\ts_cmp_lg_u32 %[cnt], 0\n\ts_cbranch_scc1 1b\n"
               "2:\n\tv_mov_b32 %[m], v104\n\tv_mov_b32 %[l], v106\n\tv_mov_b32 %[x0], v88\n\tv_mov_b32 %[y0], v90\n\tv_mov_b32 %[x1], v92\n\tv_mov_b32 %[y1], v94\n\tv_mov_b32 %[x2], v96\n\tv_mov_b32 %[y2], v98\n\tv_mov_b32 %[x3], v100\n\tv_mov_b32 %[y3], v102\n\ts_branch 4f\n"
               "3:\n\tv_mov_b32 %[m], v124\n\tv_mov_b32 %[l], v126\n\tv_mov_b32 %[x0], v108\n\tv_mov_b32 %[y0], v110\n\tv_mov_b32 %[x1], v112\n\tv_mov_b32 %[y1], v114\n\tv_mov_b32 %[x2], v116\n\tv_mov_b32 %[y2], v118\n\tv_mov_b32 %[x3], v120\n\tv_mov_b32 %[y3], v122\n"
               "4:"
               : [x0] "=&v"(x[0]), [y0] "=&v"(y[0]), [x1] "=&v"(x[1]), [y1] "=&v"(y[1]), [x2] "=&v"(x[2]), [y2] "=&v"(y[2]), [x3] "=&v"(x[3]), [y3] "=&v"(y[3]), [m] "=&v"(m), [l] "=&v"(l),
                 [t0] "=&v"(t0), [t1] "=&v"(t1), [t2] "=&v"(t2), [cnt] "+&s"(cnt)
               : [off] "v"(off), [off5] "v"(off5), [base] "s"(base), [tag] "s"(tag), [gap] "n"(GAP)
               : "memory", "vcc", "scc", "v88", "v89", "v90", "v91", "v92", "v93", "v94", "v95", "v96", "v97", "v98", "v99", "v100", "v101", "v102", "v103", "v104", "v105", "v106", "v107", "v108", "v109", "v110", "v111",
                 "v112", "v113", "v114", "v115", "v116", "v117", "v118", "v119", "v120", "v121", "v122", "v123", "v124", "v125", "v126", "v127");
}
template <int CTRL> __device__ __forceinline__ float dppx(float x) { return __builtin_bit_cast(float, __builtin_amdgcn_update_dpp(0, __builtin_bit_cast(int, x), CTRL, 0xf, 0xf, true)); }
__device__ __forceinline__ bool none(unsigned bad) { return __builtin_amdgcn_ballot_w64(bad != 0u) == 0ull; }

__global__ void __launch_bounds__(NTHR, 4) fused_decode(Params p) {
  __shared__ __attribute__((aligned(16))) unsigned char lds_raw[L_END + LDS_EXTRA];
  LAS unsigned char* L = (LAS unsigned char*)lds_raw;
  unsigned long long pc0, tt0;
  asm volatile("s_memrealtime %0" : "=s"(tt0));
  asm volatile("s_getpc_b64 %0" : "=s"(pc0));
  const int tid = threadIdx.x, lane = tid & 63, wave = __builtin_amdgcn_readfirstlane(tid >> 6);
  const int b = blockIdx.x, g = b & 15, s = b >> 4, br = g >> 3, kvh = g & 7;
  const int go = g ^ XG, kvo = kvh ^ XG;
  const int lr = lane >> 4, lc = lane & 15;
  const size_t kvoff = ((size_t)kvh * SK + (SK - WIN) + s * 256) * DH;
  const u16 *px = p.x, *pgw = p.gw, *pwq = p.wq[br], *pk = p.k[br], *pv = p.v[br], *pwo = p.wo[br];
  u16* pout = p.out; unsigned char* pws = p.ws;
  asm volatile("s_waitcnt lgkmcnt(0)" : "+s"(px), "+s"(pgw), "+s"(pwq), "+s"(pk), "+s"(pv), "+s"(pwo), "+s"(pout), "+s"(pws), "+s"(tt0));
  const unsigned t0 = (unsigned)tt0;

  if (wave >= NCW && wave < NCW + NLW) {
    const int j = wave - NCW;
    if (j == 0) {
      *(LAS unsigned*)(L + L_CNT + 4 * lane) = (L_CNT + 4 * lane == C_TG || L_CNT + 4 * lane == C_TQ) ? t0 : 0u;
      if (lane < 32) { *(LAS unsigned*)(L + L_MX + 4 * lane) = EMPTY; *(LAS unsigned*)(L + L_O + 4 * lane) = lane < 16 ? EMPTY : 0u; }
    }
    const unsigned char* kb = (const unsigned char*)(pk + kvoff) + j * 1024;
    const unsigned char* vb = (const unsigned char*)(pv + kvoff) + j * 1024;
    const GAS v4u* wb = (const GAS v4u*)(pwo + ((size_t)kvo * 512 + s * 32 + j * 8) * DM) + lane;
    const unsigned ko = lr * 256 + ((lc ^ (4 * j + lr)) << 4);
    const unsigned vo = lr * 256 + ((lc ^ ((lr | ((j & 2) << 1)) << 1)) << 4);
    const unsigned ldum = L_DUM + 8 * lane;
    BARRIER();
    if (KDELAY) __builtin_amdgcn_s_sleep(KDELAY);
    const unsigned rot = RA * g + RB * j + RC * s + ((RD * g) >> 1);
    const unsigned rw = ROTW ? rot : 0;
    dma_run_rot<KWND, DK>(kb, ko, L_K + j * 1024, 0, 16, rot, C_TG, TK0);
    dma_run_rot<VWND, DV>(vb, vo, L_V + j * 1024, 0, VWND - 1, rot, C_TG, TV0);
    asm volatile("s_waitcnt vmcnt(%0)" :: "n"(VWND - 1) : "memory");
    *(volatile LAS unsigned*)(L + (lane == 0 ? C_KL + 4 * j : ldum)) = 1u;
    STAMPX(1);
    dma_run_rot<VWND, DV>(vb, vo, L_V + j * 1024, VWND - 1, 8, rot, C_TG, TV0 + (VWND - 1) * DV);
    asm volatile("s_waitcnt vmcnt(%0)" :: "n"(VWND - 1) : "memory");
    *(volatile LAS unsigned*)(L + (lane == 0 ? C_VH + 4 * j : ldum)) = 1u;
    dma_run_rot<VWND, DV>(vb, vo, L_V + j * 1024, VWND + 7, 9 - VWND, rot, C_TG, TV0 + (VWND + 7) * DV);
    v4u w[8];
    if (WOA) {
      if (WOGATE) wait_until(C_TG, WOGATE);
#pragma unroll
      for (int i = 0; i < WOA; ++i) {
        if (WW) asm volatile("s_waitcnt vmcnt(%0)" :: "n"(WW ? WW - 1 : 0) : "memory");
        w[i] = __builtin_nontemporal_load(wb + ((i + rw) & 7) * 64);
        if (WW) RELOAD();
      }
      RELOAD();
    }
    asm volatile("s_waitcnt vmcnt(%0)" :: "n"(WOA) : "memory");
    STAMPX(8);
    BARRIER();
    if (WOM) {
#pragma unroll
      for (int i = WOA; i < WOA + WOM; ++i) w[i] = __builtin_nontemporal_load(wb + ((i + rw) & 7) * 64);
      RELOAD();
    }
    if (WOA + WOM < 8) {
#pragma clang loop unroll(disable)
      for (unsigned i = 0; i < SPIN_CAP; ++i) if (none(*(volatile LAS unsigned*)(L + C_OP + 4 * (lane & 7)) == 0u)) break;
      RELOAD();
#pragma unroll
      for (int i = WOA + WOM; i < 8; ++i) w[i] = __builtin_nontemporal_load(wb + ((i + rw) & 7) * 64);
      RELOAD();
    } else if (WODELAY) __builtin_amdgcn_s_sleep(WODELAY);
    STAMPX(4);
#pragma unroll
    for (int i = 0; i < 8; ++i) {
      { const unsigned r = (i + rw) & 7; *(LAS v4u*)(L + L_WO + (8 * j + r) * 1024 + ((lane ^ (2 * r)) << 4)) = w[i]; }
      if (i == WOTRIG - 1) { RELOAD(); *(volatile LAS unsigned*)(L + (lane == 0 ? C_WT + 4 * j : ldum)) = 1u; RELOAD(); }

    }
    *(volatile LAS unsigned*)(L + (lane == 0 ? C_WO + 4 * j : ldum)) = 1u;
    STAMPX(7);
    return;
  }

  const __amdgpu_buffer_rsrc_t wsr = __builtin_amdgcn_make_buffer_rsrc(pws, 0, W_END, 0x00020000);
  if (wave == NCW + NLW) {
    unsigned c0 = 0, c1 = 0;
    if (CODEPF && b < CODEPF) {
      const GAS unsigned char* cp = (const GAS unsigned char*)(pc0 & ~127ull) + lane * 128;
      c0 = *(const GAS unsigned*)cp; if (lane < CODEPF_TAIL) c1 = *(const GAS unsigned*)(cp + 8192);
    }
    asm volatile("s_barrier" ::: "memory");
    unsigned ep;
    asm volatile("s_load_dword %0, %1, 0x0\n\ts_waitcnt lgkmcnt(0)" : "=s"(ep) : "s"(pws + W_E) : "memory");
    const unsigned tag = ep + 1u;
    {
    __builtin_amdgcn_raw_buffer_store_b64(v2u{t0, tag}, wsr, (b < 8 && lane == 0) ? W_T + 8 * b : W_END, 0, 16);
    asm volatile("" :: "v"(c0), "v"(c1));
    __builtin_amdgcn_s_sleep(TSLEEP);
    v2u ts;
#pragma clang loop unroll(disable)
    for (unsigned i = 0; i < SPIN_CAP; ++i) { RELOAD(); ts = __builtin_amdgcn_raw_buffer_load_b64(wsr, W_T + 8 * (lane & 7), 0, 16); if (none(ts[1] ^ tag)) break; }
    int d = (int)(ts[0] - t0);
    const unsigned tq = t0 + (unsigned)__builtin_amdgcn_readlane(d, kvo);
    unsigned tg;
    if (TGMEAN) {
      int dm = d;
      dm = max(dm, __builtin_amdgcn_mov_dpp(dm, 0xB1, 0xf, 0xf, true)); dm = max(dm, __builtin_amdgcn_mov_dpp(dm, 0x4E, 0xf, 0xf, true)); dm = max(dm, __builtin_amdgcn_mov_dpp(dm, 0x124, 0xf, 0xf, true));
      d += __builtin_amdgcn_mov_dpp(d, 0xB1, 0xf, 0xf, true); d += __builtin_amdgcn_mov_dpp(d, 0x4E, 0xf, 0xf, true); d += __builtin_amdgcn_mov_dpp(d, 0x124, 0xf, 0xf, true);
      tg = t0 + (unsigned)(((4 - TGMAX) * (__builtin_amdgcn_readfirstlane(d) >> 3) + TGMAX * __builtin_amdgcn_readfirstlane(dm)) >> 2);
    } else {
      d = min(d, __builtin_amdgcn_mov_dpp(d, 0xB1, 0xf, 0xf, true)); d = min(d, __builtin_amdgcn_mov_dpp(d, 0x4E, 0xf, 0xf, true)); d = min(d, __builtin_amdgcn_mov_dpp(d, 0x124, 0xf, 0xf, true));
      tg = t0 + (unsigned)min(__builtin_amdgcn_readfirstlane(d), 0);
    }
    *(volatile LAS unsigned*)(L + (lane == 0 ? C_TG : lane == 1 ? C_TQ : L_DUM + 8 * lane)) = lane == 1 ? tq : tg;
    }
    unsigned moff = W_ML + ((go * 16 + ((lc & 12) | ((lc + lr) & 3))) * 4 + (s >> 2)) * 16;
    unsigned oaddr = lane < 16 ? L_O + 16 * ((lane >> 1) & 3) + 8 * (lane >> 3) + 4 * (lane & 1) : L_DUM + 8 * lane;
    unsigned ooff = W_O + (go * 16 + s) * 4096 + lane * 16;
    asm volatile("" : "+v"(moff), "+v"(oaddr), "+v"(ooff));
    BARRIER();
    __builtin_amdgcn_s_setprio(3);
    float x[4], y[4], m, l;
    if (WOTRIG) {
#pragma clang loop unroll(disable)
      for (unsigned i = 0; i < SPIN_CAP; ++i) if (none(*(volatile LAS unsigned*)(L + C_WT + 4 * (lane & 3)) == 0u)) break;
      RELOAD();
    } else if (TUNE1) __builtin_amdgcn_s_sleep(TUNE1);
    poll5<GAP1>(x, y, m, l, pws, ooff, moff, tag);
    float M = m;
    M = fmaxf(M, dppx<0x128>(M)); M = fmaxf(M, dppx<0x124>(M)); M = fmaxf(M, dppx<0x122>(M)); M = fmaxf(M, dppx<0x121>(M));
    const float e = __builtin_amdgcn_exp2f(m - M);
    float Ls = e * l;
    Ls += dppx<0x128>(Ls); Ls += dppx<0x124>(Ls); Ls += dppx<0x122>(Ls); Ls += dppx<0x121>(Ls);
    const float wn = e * __builtin_amdgcn_rcpf(Ls);
    const float wgt[4] = {dppx<0x150>(wn), dppx<0x154>(wn), dppx<0x158>(wn), dppx<0x15C>(wn)};
    float ov = rs32(fmaf(wgt[0], x[0], wgt[1] * x[1]) + fmaf(wgt[2], x[2], wgt[3] * x[3]), fmaf(wgt[0], y[0], wgt[1] * y[1]) + fmaf(wgt[2], y[2], wgt[3] * y[3]));
    ov = rs16(ov, ov);
    const auto sw = __builtin_amdgcn_permlane32_swap(f2u(ov), f2u(ov), false, false);
    *(LAS unsigned*)(L + oaddr) = cvtpk(u2f(sw[0]), u2f(sw[1]));
    STAMP(10);
    return;
  }

  const int cw = wave;
  __builtin_amdgcn_s_setprio(2);
  STAMP0(0);
  unsigned tag;
  float gate = 0.f;
  const int q4 = lc >> 2, p4 = lane & 3;
  const bool qb0 = (lane & 4) != 0, qb1 = (lane & 8) != 0;
  const unsigned dum = L_DUM + 8 * lane;
  {
    const int kq = s >> 2, hq = s & 3, row0 = 128 * kq + 16 * cw;
    const GAS v4u* wp = (const GAS v4u*)(pwq + (size_t)(row0 + lr) * NQ + kvo * 512 + 128 * hq + 8 * lc);
    v4u w[4];
#pragma unroll
    for (int i = 0; i < 4; ++i) w[i] = __builtin_nontemporal_load(wp + (size_t)i * (NQ / 2));
    typedef unsigned s8u __attribute__((ext_vector_type(8)));
    s8u xs;
    asm volatile("s_load_dwordx8 %0, %1, 0x0" : "=s"(xs) : "s"(px + row0) : "memory");
    const bool gw = wave == 0 && b < 32;
    const v4u gx = *(gw ? (const GAS v4u*)(px + 8 * lane) : wp), gg = *(gw ? (const GAS v4u*)(pgw + 8 * lane) : wp);
    RELOAD();
    unsigned tlb = 0;
    if (TLBPF) {
      const unsigned char *a0 = (const unsigned char*)(pk + kvoff), *a1 = (const unsigned char*)(pv + kvoff), *a2 = (const unsigned char*)(pwo + ((size_t)kvo * 512 + s * 32) * DM), *a3 = pws + W_F + b * 4096;
      asm volatile("" : "+s"(a0), "+s"(a1), "+s"(a2), "+s"(a3));
      const unsigned char* a = (wave & 2) ? ((wave & 1) ? a3 : a2) : ((wave & 1) ? a1 : a0);
      asm volatile("s_load_dword %0, %1, 0x0" : "=s"(tlb) : "s"(a) : "memory");
    }
    asm volatile("s_barrier" ::: "memory");
    unsigned ep;
    asm volatile("s_load_dword %0, %1, 0x0" : "=s"(ep) : "s"(pws + W_E) : "memory");
    asm volatile("s_waitcnt lgkmcnt(0)" : "+s"(xs), "+s"(ep), "+s"(tlb), "+v"(w[0]), "+v"(w[1]) :: "memory");
    tag = ep + 1u;
    const unsigned xd[4] = {lr & 2 ? xs[1] : xs[0], lr & 2 ? xs[3] : xs[2], lr & 2 ? xs[5] : xs[4], lr & 2 ? xs[7] : xs[6]};
    const unsigned xsel = lr & 1 ? 0x07060302u : 0x05040100u;
    float acc[8];
#pragma unroll
    for (int k = 0; k < 8; ++k) acc[k] = 0.f;
#pragma unroll
    for (int i = 0; i < 4; i += 2) {
      const unsigned xp = __builtin_amdgcn_perm(xd[i + 1], xd[i], xsel);
#pragma unroll
      for (int d = 0; d < 4; ++d) {
        acc[2 * d] = dot2(__builtin_amdgcn_perm(w[i + 1][d], w[i][d], 0x05040100u), xp, acc[2 * d]);
        acc[2 * d + 1] = dot2(__builtin_amdgcn_perm(w[i + 1][d], w[i][d], 0x07060302u), xp, acc[2 * d + 1]);
      }
    }
    const float r0 = rs32(acc[0], acc[4]), r1 = rs32(acc[1], acc[5]), r2 = rs32(acc[2], acc[6]), r3 = rs32(acc[3], acc[7]);
    const float u0 = rs16(r0, r2), u1 = rs16(r1, r3);
    *(LAS f32x2*)(L + L_QP + (cw * 128 + 8 * lc + 4 * (lane >> 5) + 2 * (lr & 1)) * 4) = f32x2{u0, u1};
    if (__builtin_expect(lds_add(C_QP) == NCW - 1, 1)) {
      f32x2 qs = {0.f, 0.f};
#pragma unroll
      for (int i = 0; i < 8; i += 2) qs += *(const LAS f32x2*)(L + L_QP + (i * 128 + 2 * lane) * 4) + *(const LAS f32x2*)(L + L_QP + ((i + 1) * 128 + 2 * lane) * 4);
      __builtin_amdgcn_raw_buffer_store_b128(v4u{f2u(qs[0]), tag, f2u(qs[1]), tag}, wsr, W_Q + (go * 4 + kq) * 4096 + (128 * hq + 2 * lane) * 8, 0, 16);
      STAMP(2);
    }
    v4u gx_ = gx, gg_ = gg;
    asm volatile("" : "+v"(gx_), "+v"(gg_));
    if (__builtin_expect(gw, 1)) {
      float z = 0.f;
#pragma unroll
      for (int d = 0; d < 4; ++d) z += rbf(bflo(gx_[d]) * bflo(gg_[d])) + rbf(bfhi(gx_[d]) * bfhi(gg_[d]));
      z += dpp<0xB1>(z); z += dpp<0x4E>(z); z += dpp<0x124>(z); z += dpp<0x128>(z); z = xrow_sum(z);
      gate = rbf(__builtin_amdgcn_rcpf(1.f + __expf(-rbf(z))));
    }
  }
  {
    const int off = W_Q + g * 16384 + (lane >> 5) * 4096 + (64 * cw + 2 * (lane & 31)) * 8;
    v4u a, a2;
    unsigned pia = lane == 0 ? C_PI + 4 * cw : dum;
    unsigned pca = lane == 0 ? C_PC : dum;
    asm volatile("" : "+v"(pia), "+v"(pca));
    if (TQ) wait_until(C_TQ, TQ); else if (TUNEQ) __builtin_amdgcn_s_sleep(TUNEQ);
#pragma clang loop unroll(disable)
    for (unsigned i = 0; i < SPIN_CAP; ++i) {
      RELOAD(); a = ld_sc1(wsr, off); a2 = ld_sc1(wsr, off + 8192);
      if (GATE1 | GATE2) { RELOAD(); *(volatile LAS unsigned*)(L + pia) = i + 1; RELOAD(); }
      if (KOPEN) { asm volatile("ds_add_u32 %0, %1" :: "v"(pca), "v"(1u) : "memory"); pca = dum; }
      if (none(tag_bad(a, tag) | tag_bad(a2, tag))) break;
    }
    if (GATE1 | GATE2) *(volatile LAS unsigned*)(L + pia) = SPIN_CAP;
    const float qv = rs32(u2f(a[0]) + u2f(a2[0]), u2f(a[2]) + u2f(a2[2]));
    *(LAS u16*)(L + L_Q + (cw >> 1) * Q_STR + ((cw & 1) * 64 + 2 * (lane & 31) + (lane >> 5)) * 2) = (u16)cvtpk(qv, qv);
    *(volatile LAS unsigned*)(L + (lane == 0 ? C_FLAG + 4 * cw : dum)) = 1u;
    STAMPX(3);
  }
  unsigned ka = L_K + (32 * cw + lc) * 256 + ((lr ^ lc) << 4), qa = L_Q + p4 * Q_STR + 16 * lr, pa = L_P + p4 * P_STR + 16 * lr;
  unsigned pw = (lane & 12) == 0 ? L_P + p4 * P_STR + (32 * cw + 4 * lr) * 2 : dum, pw2 = (lane & 12) == 0 ? pw + 32 : dum;
  unsigned va = L_V + (8 * lr + q4) * 256 + (((2 * cw + (p4 >> 1)) ^ ((q4 | ((lr & 1) << 2)) << 1)) << 4) + 8 * (p4 & 1);
  unsigned wa = L_WO + (4 * lr + q4) * 1024 + (((8 * cw + (p4 >> 1)) ^ ((q4 | ((lr & 1) << 2)) << 1)) << 4) + 8 * (p4 & 1);
  unsigned oo = W_O + ((g * 16 + 4 * p4 + (cw >> 1)) * 16 + s) * 256 + (16 * (cw & 1) + 4 * lr + q4) * 8;
  unsigned fo = W_F + ((4 * cw + p4) * 256 + go * 16 + s) * 128 + (4 * lr + q4) * 8;
  unsigned opa = lane == 0 ? C_OP + 4 * cw : dum;
  asm volatile("" : "+v"(opa));
  unsigned ba0 = p4 == 0 ? L_O + 16 * lr : L_Z, ba1 = p4 == 1 ? L_O + 16 * lr : L_Z, ba2 = p4 == 2 ? L_O + 16 * lr : L_Z, ba3 = p4 == 3 ? L_O + 16 * lr : L_Z;
  asm volatile("" : "+v"(ka), "+v"(qa), "+v"(pw), "+v"(pw2), "+v"(pa), "+v"(va), "+v"(wa), "+v"(oo), "+v"(fo), "+v"(ba0), "+v"(ba1), "+v"(ba2), "+v"(ba3));
#pragma clang loop unroll(disable)
  for (unsigned i = 0; i < SPIN_CAP; ++i) if (none(*(volatile LAS unsigned*)(L + C_FLAG + 4 * (lane < 12 ? lane : 0)) == 0u)) break;
  RELOAD();
  STAMP0(5);
  float mc;
  {
    v4u qf[4], kf[2][4];
#pragma unroll
    for (int ks = 0; ks < 4; ++ks) { kf[0][ks] = *(const LAS v4u*)(L + (ka ^ (64 * ks))); qf[ks] = *(const LAS v4u*)(L + qa + 64 * ks); kf[1][ks] = *(const LAS v4u*)(L + (ka ^ (64 * ks)) + 4096); }
    __builtin_amdgcn_sched_barrier(0);
    f32x4 sc[2] = {{0.f, 0.f, 0.f, 0.f}, {0.f, 0.f, 0.f, 0.f}};
#pragma unroll
    for (int ks = 0; ks < 4; ++ks) { sc[0] = mfma(kf[0][ks], qf[ks], sc[0]); sc[1] = mfma(kf[1][ks], qf[ks], sc[1]); }
    float m = fmaxf(fmaxf(fmaxf(sc[0][0], sc[0][1]), fmaxf(sc[0][2], sc[0][3])), fmaxf(fmaxf(sc[1][0], sc[1][1]), fmaxf(sc[1][2], sc[1][3])));
    m = xrow_max(m);
    *(volatile LAS float*)(L + (lane < 4 ? L_MX + (lane * 8 + cw) * 4 : dum)) = m;
    constexpr float C2 = 0.08838834764831845f * 1.4426950408889634f;
    v4u mx0, mx1;
#pragma clang loop unroll(disable)
    for (unsigned i = 0; i < SPIN_CAP; ++i) {
      mx0 = *(volatile LAS v4u*)(L + L_MX + 32 * p4); mx1 = *(volatile LAS v4u*)(L + L_MX + 32 * p4 + 16);
      const unsigned top = max(max(max(mx0[0], mx0[1]), max(mx0[2], mx0[3])), max(max(mx1[0], mx1[1]), max(mx1[2], mx1[3])));
      if (none(top == EMPTY)) break;
    }
    mc = fmaxf(fmaxf(fmaxf(u2f(mx0[0]), u2f(mx0[1])), fmaxf(u2f(mx0[2]), u2f(mx0[3]))), fmaxf(fmaxf(u2f(mx1[0]), u2f(mx1[1])), fmaxf(u2f(mx1[2]), u2f(mx1[3])))) * C2;
    float ls = 0.f;
    v2u pk[2];
#pragma unroll
    for (int t = 0; t < 2; ++t) {
      float e[4];
#pragma unroll
      for (int r = 0; r < 4; ++r) { e[r] = __builtin_amdgcn_exp2f(fmaf(sc[t][r], C2, -mc)); ls += e[r]; }
      pk[t] = v2u{cvtpk(e[0], e[1]), cvtpk(e[2], e[3])};
    }
    ls = xrow_sum(ls);
    *(LAS v2u*)(L + pw) = pk[0]; *(LAS v2u*)(L + pw2) = pk[1];
    *(LAS float*)(L + (lane < 4 ? L_LS + (lane * 8 + cw) * 4 : dum)) = ls;
    *(volatile LAS unsigned*)(L + (lane == 0 ? C_PF + 4 * cw : dum)) = 1u;
    STAMPX(6);
  }
  f32x4 o = {0.f, 0.f, 0.f, 0.f};
  v4u pf[8];
  {
#pragma clang loop unroll(disable)
    for (unsigned i = 0; i < SPIN_CAP; ++i) if (none(*(volatile LAS unsigned*)(L + C_PF + 4 * (lane < 12 ? lane : 0)) == 0u)) break;
    RELOAD();
    v4u vf[4];
#pragma unroll
    for (int ks = 0; ks < 4; ++ks) { vf[ks] = tr_pair(L + va + 8192 * ks, 1024); pf[ks] = *(const LAS v4u*)(L + pa + 64 * ks); }
#pragma unroll
    for (int ks = 4; ks < 8; ++ks) pf[ks] = *(const LAS v4u*)(L + pa + 64 * ks);
    if (__builtin_expect(cw == NCW - 1, 1)) {
      const f32x4 l0 = *(const LAS f32x4*)(L + L_LS + 32 * p4), l1 = *(const LAS f32x4*)(L + L_LS + 32 * p4 + 16);
      const v4u ml = {f2u(mc), tag, f2u(((l0[0] + l0[1]) + (l0[2] + l0[3])) + ((l1[0] + l1[1]) + (l1[2] + l1[3]))), tag};
      __builtin_amdgcn_raw_buffer_store_b128(ml, wsr, lane < 4 ? W_ML + ((g * 16 + s) * 4 + lane) * 16 : W_END, 0, 16);
    }
#pragma unroll
    for (int ks = 0; ks < 4; ++ks) o = mfma(vf[ks], pf[ks], o);
    asm volatile("" : "+v"(o), "+v"(pf[4]), "+v"(pf[5]), "+v"(pf[6]), "+v"(pf[7]));
  }
  BARRIER();
  {
    v4u vf[4];
#pragma unroll
    for (int ks = 0; ks < 4; ++ks) vf[ks] = tr_pair(L + va + 8192 * (ks + 4), 1024);
    __builtin_amdgcn_sched_barrier(0);
#pragma unroll
    for (int ks = 0; ks < 4; ++ks) o = mfma(vf[ks], pf[ks + 4], o);
    st_granule(pws, oo, pick4(o, qb0, qb1), tag);
    if (WOA + WOM < 8) { RELOAD(); *(volatile LAS unsigned*)(L + opa) = 1u; }
    STAMPX(9);
  }
  {
#pragma clang loop unroll(disable)
    for (unsigned i = 0; i < SPIN_CAP; ++i) if (none(*(volatile LAS unsigned*)(L + C_WO + 4 * (lane & 3)) == 0u)) break;
    RELOAD();
    v4u wf[4];
#pragma unroll
    for (int t = 0; t < 4; ++t) wf[t] = tr_pair(L + (wa ^ (32 * t)), 16384);
    asm volatile("" : "+v"(wf[0]), "+v"(wf[1]), "+v"(wf[2]), "+v"(wf[3]));
    v4u b0, b1, b2, b3;
#pragma clang loop unroll(disable)
    for (unsigned i = 0; i < SPIN_CAP; ++i) {
      b0 = *(volatile LAS v4u*)(L + ba0); b1 = *(volatile LAS v4u*)(L + ba1); b2 = *(volatile LAS v4u*)(L + ba2); b3 = *(volatile LAS v4u*)(L + ba3);
      if (none(((b0[0] | b1[0]) | (b2[0] | b3[0])) == EMPTY)) break;
    }
    f32x4 r = {0.f, 0.f, 0.f, 0.f};
    r = mfma(wf[0], b0, r); r = mfma(wf[1], b1, r); r = mfma(wf[2], b2, r); r = mfma(wf[3], b3, r);
    st_granule(pws, fo, pick4(r, qb0, qb1), tag);
    STAMPX(12);
  }
  if (__builtin_expect(b < 32, 1)) {
    unsigned ra_ = (lane & 24) == 0 ? L_RED + ((2 * (lane & 7) + (lane >> 5)) * 8 + cw) * 4 : dum;
    float g1 = rbf(1.f - gate);
    asm volatile("" : "+v"(ra_), "+v"(g1));
    if (TUNE2) __builtin_amdgcn_s_sleep(TUNE2);
    float x[4], y[4];
    poll4<GAP2>(x, y, pws, W_F + b * 32768 + cw * 4096 + lane * 16, tag);
    float t = rs32((x[0] + x[1]) + (x[2] + x[3]), (y[0] + y[1]) + (y[2] + y[3]));
    t = rs16(t, t); t += dpp<0x128>(t);
    *(LAS float*)(L + ra_) = t;
    STAMPX(13);
    BARRIER();
    if (__builtin_expect(cw == 0, 1)) {
      const f32x4 ra = *(const LAS f32x4*)(L + L_RED + 32 * lc), rb = *(const LAS f32x4*)(L + L_RED + 32 * lc + 16);
      const float A = rbf((ra[0] + ra[1]) + (ra[2] + ra[3])), B = rbf((rb[0] + rb[1]) + (rb[2] + rb[3]));
      const u16 res = (u16)cvtpk(rbf(gate * A) + rbf(g1 * B), 0.f);
      __builtin_amdgcn_raw_buffer_store_b16(res, __builtin_amdgcn_make_buffer_rsrc(pout, 0, 2 * DM, 0x00020000), lane < 16 ? (16 * b + lane) * 2 : 2 * DM, 0, 0);
      __builtin_amdgcn_raw_buffer_store_b32(tag, wsr, (b | lane) == 0 ? W_E : W_END, 0, 16);
      STAMP(14);
    }
  }
}

extern "C" void kernel_launch(void* const* d_in, const int* in_sizes, int n_in, void* d_out, int out_size, void* d_ws, size_t ws_size, hipStream_t stream) {
  Params p;
  p.x = (const u16*)d_in[0]; p.gw = (const u16*)d_in[10];
  p.wq[0] = (const u16*)d_in[3]; p.wq[1] = (const u16*)d_in[8];
  p.k[0] = (const u16*)d_in[1]; p.k[1] = (const u16*)d_in[6];
  p.v[0] = (const u16*)d_in[2]; p.v[1] = (const u16*)d_in[7];
  p.wo[0] = (const u16*)d_in[4]; p.wo[1] = (const u16*)d_in[9];
  p.out = (u16*)d_out; p.ws = (unsigned char*)d_ws;
  fused_decode<<<NWG, NTHR, 0, stream>>>(p);
}
```

```cpp
#include <hip/hip_runtime.h>
#include <hip/hip_bf16.h>

typedef unsigned short u16;
typedef unsigned v4u __attribute__((ext_vector_type(4)));
typedef unsigned v2u __attribute__((ext_vector_type(2)));
typedef float f32x2 __attribute__((ext_vector_type(2)));
typedef float f32x4 __attribute__((ext_vector_type(4)));
typedef short s16x4 __attribute__((ext_vector_type(4)));
typedef __bf16 bf16x2 __attribute__((ext_vector_type(2)));
typedef __bf16 bf16x8 __attribute__((ext_vector_type(8)));
#define GAS __attribute__((address_space(1)))
#define LAS __attribute__((address_space(3)))
#define RLX_AGENT __ATOMIC_RELAXED, __HIP_MEMORY_SCOPE_AGENT

constexpr int DM = 512, SK = 32768, DH = 128, WIN = 4096, NQ = 4096;
constexpr int NWG = 256, NCW = 8, NLW = 4, NTHR = 64 * (NCW + NLW + 1);
constexpr int L_K = 0, L_WO = 0, L_V = 65536, L_P = 131072, P_STR = 576, L_Q = L_P + 4 * P_STR, Q_STR = 320, L_QP = L_Q + 4 * Q_STR,
              L_MX = L_QP + 4096, L_LS = L_MX + 128, L_O = L_LS + 128, L_Z = L_O + 64, L_RED = L_Z + 64, L_CNT = L_RED + 512, L_DUM = L_CNT + 256, L_END = L_DUM + 512;
enum { C_FLAG = L_CNT, C_KL = L_CNT + 32, C_QP = L_CNT + 48, C_PF = L_CNT + 64, C_VH = L_CNT + 96, C_WO = L_CNT + 112, C_PI = L_CNT + 128, C_TG = L_CNT + 160, C_TQ = L_CNT + 164, C_WT = L_CNT + 176, C_OP = L_CNT + 192, C_PC = L_CNT + 224 };
constexpr unsigned EMPTY = 0xFFFFFFFFu;
constexpr unsigned W_E = 0, W_Q = 4096, W_ML = W_Q + 16 * 16384, W_O = W_ML + 16384, W_F = W_O + (1u << 20), W_T = W_F + (1u << 20), W_END = W_T + 4096;
constexpr unsigned SPIN_CAP = 1u << 20;

#define XG 4
#define TLBPF 1
#define KWND 7
#define LWND 63
#define TUNEQ 44
#define TUNE2 12
#define WODELAY 4
#define KDELAY 8
#define CODEPF 8
#define CODEPF_TAIL 16
#define GAP1 8
#define GAP2 8

#define GATE1 0
#define GATE2 0
#define WOGATE 590
#define TSLEEP 40
#define KOPEN 0
#define V2EARLY 1
#define KPACE 1
#define TK0 100
#define DK 15
#define TV0 340
#define DV 15
#define VWND 8
#define TQ 380
#define TGMEAN 1
#define WOM 0
#define WW 7
#define ROT 3
#define ROTJ 2
#define ROTW 1
#define TGMAX 0
#define RA 0
#define RB 2
#define RC 8
#define RD 7
#define NRED 32
#define NSF 4
#define GAPF 5
#define MASKED 1
#define POLL1_(n) pollm_##n
#define POLL1(n) POLL1_(n)
#define FM 0
#define GM 0
#define FIRSTFULL 1
#define PM4_(n) pollf4_##n
#define PM5_(n) pollf5_##n
#define PM4(n) PM4_(n)
#define PM5(n) PM5_(n)
#define TF 940
#define TO 0
#define WOA 6
#define TUNE1 24
#define WOTRIG 0
#define STAMPX(i) do { } while (0)
#define STAMP(i) do { } while (0)
#define LDS_EXTRA 0
#define STAMP0(i) do { if (wave == 0) STAMP(i); } while (0)

struct Params { const u16* x; const u16* gw; const u16* wq[2]; const u16* k[2]; const u16* v[2]; const u16* wo[2]; u16* out; unsigned char* ws; };

__device__ __forceinline__ float bflo(unsigned w) { return __builtin_bit_cast(float, w << 16); }
__device__ __forceinline__ float bfhi(unsigned w) { return __builtin_bit_cast(float, w & 0xffff0000u); }
__device__ __forceinline__ unsigned cvtpk(float lo, float hi) { return __builtin_bit_cast(unsigned, __builtin_convertvector(f32x2{lo, hi}, bf16x2)); }
__device__ __forceinline__ float rbf(float f) { return bflo(cvtpk(f, f)); }
__device__ __forceinline__ float dot2(unsigned a, unsigned b, float acc) { return __builtin_amdgcn_fdot2_f32_bf16(__builtin_bit_cast(bf16x2, a), __builtin_bit_cast(bf16x2, b), acc, false); }
template <int CTRL> __device__ __forceinline__ float dpp(float x) { return __builtin_bit_cast(float, __builtin_amdgcn_mov_dpp(__builtin_bit_cast(int, x), CTRL, 0xf, 0xf, true)); }
__device__ __forceinline__ float u2f(unsigned u) { return __builtin_bit_cast(float, u); }
__device__ __forceinline__ unsigned f2u(float f) { return __builtin_bit_cast(unsigned, f); }
__device__ __forceinline__ float rs32(float a, float b) { auto r = __builtin_amdgcn_permlane32_swap(f2u(a), f2u(b), false, false); return u2f(r[0]) + u2f(r[1]); }
__device__ __forceinline__ float rs16(float a, float b) { auto r = __builtin_amdgcn_permlane16_swap(f2u(a), f2u(b), false, false); return u2f(r[0]) + u2f(r[1]); }
__device__ __forceinline__ float xrow_max(float x) {
  auto s = __builtin_amdgcn_permlane16_swap(f2u(x), f2u(x), false, false); x = fmaxf(u2f(s[0]), u2f(s[1]));
  auto t = __builtin_amdgcn_permlane32_swap(f2u(x), f2u(x), false, false); return fmaxf(u2f(t[0]), u2f(t[1]));
}
__device__ __forceinline__ float xrow_sum(float x) { x = rs16(x, x); return rs32(x, x); }
__device__ __forceinline__ f32x4 mfma(v4u a, v4u b, f32x4 c) { return __builtin_amdgcn_mfma_f32_16x16x32_bf16(__builtin_bit_cast(bf16x8, a), __builtin_bit_cast(bf16x8, b), c, 0, 0, 0); }
__device__ __forceinline__ v4u tr_pair(const LAS unsigned char* a0, int off2) {
  const s16x4 lo = __builtin_amdgcn_ds_read_tr16_b64_v4i16((LAS s16x4*)a0), hi = __builtin_amdgcn_ds_read_tr16_b64_v4i16((LAS s16x4*)(a0 + off2));
  return __builtin_bit_cast(v4u, __builtin_shufflevector(lo, hi, 0, 1, 2, 3, 4, 5, 6, 7));
}
__device__ __forceinline__ float pick4(f32x4 o, bool b0, bool b1) { const float x = b0 ? o[1] : o[0], y = b0 ? o[3] : o[2]; return b1 ? y : x; }
template <int STRIDE, int WND> __device__ __forceinline__ void dma_run(const void* sbase, unsigned voff, unsigned ldst, unsigned n) {
  asm volatile("s_mov_b32 m0, %3\n\ts_nop 0\n"
               "1:\n\ts_waitcnt vmcnt(%5)\n\tglobal_load_lds_dwordx4 %0, %2 nt\n\tv_add_u32 %0, %4, %0\n\ts_add_u32 m0, m0, %4\n\ts_sub_u32 %1, %1, 1\n\ts_cmp_lg_u32 %1, 0\n\ts_cbranch_scc1 1b"
               : "+&v"(voff), "+&s"(n) : "s"(sbase), "s"(ldst), "n"(STRIDE), "n"(WND - 1) : "memory", "scc");
}
template <int STRIDE, int WND, int OPEN> __device__ __forceinline__ void dma_run_open(const void* sbase, unsigned voff, unsigned ldst, unsigned n, unsigned faddr) {
  unsigned f, sf;
  asm volatile("s_mov_b32 m0, %5\n\ts_nop 0\n"
               "1:\n\tds_read_b32 %2, %9\n\ts_waitcnt vmcnt(%7)\n\tglobal_load_lds_dwordx4 %0, %4 nt\n\tv_add_u32 %0, %6, %0\n\ts_add_u32 m0, m0, %6\n\ts_sub_u32 %1, %1, 1\n\ts_cmp_eq_u32 %1, 0\n\ts_cbranch_scc1 3f\n\t"
               "s_waitcnt lgkmcnt(0)\n\tv_readfirstlane_b32 %3, %2\n\ts_cmp_lt_u32 %3, %8\n\ts_cbranch_scc1 1b\n"
               "2:\n\tglobal_load_lds_dwordx4 %0, %4 nt\n\tv_add_u32 %0, %6, %0\n\ts_add_u32 m0, m0, %6\n\ts_sub_u32 %1, %1, 1\n\ts_cmp_lg_u32 %1, 0\n\ts_cbranch_scc1 2b\n"
               "3:\n\ts_waitcnt lgkmcnt(0)"
               : "+&v"(voff), "+&s"(n), "=&v"(f), "=&s"(sf) : "s"(sbase), "s"(ldst), "n"(STRIDE), "n"(WND - 1), "n"(OPEN), "v"(faddr) : "memory", "scc");
}
template <int STRIDE, int WND, int DELTA> __device__ __forceinline__ void dma_run_paced(const void* sbase, unsigned& voff, unsigned ldst, unsigned n, unsigned taddr, unsigned tnext) {
  unsigned f, sf;
  asm volatile("s_mov_b32 m0, %6\n"
               "1:\n\ts_waitcnt vmcnt(%8)\n"
               "2:\n\tds_read_b32 %3, %10\n\ts_memrealtime s[60:61]\n\ts_waitcnt lgkmcnt(0)\n\tv_readfirstlane_b32 %4, %3\n\ts_sub_u32 %4, s60, %4\n\ts_cmp_ge_i32 %4, %2\n\ts_cbranch_scc1 3f\n\ts_sleep 1\n\ts_branch 2b\n"
               "3:\n\tglobal_load_lds_dwordx4 %0, %5 nt\n\tv_add_u32 %0, %7, %0\n\ts_add_u32 m0, m0, %7\n\ts_add_u32 %2, %2, %9\n\ts_sub_u32 %1, %1, 1\n\ts_cmp_lg_u32 %1, 0\n\ts_cbranch_scc1 1b"
               : "+&v"(voff), "+&s"(n), "+&s"(tnext), "=&v"(f), "=&s"(sf) : "s"(sbase), "s"(ldst), "n"(STRIDE), "n"(WND - 1), "n"(DELTA), "v"(taddr) : "memory", "scc", "s60", "s61");
}
template <int WND, int DELTA> __device__ __forceinline__ void dma_run_rot(const void* sbase, unsigned voff, unsigned ldst, unsigned p0, unsigned n, unsigned rot, unsigned taddr, unsigned tnext) {
  unsigned f, sf, vt;
  asm volatile("s_lshl_b32 %4, %0, 12\n\ts_add_u32 m0, %8, %4\n\ts_add_u32 %0, %0, %9\n"
               "1:\n\ts_waitcnt vmcnt(%10)\n"
               "2:\n\tds_read_b32 %3, %12\n\ts_memrealtime s[60:61]\n\ts_waitcnt lgkmcnt(0)\n\tv_readfirstlane_b32 %4, %3\n\ts_sub_u32 %4, s60, %4\n\ts_cmp_ge_i32 %4, %2\n\ts_cbranch_scc1 3f\n\ts_sleep 1\n\ts_branch 2b\n"
               "3:\n\ts_and_b32 %4, %0, 15\n\ts_lshl_b32 %4, %4, 12\n\tv_add_u32 %5, %4, %7\n\t"
               "global_load_lds_dwordx4 %5, %6 nt\n\ts_add_u32 m0, m0, 4096\n\ts_add_u32 %0, %0, 1\n\ts_add_u32 %2, %2, %11\n\ts_sub_u32 %1, %1, 1\n\ts_cmp_lg_u32 %1, 0\n\ts_cbranch_scc1 1b"
               : "+&s"(p0), "+&s"(n), "+&s"(tnext), "=&v"(f), "=&s"(sf), "=&v"(vt)
               : "s"(sbase), "v"(voff), "s"(ldst), "s"(rot), "n"(WND - 1), "n"(DELTA), "v"(taddr) : "memory", "scc", "s60", "s61");
}
__device__ __forceinline__ void wait_until(unsigned taddr, unsigned t) {
  unsigned f, sf;
  asm volatile("2:\n\tds_read_b32 %0, %2\n\ts_memrealtime s[60:61]\n\ts_waitcnt lgkmcnt(0)\n\tv_readfirstlane_b32 %1, %0\n\ts_sub_u32 %1, s60, %1\n\ts_cmp_ge_i32 %1, %3\n\ts_cbranch_scc1 3f\n\ts_sleep 1\n\ts_branch 2b\n3:"
               : "=&v"(f), "=&s"(sf) : "v"(taddr), "s"(t) : "memory", "scc", "s60", "s61");
}
#define WAIT_VM(n) asm volatile("s_waitcnt vmcnt(" #n ")" ::: "memory")
#define WAIT_LGKM() asm volatile("s_waitcnt lgkmcnt(0)" ::: "memory")
#define BARRIER() asm volatile("s_waitcnt lgkmcnt(0)\n\ts_barrier" ::: "memory")
#define RELOAD() asm volatile("" ::: "memory")
__device__ __forceinline__ unsigned lds_add(unsigned addr) {
  unsigned old, one = 1u;
  asm volatile("s_waitcnt lgkmcnt(0)\n\ts_mov_b64 exec, 1\n\tds_add_rtn_u32 %0, %1, %2\n\ts_waitcnt lgkmcnt(0)\n\ts_mov_b64 exec, -1" : "=&v"(old) : "v"(addr), "v"(one) : "memory");
  return __builtin_amdgcn_readfirstlane(old);
}
__device__ __forceinline__ void st_granule(unsigned char* ws, unsigned off, float v, unsigned tag) {
  __hip_atomic_store((GAS unsigned long long*)(ws + off), ((unsigned long long)tag << 32) | f2u(v), RLX_AGENT);
}
__device__ __forceinline__ v4u ld_sc1(__amdgpu_buffer_rsrc_t r, int off) { return __builtin_amdgcn_raw_buffer_load_b128(r, off, 0, 16); }
__device__ __forceinline__ unsigned tag_bad(v4u d, unsigned tag) { return (d[1] ^ tag) | (d[3] ^ tag); }
#define PL4(a, b, c, d) "global_load_dwordx4 " a ", %[off], %[base] sc1\n\tglobal_load_dwordx4 " b ", %[off], %[base] offset:1024 sc1\n\t" \
                        "global_load_dwordx4 " c ", %[off], %[base] offset:2048 sc1\n\tglobal_load_dwordx4 " d ", %[off], %[base] offset:3072 sc1\n\t"
#define PL4A PL4("v[96:99]", "v[100:103]", "v[104:107]", "v[108:111]")
#define PL4B PL4("v[112:115]", "v[116:119]", "v[120:123]", "v[124:127]")
#define PCHK(a, b, c, d, e, f, g, h) "v_xor_b32 %[t0], %[tag], " a "\n\tv_xor_b32 %[t1], %[tag], " b "\n\tv_xor_b32 %[t2], %[tag], " c "\n\tv_or3_b32 %[t0], %[t0], %[t1], %[t2]\n\t" \
  "v_xor_b32 %[t1], %[tag], " d "\n\tv_xor_b32 %[t2], %[tag], " e "\n\tv_or3_b32 %[t0], %[t0], %[t1], %[t2]\n\t" \
  "v_xor_b32 %[t1], %[tag], " f "\n\tv_xor_b32 %[t2], %[tag], " g "\n\tv_or3_b32 %[t0], %[t0], %[t1], %[t2]\n\t" \
  "v_xor_b32 %[t1], %[tag], " h "\n\tv_or_b32 %[t0], %[t0], %[t1]\n\tv_cmp_ne_u32 vcc, 0, %[t0]\n\t"
template <int GAP> __device__ __forceinline__ void poll4(float (&x)[4], float (&y)[4], const unsigned char* base, unsigned off, unsigned tag) {
  unsigned t0, t1, t2, cnt = SPIN_CAP;
  asm volatile(PL4A "s_sleep %[gap]\n"
               "1:\n\t" PL4B "s_waitcnt vmcnt(4)\n\t" PCHK("v97", "v99", "v101", "v103", "v105", "v107", "v109", "v111") "s_cbranch_vccz 2f\n\t"
               PL4A "s_waitcnt vmcnt(4)\n\t" PCHK("v113", "v115", "v117", "v119", "v121", "v123", "v125", "v127") "s_cbranch_vccz 3f\n\t"
               "s_sub_u32 %[cnt], %[cnt], 1\n\ts_cmp_lg_u32 %[cnt], 0\n\ts_cbranch_scc1 1b\n"
               "2:\n\tv_mov_b32 %[x0], v96\n\tv_mov_b32 %[y0], v98\n\tv_mov_b32 %[x1], v100\n\tv_mov_b32 %[y1], v102\n\tv_mov_b32 %[x2], v104\n\tv_mov_b32 %[y2], v106\n\tv_mov_b32 %[x3], v108\n\tv_mov_b32 %[y3], v110\n\ts_branch 4f\n"
               "3:\n\tv_mov_b32 %[x0], v112\n\tv_mov_b32 %[y0], v114\n\tv_mov_b32 %[x1], v116\n\tv_mov_b32 %[y1], v118\n\tv_mov_b32 %[x2], v120\n\tv_mov_b32 %[y2], v122\n\tv_mov_b32 %[x3], v124\n\tv_mov_b32 %[y3], v126\n"
               "4:"
               : [x0] "=&v"(x[0]), [y0] "=&v"(y[0]), [x1] "=&v"(x[1]), [y1] "=&v"(y[1]), [x2] "=&v"(x[2]), [y2] "=&v"(y[2]), [x3] "=&v"(x[3]), [y3] "=&v"(y[3]),
                 [t0] "=&v"(t0), [t1] "=&v"(t1), [t2] "=&v"(t2), [cnt] "+&s"(cnt)
               : [off] "v"(off), [base] "s"(base), [tag] "s"(tag), [gap] "n"(GAP)
               : "memory", "vcc", "scc", "v96", "v97", "v98", "v99", "v100", "v101", "v102", "v103", "v104", "v105", "v106", "v107", "v108", "v109", "v110", "v111",
                 "v112", "v113", "v114", "v115", "v116", "v117", "v118", "v119", "v120", "v121", "v122", "v123", "v124", "v125", "v126", "v127");
}
#define PL5(a, b, c, d, e) PL4(a, b, c, d) "global_load_dwordx4 " e ", %[off5], %[base] sc1\n\t"
#define PL5A PL5("v[88:91]", "v[92:95]", "v[96:99]", "v[100:103]", "v[104:107]")
#define PL5B PL5("v[108:111]", "v[112:115]", "v[116:119]", "v[120:123]", "v[124:127]")
#define PCHK5(a, b, c, d, e, f, g, h, i, j) "v_xor_b32 %[t0], %[tag], " a "\n\tv_xor_b32 %[t1], %[tag], " b "\n\tv_xor_b32 %[t2], %[tag], " c "\n\tv_or3_b32 %[t0], %[t0], %[t1], %[t2]\n\t" \
  "v_xor_b32 %[t1], %[tag], " d "\n\tv_xor_b32 %[t2], %[tag], " e "\n\tv_or3_b32 %[t0], %[t0], %[t1], %[t2]\n\t" \
  "v_xor_b32 %[t1], %[tag], " f "\n\tv_xor_b32 %[t2], %[tag], " g "\n\tv_or3_b32 %[t0], %[t0], %[t1], %[t2]\n\t" \
  "v_xor_b32 %[t1], %[tag], " h "\n\tv_xor_b32 %[t2], %[tag], " i "\n\tv_or3_b32 %[t0], %[t0], %[t1], %[t2]\n\t" \
  "v_xor_b32 %[t1], %[tag], " j "\n\tv_or_b32 %[t0], %[t0], %[t1]\n\tv_cmp_ne_u32 vcc, 0, %[t0]\n\t"
template <int GAP> __device__ __forceinline__ void poll5(float (&x)[4], float (&y)[4], float& m, float& l, const unsigned char* base, unsigned off, unsigned off5, unsigned tag) {
  unsigned t0, t1, t2, cnt = SPIN_CAP;
  asm volatile(PL5A "s_sleep %[gap]\n"
               "1:\n\t" PL5B "s_waitcnt vmcnt(5)\n\t" PCHK5("v89", "v91", "v93", "v95", "v97", "v99", "v101", "v103", "v105", "v107") "s_cbranch_vccz 2f\n\t"
               PL5A "s_waitcnt vmcnt(5)\n\t" PCHK5("v109", "v111", "v113", "v115", "v117", "v119", "v121", "v123", "v125", "v127") "s_cbranch_vccz 3f\n\t"
               "s_sub_u32 %[cnt], %[cnt], 1\n\ts_cmp_lg_u32 %[cnt], 0\n\ts_cbranch_scc1 1b\n"
               "2:\n\tv_mov_b32 %[m], v104\n\tv_mov_b32 %[l], v106\n\tv_mov_b32 %[x0], v88\n\tv_mov_b32 %[y0], v90\n\tv_mov_b32 %[x1], v92\n\tv_mov_b32 %[y1], v94\n\tv_mov_b32 %[x2], v96\n\tv_mov_b32 %[y2], v98\n\tv_mov_b32 %[x3], v100\n\tv_mov_b32 %[y3], v102\n\ts_branch 4f\n"
               "3:\n\tv_mov_b32 %[m], v124\n\tv_mov_b32 %[l], v126\n\tv_mov_b32 %[x0], v108\n\tv_mov_b32 %[y0], v110\n\tv_mov_b32 %[x1], v112\n\tv_mov_b32 %[y1], v114\n\tv_mov_b32 %[x2], v116\n\tv_mov_b32 %[y2], v118\n\tv_mov_b32 %[x3], v120\n\tv_mov_b32 %[y3], v122\n"
               "4:"
               : [x0] "=&v"(x[0]), [y0] "=&v"(y[0]), [x1] "=&v"(x[1]), [y1] "=&v"(y[1]), [x2] "=&v"(x[2]), [y2] "=&v"(y[2]), [x3] "=&v"(x[3]), [y3] "=&v"(y[3]), [m] "=&v"(m), [l] "=&v"(l),
                 [t0] "=&v"(t0), [t1] "=&v"(t1), [t2] "=&v"(t2), [cnt] "+&s"(cnt)
               : [off] "v"(off), [off5] "v"(off5), [base] "s"(base), [tag] "s"(tag), [gap] "n"(GAP)
               : "memory", "vcc", "scc", "v88", "v89", "v90", "v91", "v92", "v93", "v94", "v95", "v96", "v97", "v98", "v99", "v100", "v101", "v102", "v103", "v104", "v105", "v106", "v107", "v108", "v109", "v110", "v111",
                 "v112", "v113", "v114", "v115", "v116", "v117", "v118", "v119", "v120", "v121", "v122", "v123", "v124", "v125", "v126", "v127");
}
template <int GAP> __device__ __forceinline__ void poll1_2(float& x, float& y, const unsigned char* base, unsigned off, unsigned tag) {
  unsigned t0, t1, cnt = SPIN_CAP;
  asm volatile("global_load_dwordx4 v[96:99], %[off], %[base] sc1\n\ts_sleep %[gap]\n\tglobal_load_dwordx4 v[100:103], %[off], %[base] sc1\n\t\n1:\n\ts_waitcnt vmcnt(1)\n\tv_xor_b32 %[t0], %[tag], v97\n\tv_xor_b32 %[t1], %[tag], v99\n\tv_or_b32 %[t0], %[t0], %[t1]\n\tv_cmp_ne_u32 vcc, 0, %[t0]\n\ts_cbranch_vccz 10f\n\tglobal_load_dwordx4 v[96:99], %[off], %[base] sc1\n\ts_waitcnt vmcnt(1)\n\tv_xor_b32 %[t0], %[tag], v101\n\tv_xor_b32 %[t1], %[tag], v103\n\tv_or_b32 %[t0], %[t0], %[t1]\n\tv_cmp_ne_u32 vcc, 0, %[t0]\n\ts_cbranch_vccz 11f\n\tglobal_load_dwordx4 v[100:103], %[off], %[base] sc1\n\ts_sub_u32 %[cnt], %[cnt], 1\n\ts_cmp_lg_u32 %[cnt], 0\n\ts_cbranch_scc1 1b\n10:\n\tv_mov_b32 %[x], v96\n\tv_mov_b32 %[y], v98\n\ts_branch 4f\n11:\n\tv_mov_b32 %[x], v100\n\tv_mov_b32 %[y], v102\n\t\n4:"
               : [x] "=&v"(x), [y] "=&v"(y), [t0] "=&v"(t0), [t1] "=&v"(t1), [cnt] "+&s"(cnt)
               : [off] "v"(off), [base] "s"(base), [tag] "s"(tag), [gap] "n"(GAP)
               : "memory", "vcc", "scc", "v96", "v97", "v98", "v99", "v100", "v101", "v102", "v103", "v104", "v105", "v106", "v107", "v108", "v109", "v110", "v111", "v112", "v113", "v114", "v115", "v116", "v117", "v118", "v119", "v120", "v121", "v122", "v123", "v124", "v125", "v126", "v127");
}
template <int GAP> __device__ __forceinline__ void poll1_3(float& x, float& y, const unsigned char* base, unsigned off, unsigned tag) {
  unsigned t0, t1, cnt = SPIN_CAP;
  asm volatile("global_load_dwordx4 v[96:99], %[off], %[base] sc1\n\ts_sleep %[gap]\n\tglobal_load_dwordx4 v[100:103], %[off], %[base] sc1\n\ts_sleep %[gap]\n\tglobal_load_dwordx4 v[104:107], %[off], %[base] sc1\n\t\n1:\n\ts_waitcnt vmcnt(2)\n\tv_xor_b32 %[t0], %[tag], v97\n\tv_xor_b32 %[t1], %[tag], v99\n\tv_or_b32 %[t0], %[t0], %[t1]\n\tv_cmp_ne_u32 vcc, 0, %[t0]\n\ts_cbranch_vccz 10f\n\tglobal_load_dwordx4 v[96:99], %[off], %[base] sc1\n\ts_waitcnt vmcnt(2)\n\tv_xor_b32 %[t0], %[tag], v101\n\tv_xor_b32 %[t1], %[tag], v103\n\tv_or_b32 %[t0], %[t0], %[t1]\n\tv_cmp_ne_u32 vcc, 0, %[t0]\n\ts_cbranch_vccz 11f\n\tglobal_load_dwordx4 v[100:103], %[off], %[base] sc1\n\ts_waitcnt vmcnt(2)\n\tv_xor_b32 %[t0], %[tag], v105\n\tv_xor_b32 %[t1], %[tag], v107\n\tv_or_b32 %[t0], %[t0], %[t1]\n\tv_cmp_ne_u32 vcc, 0, %[t0]\n\ts_cbranch_vccz 12f\n\tglobal_load_dwordx4 v[104:107], %[off], %[base] sc1\n\ts_sub_u32 %[cnt], %[cnt], 1\n\ts_cmp_lg_u32 %[cnt], 0\n\ts_cbranch_scc1 1b\n10:\n\tv_mov_b32 %[x], v96\n\tv_mov_b32 %[y], v98\n\ts_branch 4f\n11:\n\tv_mov_b32 %[x], v100\n\tv_mov_b32 %[y], v102\n\ts_branch 4f\n12:\n\tv_mov_b32 %[x], v104\n\tv_mov_b32 %[y], v106\n\t\n4:"
               : [x] "=&v"(x), [y] "=&v"(y), [t0] "=&v"(t0), [t1] "=&v"(t1), [cnt] "+&s"(cnt)
               : [off] "v"(off), [base] "s"(base), [tag] "s"(tag), [gap] "n"(GAP)
               : "memory", "vcc", "scc", "v96", "v97", "v98", "v99", "v100", "v101", "v102", "v103", "v104", "v105", "v106", "v107", "v108", "v109", "v110", "v111", "v112", "v113", "v114", "v115", "v116", "v117", "v118", "v119", "v120", "v121", "v122", "v123", "v124", "v125", "v126", "v127");
}
template <int GAP> __device__ __forceinline__ void poll1_4(float& x, float& y, const unsigned char* base, unsigned off, unsigned tag) {
  unsigned t0, t1, cnt = SPIN_CAP;
  asm volatile("global_load_dwordx4 v[96:99], %[off], %[base] sc1\n\ts_sleep %[gap]\n\tglobal_load_dwordx4 v[100:103], %[off], %[base] sc1\n\ts_sleep %[gap]\n\tglobal_load_dwordx4 v[104:107], %[off], %[base] sc1\n\ts_sleep %[gap]\n\tglobal_load_dwordx4 v[108:111], %[off], %[base] sc1\n\t\n1:\n\ts_waitcnt vmcnt(3)\n\tv_xor_b32 %[t0], %[tag], v97\n\tv_xor_b32 %[t1], %[tag], v99\n\tv_or_b32 %[t0], %[t0], %[t1]\n\tv_cmp_ne_u32 vcc, 0, %[t0]\n\ts_cbranch_vccz 10f\n\tglobal_load_dwordx4 v[96:99], %[off], %[base] sc1\n\ts_waitcnt vmcnt(3)\n\tv_xor_b32 %[t0], %[tag], v101\n\tv_xor_b32 %[t1], %[tag], v103\n\tv_or_b32 %[t0], %[t0], %[t1]\n\tv_cmp_ne_u32 vcc, 0, %[t0]\n\ts_cbranch_vccz 11f\n\tglobal_load_dwordx4 v[100:103], %[off], %[base] sc1\n\ts_waitcnt vmcnt(3)\n\tv_xor_b32 %[t0], %[tag], v105\n\tv_xor_b32 %[t1], %[tag], v107\n\tv_or_b32 %[t0], %[t0], %[t1]\n\tv_cmp_ne_u32 vcc, 0, %[t0]\n\ts_cbranch_vccz 12f\n\tglobal_load_dwordx4 v[104:107], %[off], %[base] sc1\n\ts_waitcnt vmcnt(3)\n\tv_xor_b32 %[t0], %[tag], v109\n\tv_xor_b32 %[t1], %[tag], v111\n\tv_or_b32 %[t0], %[t0], %[t1]\n\tv_cmp_ne_u32 vcc, 0, %[t0]\n\ts_cbranch_vccz 13f\n\tglobal_load_dwordx4 v[108:111], %[off], %[base] sc1\n\ts_sub_u32 %[cnt], %[cnt], 1\n\ts_cmp_lg_u32 %[cnt], 0\n\ts_cbranch_scc1 1b\n10:\n\tv_mov_b32 %[x], v96\n\tv_mov_b32 %[y], v98\n\ts_branch 4f\n11:\n\tv_mov_b32 %[x], v100\n\tv_mov_b32 %[y], v102\n\ts_branch 4f\n12:\n\tv_mov_b32 %[x], v104\n\tv_mov_b32 %[y], v106\n\ts_branch 4f\n13:\n\tv_mov_b32 %[x], v108\n\tv_mov_b32 %[y], v110\n\t\n4:"
               : [x] "=&v"(x), [y] "=&v"(y), [t0] "=&v"(t0), [t1] "=&v"(t1), [cnt] "+&s"(cnt)
               : [off] "v"(off), [base] "s"(base), [tag] "s"(tag), [gap] "n"(GAP)
               : "memory", "vcc", "scc", "v96", "v97", "v98", "v99", "v100", "v101", "v102", "v103", "v104", "v105", "v106", "v107", "v108", "v109", "v110", "v111", "v112", "v113", "v114", "v115", "v116", "v117", "v118", "v119", "v120", "v121", "v122", "v123", "v124", "v125", "v126", "v127");
}
template <int GAP> __device__ __forceinline__ void poll1_6(float& x, float& y, const unsigned char* base, unsigned off, unsigned tag) {
  unsigned t0, t1, cnt = SPIN_CAP;
  asm volatile("global_load_dwordx4 v[96:99], %[off], %[base] sc1\n\ts_sleep %[gap]\n\tglobal_load_dwordx4 v[100:103], %[off], %[base] sc1\n\ts_sleep %[gap]\n\tglobal_load_dwordx4 v[104:107], %[off], %[base] sc1\n\ts_sleep %[gap]\n\tglobal_load_dwordx4 v[108:111], %[off], %[base] sc1\n\ts_sleep %[gap]\n\tglobal_load_dwordx4 v[112:115], %[off], %[base] sc1\n\ts_sleep %[gap]\n\tglobal_load_dwordx4 v[116:119], %[off], %[base] sc1\n\t\n1:\n\ts_waitcnt vmcnt(5)\n\tv_xor_b32 %[t0], %[tag], v97\n\tv_xor_b32 %[t1], %[tag], v99\n\tv_or_b32 %[t0], %[t0], %[t1]\n\tv_cmp_ne_u32 vcc, 0, %[t0]\n\ts_cbranch_vccz 10f\n\tglobal_load_dwordx4 v[96:99], %[off], %[base] sc1\n\ts_waitcnt vmcnt(5)\n\tv_xor_b32 %[t0], %[tag], v101\n\tv_xor_b32 %[t1], %[tag], v103\n\tv_or_b32 %[t0], %[t0], %[t1]\n\tv_cmp_ne_u32 vcc, 0, %[t0]\n\ts_cbranch_vccz 11f\n\tglobal_load_dwordx4 v[100:103], %[off], %[base] sc1\n\ts_waitcnt vmcnt(5)\n\tv_xor_b32 %[t0], %[tag], v105\n\tv_xor_b32 %[t1], %[tag], v107\n\tv_or_b32 %[t0], %[t0], %[t1]\n\tv_cmp_ne_u32 vcc, 0, %[t0]\n\ts_cbranch_vccz 12f\n\tglobal_load_dwordx4 v[104:107], %[off], %[base] sc1\n\ts_waitcnt vmcnt(5)\n\tv_xor_b32 %[t0], %[tag], v109\n\tv_xor_b32 %[t1], %[tag], v111\n\tv_or_b32 %[t0], %[t0], %[t1]\n\tv_cmp_ne_u32 vcc, 0, %[t0]\n\ts_cbranch_vccz 13f\n\tglobal_load_dwordx4 v[108:111], %[off], %[base] sc1\n\ts_waitcnt vmcnt(5)\n\tv_xor_b32 %[t0], %[tag], v113\n\tv_xor_b32 %[t1], %[tag], v115\n\tv_or_b32 %[t0], %[t0], %[t1]\n\tv_cmp_ne_u32 vcc, 0, %[t0]\n\ts_cbranch_vccz 14f\n\tglobal_load_dwordx4 v[112:115], %[off], %[base] sc1\n\ts_waitcnt vmcnt(5)\n\tv_xor_b32 %[t0], %[tag], v117\n\tv_xor_b32 %[t1], %[tag], v119\n\tv_or_b32 %[t0], %[t0], %[t1]\n\tv_cmp_ne_u32 vcc, 0, %[t0]\n\ts_cbranch_vccz 15f\n\tglobal_load_dwordx4 v[116:119], %[off], %[base] sc1\n\ts_sub_u32 %[cnt], %[cnt], 1\n\ts_cmp_lg_u32 %[cnt], 0\n\ts_cbranch_scc1 1b\n10:\n\tv_mov_b32 %[x], v96\n\tv_mov_b32 %[y], v98\n\ts_branch 4f\n11:\n\tv_mov_b32 %[x], v100\n\tv_mov_b32 %[y], v102\n\ts_branch 4f\n12:\n\tv_mov_b32 %[x], v104\n\tv_mov_b32 %[y], v106\n\ts_branch 4f\n13:\n\tv_mov_b32 %[x], v108\n\tv_mov_b32 %[y], v110\n\ts_branch 4f\n14:\n\tv_mov_b32 %[x], v112\n\tv_mov_b32 %[y], v114\n\ts_branch 4f\n15:\n\tv_mov_b32 %[x], v116\n\tv_mov_b32 %[y], v118\n\t\n4:"
               : [x] "=&v"(x), [y] "=&v"(y), [t0] "=&v"(t0), [t1] "=&v"(t1), [cnt] "+&s"(cnt)
               : [off] "v"(off), [base] "s"(base), [tag] "s"(tag), [gap] "n"(GAP)
               : "memory", "vcc", "scc", "v96", "v97", "v98", "v99", "v100", "v101", "v102", "v103", "v104", "v105", "v106", "v107", "v108", "v109", "v110", "v111", "v112", "v113", "v114", "v115", "v116", "v117", "v118", "v119", "v120", "v121", "v122", "v123", "v124", "v125", "v126", "v127");
}
template <int GAP> __device__ __forceinline__ void poll1_8(float& x, float& y, const unsigned char* base, unsigned off, unsigned tag) {
  unsigned t0, t1, cnt = SPIN_CAP;
  asm volatile("global_load_dwordx4 v[96:99], %[off], %[base] sc1\n\ts_sleep %[gap]\n\tglobal_load_dwordx4 v[100:103], %[off], %[base] sc1\n\ts_sleep %[gap]\n\tglobal_load_dwordx4 v[104:107], %[off], %[base] sc1\n\ts_sleep %[gap]\n\tglobal_load_dwordx4 v[108:111], %[off], %[base] sc1\n\ts_sleep %[gap]\n\tglobal_load_dwordx4 v[112:115], %[off], %[base] sc1\n\ts_sleep %[gap]\n\tglobal_load_dwordx4 v[116:119], %[off], %[base] sc1\n\ts_sleep %[gap]\n\tglobal_load_dwordx4 v[120:123], %[off], %[base] sc1\n\ts_sleep %[gap]\n\tglobal_load_dwordx4 v[124:127], %[off], %[base] sc1\n\t\n1:\n\ts_waitcnt vmcnt(7)\n\tv_xor_b32 %[t0], %[tag], v97\n\tv_xor_b32 %[t1], %[tag], v99\n\tv_or_b32 %[t0], %[t0], %[t1]\n\tv_cmp_ne_u32 vcc, 0, %[t0]\n\ts_cbranch_vccz 10f\n\tglobal_load_dwordx4 v[96:99], %[off], %[base] sc1\n\ts_waitcnt vmcnt(7)\n\tv_xor_b32 %[t0], %[tag], v101\n\tv_xor_b32 %[t1], %[tag], v103\n\tv_or_b32 %[t0], %[t0], %[t1]\n\tv_cmp_ne_u32 vcc, 0, %[t0]\n\ts_cbranch_vccz 11f\n\tglobal_load_dwordx4 v[100:103], %[off], %[base] sc1\n\ts_waitcnt vmcnt(7)\n\tv_xor_b32 %[t0], %[tag], v105\n\tv_xor_b32 %[t1], %[tag], v107\n\tv_or_b32 %[t0], %[t0], %[t1]\n\tv_cmp_ne_u32 vcc, 0, %[t0]\n\ts_cbranch_vccz 12f\n\tglobal_load_dwordx4 v[104:107], %[off], %[base] sc1\n\ts_waitcnt vmcnt(7)\n\tv_xor_b32 %[t0], %[tag], v109\n\tv_xor_b32 %[t1], %[tag], v111\n\tv_or_b32 %[t0], %[t0], %[t1]\n\tv_cmp_ne_u32 vcc, 0, %[t0]\n\ts_cbranch_vccz 13f\n\tglobal_load_dwordx4 v[108:111], %[off], %[base] sc1\n\ts_waitcnt vmcnt(7)\n\tv_xor_b32 %[t0], %[tag], v113\n\tv_xor_b32 %[t1], %[tag], v115\n\tv_or_b32 %[t0], %[t0], %[t1]\n\tv_cmp_ne_u32 vcc, 0, %[t0]\n\ts_cbranch_vccz 14f\n\tglobal_load_dwordx4 v[112:115], %[off], %[base] sc1\n\ts_waitcnt vmcnt(7)\n\tv_xor_b32 %[t0], %[tag], v117\n\tv_xor_b32 %[t1], %[tag], v119\n\tv_or_b32 %[t0], %[t0], %[t1]\n\tv_cmp_ne_u32 vcc, 0, %[t0]\n\ts_cbranch_vccz 15f\n\tglobal_load_dwordx4 v[116:119], %[off], %[base] sc1\n\ts_waitcnt vmcnt(7)\n\tv_xor_b32 %[t0], %[tag], v121\n\tv_xor_b32 %[t1], %[tag], v123\n\tv_or_b32 %[t0], %[t0], %[t1]\n\tv_cmp_ne_u32 vcc, 0, %[t0]\n\ts_cbranch_vccz 16f\n\tglobal_load_dwordx4 v[120:123], %[off], %[base] sc1\n\ts_waitcnt vmcnt(7)\n\tv_xor_b32 %[t0], %[tag], v125\n\tv_xor_b32 %[t1], %[tag], v127\n\tv_or_b32 %[t0], %[t0], %[t1]\n\tv_cmp_ne_u32 vcc, 0, %[t0]\n\ts_cbranch_vccz 17f\n\tglobal_load_dwordx4 v[124:127], %[off], %[base] sc1\n\ts_sub_u32 %[cnt], %[cnt], 1\n\ts_cmp_lg_u32 %[cnt], 0\n\ts_cbranch_scc1 1b\n10:\n\tv_mov_b32 %[x], v96\n\tv_mov_b32 %[y], v98\n\ts_branch 4f\n11:\n\tv_mov_b32 %[x], v100\n\tv_mov_b32 %[y], v102\n\ts_branch 4f\n12:\n\tv_mov_b32 %[x], v104\n\tv_mov_b32 %[y], v106\n\ts_branch 4f\n13:\n\tv_mov_b32 %[x], v108\n\tv_mov_b32 %[y], v110\n\ts_branch 4f\n14:\n\tv_mov_b32 %[x], v112\n\tv_mov_b32 %[y], v114\n\ts_branch 4f\n15:\n\tv_mov_b32 %[x], v116\n\tv_mov_b32 %[y], v118\n\ts_branch 4f\n16:\n\tv_mov_b32 %[x], v120\n\tv_mov_b32 %[y], v122\n\ts_branch 4f\n17:\n\tv_mov_b32 %[x], v124\n\tv_mov_b32 %[y], v126\n\t\n4:"
               : [x] "=&v"(x), [y] "=&v"(y), [t0] "=&v"(t0), [t1] "=&v"(t1), [cnt] "+&s"(cnt)
               : [off] "v"(off), [base] "s"(base), [tag] "s"(tag), [gap] "n"(GAP)
               : "memory", "vcc", "scc", "v96", "v97", "v98", "v99", "v100", "v101", "v102", "v103", "v104", "v105", "v106", "v107", "v108", "v109", "v110", "v111", "v112", "v113", "v114", "v115", "v116", "v117", "v118", "v119", "v120", "v121", "v122", "v123", "v124", "v125", "v126", "v127");
}
template <int GAP> __device__ __forceinline__ void pollm_2(float& x, float& y, const unsigned char* base, unsigned off, unsigned tag) {
  unsigned t0, t1, cnt = SPIN_CAP;
  unsigned long long pm;
  x = 0.f; y = 0.f;
  asm volatile("s_mov_b64 %[pm], exec\n\tglobal_load_dwordx4 v[96:99], %[off], %[base] sc1\n\ts_sleep %[gap]\n\tglobal_load_dwordx4 v[100:103], %[off], %[base] sc1\n\t\n1:\n\ts_waitcnt vmcnt(1)\n\tv_xor_b32 %[t0], %[tag], v97\n\tv_xor_b32 %[t1], %[tag], v99\n\tv_or_b32 %[t0], %[t0], %[t1]\n\tv_cmp_eq_u32 vcc, 0, %[t0]\n\ts_mov_b64 exec, vcc\n\tv_mov_b32 %[x], v96\n\tv_mov_b32 %[y], v98\n\ts_andn2_b64 %[pm], %[pm], vcc\n\ts_cbranch_scc0 4f\n\ts_mov_b64 exec, %[pm]\n\tglobal_load_dwordx4 v[96:99], %[off], %[base] sc1\n\ts_waitcnt vmcnt(1)\n\tv_xor_b32 %[t0], %[tag], v101\n\tv_xor_b32 %[t1], %[tag], v103\n\tv_or_b32 %[t0], %[t0], %[t1]\n\tv_cmp_eq_u32 vcc, 0, %[t0]\n\ts_mov_b64 exec, vcc\n\tv_mov_b32 %[x], v100\n\tv_mov_b32 %[y], v102\n\ts_andn2_b64 %[pm], %[pm], vcc\n\ts_cbranch_scc0 4f\n\ts_mov_b64 exec, %[pm]\n\tglobal_load_dwordx4 v[100:103], %[off], %[base] sc1\n\ts_sub_u32 %[cnt], %[cnt], 1\n\ts_cmp_lg_u32 %[cnt], 0\n\ts_cbranch_scc1 1b\n4:\n\ts_mov_b64 exec, -1"
               : [x] "+&v"(x), [y] "+&v"(y), [t0] "=&v"(t0), [t1] "=&v"(t1), [cnt] "+&s"(cnt), [pm] "=&s"(pm)
               : [off] "v"(off), [base] "s"(base), [tag] "s"(tag), [gap] "n"(GAP)
               : "memory", "vcc", "scc", "v96", "v97", "v98", "v99", "v100", "v101", "v102", "v103", "v104", "v105", "v106", "v107", "v108", "v109", "v110", "v111", "v112", "v113", "v114", "v115", "v116", "v117", "v118", "v119", "v120", "v121", "v122", "v123", "v124", "v125", "v126", "v127");
}
template <int GAP> __device__ __forceinline__ void pollm_3(float& x, float& y, const unsigned char* base, unsigned off, unsigned tag) {
  unsigned t0, t1, cnt = SPIN_CAP;
  unsigned long long pm;
  x = 0.f; y = 0.f;
  asm volatile("s_mov_b64 %[pm], exec\n\tglobal_load_dwordx4 v[96:99], %[off], %[base] sc1\n\ts_sleep %[gap]\n\tglobal_load_dwordx4 v[100:103], %[off], %[base] sc1\n\ts_sleep %[gap]\n\tglobal_load_dwordx4 v[104:107], %[off], %[base] sc1\n\t\n1:\n\ts_waitcnt vmcnt(2)\n\tv_xor_b32 %[t0], %[tag], v97\n\tv_xor_b32 %[t1], %[tag], v99\n\tv_or_b32 %[t0], %[t0], %[t1]\n\tv_cmp_eq_u32 vcc, 0, %[t0]\n\ts_mov_b64 exec, vcc\n\tv_mov_b32 %[x], v96\n\tv_mov_b32 %[y], v98\n\ts_andn2_b64 %[pm], %[pm], vcc\n\ts_cbranch_scc0 4f\n\ts_mov_b64 exec, %[pm]\n\tglobal_load_dwordx4 v[96:99], %[off], %[base] sc1\n\ts_waitcnt vmcnt(2)\n\tv_xor_b32 %[t0], %[tag], v101\n\tv_xor_b32 %[t1], %[tag], v103\n\tv_or_b32 %[t0], %[t0], %[t1]\n\tv_cmp_eq_u32 vcc, 0, %[t0]\n\ts_mov_b64 exec, vcc\n\tv_mov_b32 %[x], v100\n\tv_mov_b32 %[y], v102\n\ts_andn2_b64 %[pm], %[pm], vcc\n\ts_cbranch_scc0 4f\n\ts_mov_b64 exec, %[pm]\n\tglobal_load_dwordx4 v[100:103], %[off], %[base] sc1\n\ts_waitcnt vmcnt(2)\n\tv_xor_b32 %[t0], %[tag], v105\n\tv_xor_b32 %[t1], %[tag], v107\n\tv_or_b32 %[t0], %[t0], %[t1]\n\tv_cmp_eq_u32 vcc, 0, %[t0]\n\ts_mov_b64 exec, vcc\n\tv_mov_b32 %[x], v104\n\tv_mov_b32 %[y], v106\n\ts_andn2_b64 %[pm], %[pm], vcc\n\ts_cbranch_scc0 4f\n\ts_mov_b64 exec, %[pm]\n\tglobal_load_dwordx4 v[104:107], %[off], %[base] sc1\n\ts_sub_u32 %[cnt], %[cnt], 1\n\ts_cmp_lg_u32 %[cnt], 0\n\ts_cbranch_scc1 1b\n4:\n\ts_mov_b64 exec, -1"
               : [x] "+&v"(x), [y] "+&v"(y), [t0] "=&v"(t0), [t1] "=&v"(t1), [cnt] "+&s"(cnt), [pm] "=&s"(pm)
               : [off] "v"(off), [base] "s"(base), [tag] "s"(tag), [gap] "n"(GAP)
               : "memory", "vcc", "scc", "v96", "v97", "v98", "v99", "v100", "v101", "v102", "v103", "v104", "v105", "v106", "v107", "v108", "v109", "v110", "v111", "v112", "v113", "v114", "v115", "v116", "v117", "v118", "v119", "v120", "v121", "v122", "v123", "v124", "v125", "v126", "v127");
}
template <int GAP> __device__ __forceinline__ void pollm_4(float& x, float& y, const unsigned char* base, unsigned off, unsigned tag) {
  unsigned t0, t1, cnt = SPIN_CAP;
  unsigned long long pm;
  x = 0.f; y = 0.f;
  asm volatile("s_mov_b64 %[pm], exec\n\tglobal_load_dwordx4 v[96:99], %[off], %[base] sc1\n\ts_sleep %[gap]\n\tglobal_load_dwordx4 v[100:103], %[off], %[base] sc1\n\ts_sleep %[gap]\n\tglobal_load_dwordx4 v[104:107], %[off], %[base] sc1\n\ts_sleep %[gap]\n\tglobal_load_dwordx4 v[108:111], %[off], %[base] sc1\n\t\n1:\n\ts_waitcnt vmcnt(3)\n\tv_xor_b32 %[t0], %[tag], v97\n\tv_xor_b32 %[t1], %[tag], v99\n\tv_or_b32 %[t0], %[t0], %[t1]\n\tv_cmp_eq_u32 vcc, 0, %[t0]\n\ts_mov_b64 exec, vcc\n\tv_mov_b32 %[x], v96\n\tv_mov_b32 %[y], v98\n\ts_andn2_b64 %[pm], %[pm], vcc\n\ts_cbranch_scc0 4f\n\ts_mov_b64 exec, %[pm]\n\tglobal_load_dwordx4 v[96:99], %[off], %[base] sc1\n\ts_waitcnt vmcnt(3)\n\tv_xor_b32 %[t0], %[tag], v101\n\tv_xor_b32 %[t1], %[tag], v103\n\tv_or_b32 %[t0], %[t0], %[t1]\n\tv_cmp_eq_u32 vcc, 0, %[t0]\n\ts_mov_b64 exec, vcc\n\tv_mov_b32 %[x], v100\n\tv_mov_b32 %[y], v102\n\ts_andn2_b64 %[pm], %[pm], vcc\n\ts_cbranch_scc0 4f\n\ts_mov_b64 exec, %[pm]\n\tglobal_load_dwordx4 v[100:103], %[off], %[base] sc1\n\ts_waitcnt vmcnt(3)\n\tv_xor_b32 %[t0], %[tag], v105\n\tv_xor_b32 %[t1], %[tag], v107\n\tv_or_b32 %[t0], %[t0], %[t1]\n\tv_cmp_eq_u32 vcc, 0, %[t0]\n\ts_mov_b64 exec, vcc\n\tv_mov_b32 %[x], v104\n\tv_mov_b32 %[y], v106\n\ts_andn2_b64 %[pm], %[pm], vcc\n\ts_cbranch_scc0 4f\n\ts_mov_b64 exec, %[pm]\n\tglobal_load_dwordx4 v[104:107], %[off], %[base] sc1\n\ts_waitcnt vmcnt(3)\n\tv_xor_b32 %[t0], %[tag], v109\n\tv_xor_b32 %[t1], %[tag], v111\n\tv_or_b32 %[t0], %[t0], %[t1]\n\tv_cmp_eq_u32 vcc, 0, %[t0]\n\ts_mov_b64 exec, vcc\n\tv_mov_b32 %[x], v108\n\tv_mov_b32 %[y], v110\n\ts_andn2_b64 %[pm], %[pm], vcc\n\ts_cbranch_scc0 4f\n\ts_mov_b64 exec, %[pm]\n\tglobal_load_dwordx4 v[108:111], %[off], %[base] sc1\n\ts_sub_u32 %[cnt], %[cnt], 1\n\ts_cmp_lg_u32 %[cnt], 0\n\ts_cbranch_scc1 1b\n4:\n\ts_mov_b64 exec, -1"
               : [x] "+&v"(x), [y] "+&v"(y), [t0] "=&v"(t0), [t1] "=&v"(t1), [cnt] "+&s"(cnt), [pm] "=&s"(pm)
               : [off] "v"(off), [base] "s"(base), [tag] "s"(tag), [gap] "n"(GAP)
               : "memory", "vcc", "scc", "v96", "v97", "v98", "v99", "v100", "v101", "v102", "v103", "v104", "v105", "v106", "v107", "v108", "v109", "v110", "v111", "v112", "v113", "v114", "v115", "v116", "v117", "v118", "v119", "v120", "v121", "v122", "v123", "v124", "v125", "v126", "v127");
}
template <int GAP> __device__ __forceinline__ void pollm_6(float& x, float& y, const unsigned char* base, unsigned off, unsigned tag) {
  unsigned t0, t1, cnt = SPIN_CAP;
  unsigned long long pm;
  x = 0.f; y = 0.f;
  asm volatile("s_mov_b64 %[pm], exec\n\tglobal_load_dwordx4 v[96:99], %[off], %[base] sc1\n\ts_sleep %[gap]\n\tglobal_load_dwordx4 v[100:103], %[off], %[base] sc1\n\ts_sleep %[gap]\n\tglobal_load_dwordx4 v[104:107], %[off], %[base] sc1\n\ts_sleep %[gap]\n\tglobal_load_dwordx4 v[108:111], %[off], %[base] sc1\n\ts_sleep %[gap]\n\tglobal_load_dwordx4 v[112:115], %[off], %[base] sc1\n\ts_sleep %[gap]\n\tglobal_load_dwordx4 v[116:119], %[off], %[base] sc1\n\t\n1:\n\ts_waitcnt vmcnt(5)\n\tv_xor_b32 %[t0], %[tag], v97\n\tv_xor_b32 %[t1], %[tag], v99\n\tv_or_b32 %[t0], %[t0], %[t1]\n\tv_cmp_eq_u32 vcc, 0, %[t0]\n\ts_mov_b64 exec, vcc\n\tv_mov_b32 %[x], v96\n\tv_mov_b32 %[y], v98\n\ts_andn2_b64 %[pm], %[pm], vcc\n\ts_cbranch_scc0 4f\n\ts_mov_b64 exec, %[pm]\n\tglobal_load_dwordx4 v[96:99], %[off], %[base] sc1\n\ts_waitcnt vmcnt(5)\n\tv_xor_b32 %[t0], %[tag], v101\n\tv_xor_b32 %[t1], %[tag], v103\n\tv_or_b32 %[t0], %[t0], %[t1]\n\tv_cmp_eq_u32 vcc, 0, %[t0]\n\ts_mov_b64 exec, vcc\n\tv_mov_b32 %[x], v100\n\tv_mov_b32 %[y], v102\n\ts_andn2_b64 %[pm], %[pm], vcc\n\ts_cbranch_scc0 4f\n\ts_mov_b64 exec, %[pm]\n\tglobal_load_dwordx4 v[100:103], %[off], %[base] sc1\n\ts_waitcnt vmcnt(5)\n\tv_xor_b32 %[t0], %[tag], v105\n\tv_xor_b32 %[t1], %[tag], v107\n\tv_or_b32 %[t0], %[t0], %[t1]\n\tv_cmp_eq_u32 vcc, 0, %[t0]\n\ts_mov_b64 exec, vcc\n\tv_mov_b32 %[x], v104\n\tv_mov_b32 %[y], v106\n\ts_andn2_b64 %[pm], %[pm], vcc\n\ts_cbranch_scc0 4f\n\ts_mov_b64 exec, %[pm]\n\tglobal_load_dwordx4 v[104:107], %[off], %[base] sc1\n\ts_waitcnt vmcnt(5)\n\tv_xor_b32 %[t0], %[tag], v109\n\tv_xor_b32 %[t1], %[tag], v111\n\tv_or_b32 %[t0], %[t0], %[t1]\n\tv_cmp_eq_u32 vcc, 0, %[t0]\n\ts_mov_b64 exec, vcc\n\tv_mov_b32 %[x], v108\n\tv_mov_b32 %[y], v110\n\ts_andn2_b64 %[pm], %[pm], vcc\n\ts_cbranch_scc0 4f\n\ts_mov_b64 exec, %[pm]\n\tglobal_load_dwordx4 v[108:111], %[off], %[base] sc1\n\ts_waitcnt vmcnt(5)\n\tv_xor_b32 %[t0], %[tag], v113\n\tv_xor_b32 %[t1], %[tag], v115\n\tv_or_b32 %[t0], %[t0], %[t1]\n\tv_cmp_eq_u32 vcc, 0, %[t0]\n\ts_mov_b64 exec, vcc\n\tv_mov_b32 %[x], v112\n\tv_mov_b32 %[y], v114\n\ts_andn2_b64 %[pm], %[pm], vcc\n\ts_cbranch_scc0 4f\n\ts_mov_b64 exec, %[pm]\n\tglobal_load_dwordx4 v[112:115], %[off], %[base] sc1\n\ts_waitcnt vmcnt(5)\n\tv_xor_b32 %[t0], %[tag], v117\n\tv_xor_b32 %[t1], %[tag], v119\n\tv_or_b32 %[t0], %[t0], %[t1]\n\tv_cmp_eq_u32 vcc, 0, %[t0]\n\ts_mov_b64 exec, vcc\n\tv_mov_b32 %[x], v116\n\tv_mov_b32 %[y], v118\n\ts_andn2_b64 %[pm], %[pm], vcc\n\ts_cbranch_scc0 4f\n\ts_mov_b64 exec, %[pm]\n\tglobal_load_dwordx4 v[116:119], %[off], %[base] sc1\n\ts_sub_u32 %[cnt], %[cnt], 1\n\ts_cmp_lg_u32 %[cnt], 0\n\ts_cbranch_scc1 1b\n4:\n\ts_mov_b64 exec, -1"
               : [x] "+&v"(x), [y] "+&v"(y), [t0] "=&v"(t0), [t1] "=&v"(t1), [cnt] "+&s"(cnt), [pm] "=&s"(pm)
               : [off] "v"(off), [base] "s"(base), [tag] "s"(tag), [gap] "n"(GAP)
               : "memory", "vcc", "scc", "v96", "v97", "v98", "v99", "v100", "v101", "v102", "v103", "v104", "v105", "v106", "v107", "v108", "v109", "v110", "v111", "v112", "v113", "v114", "v115", "v116", "v117", "v118", "v119", "v120", "v121", "v122", "v123", "v124", "v125", "v126", "v127");
}
template <int GAP> __device__ __forceinline__ void pollm_8(float& x, float& y, const unsigned char* base, unsigned off, unsigned tag) {
  unsigned t0, t1, cnt = SPIN_CAP;
  unsigned long long pm;
  x = 0.f; y = 0.f;
  asm volatile("s_mov_b64 %[pm], exec\n\tglobal_load_dwordx4 v[96:99], %[off], %[base] sc1\n\ts_sleep %[gap]\n\tglobal_load_dwordx4 v[100:103], %[off], %[base] sc1\n\ts_sleep %[gap]\n\tglobal_load_dwordx4 v[104:107], %[off], %[base] sc1\n\ts_sleep %[gap]\n\tglobal_load_dwordx4 v[108:111], %[off], %[base] sc1\n\ts_sleep %[gap]\n\tglobal_load_dwordx4 v[112:115], %[off], %[base] sc1\n\ts_sleep %[gap]\n\tglobal_load_dwordx4 v[116:119], %[off], %[base] sc1\n\ts_sleep %[gap]\n\tglobal_load_dwordx4 v[120:123], %[off], %[base] sc1\n\ts_sleep %[gap]\n\tglobal_load_dwordx4 v[124:127], %[off], %[base] sc1\n\t\n1:\n\ts_waitcnt vmcnt(7)\n\tv_xor_b32 %[t0], %[tag], v97\n\tv_xor_b32 %[t1], %[tag], v99\n\tv_or_b32 %[t0], %[t0], %[t1]\n\tv_cmp_eq_u32 vcc, 0, %[t0]\n\ts_mov_b64 exec, vcc\n\tv_mov_b32 %[x], v96\n\tv_mov_b32 %[y], v98\n\ts_andn2_b64 %[pm], %[pm], vcc\n\ts_cbranch_scc0 4f\n\ts_mov_b64 exec, %[pm]\n\tglobal_load_dwordx4 v[96:99], %[off], %[base] sc1\n\ts_waitcnt vmcnt(7)\n\tv_xor_b32 %[t0], %[tag], v101\n\tv_xor_b32 %[t1], %[tag], v103\n\tv_or_b32 %[t0], %[t0], %[t1]\n\tv_cmp_eq_u32 vcc, 0, %[t0]\n\ts_mov_b64 exec, vcc\n\tv_mov_b32 %[x], v100\n\tv_mov_b32 %[y], v102\n\ts_andn2_b64 %[pm], %[pm], vcc\n\ts_cbranch_scc0 4f\n\ts_mov_b64 exec, %[pm]\n\tglobal_load_dwordx4 v[100:103], %[off], %[base] sc1\n\ts_waitcnt vmcnt(7)\n\tv_xor_b32 %[t0], %[tag], v105\n\tv_xor_b32 %[t1], %[tag], v107\n\tv_or_b32 %[t0], %[t0], %[t1]\n\tv_cmp_eq_u32 vcc, 0, %[t0]\n\ts_mov_b64 exec, vcc\n\tv_mov_b32 %[x], v104\n\tv_mov_b32 %[y], v106\n\ts_andn2_b64 %[pm], %[pm], vcc\n\ts_cbranch_scc0 4f\n\ts_mov_b64 exec, %[pm]\n\tglobal_load_dwordx4 v[104:107], %[off], %[base] sc1\n\ts_waitcnt vmcnt(7)\n\tv_xor_b32 %[t0], %[tag], v109\n\tv_xor_b32 %[t1], %[tag], v111\n\tv_or_b32 %[t0], %[t0], %[t1]\n\tv_cmp_eq_u32 vcc, 0, %[t0]\n\ts_mov_b64 exec, vcc\n\tv_mov_b32 %[x], v108\n\tv_mov_b32 %[y], v110\n\ts_andn2_b64 %[pm], %[pm], vcc\n\ts_cbranch_scc0 4f\n\ts_mov_b64 exec, %[pm]\n\tglobal_load_dwordx4 v[108:111], %[off], %[base] sc1\n\ts_waitcnt vmcnt(7)\n\tv_xor_b32 %[t0], %[tag], v113\n\tv_xor_b32 %[t1], %[tag], v115\n\tv_or_b32 %[t0], %[t0], %[t1]\n\tv_cmp_eq_u32 vcc, 0, %[t0]\n\ts_mov_b64 exec, vcc\n\tv_mov_b32 %[x], v112\n\tv_mov_b32 %[y], v114\n\ts_andn2_b64 %[pm], %[pm], vcc\n\ts_cbranch_scc0 4f\n\ts_mov_b64 exec, %[pm]\n\tglobal_load_dwordx4 v[112:115], %[off], %[base] sc1\n\ts_waitcnt vmcnt(7)\n\tv_xor_b32 %[t0], %[tag], v117\n\tv_xor_b32 %[t1], %[tag], v119\n\tv_or_b32 %[t0], %[t0], %[t1]\n\tv_cmp_eq_u32 vcc, 0, %[t0]\n\ts_mov_b64 exec, vcc\n\tv_mov_b32 %[x], v116\n\tv_mov_b32 %[y], v118\n\ts_andn2_b64 %[pm], %[pm], vcc\n\ts_cbranch_scc0 4f\n\ts_mov_b64 exec, %[pm]\n\tglobal_load_dwordx4 v[116:119], %[off], %[base] sc1\n\ts_waitcnt vmcnt(7)\n\tv_xor_b32 %[t0], %[tag], v121\n\tv_xor_b32 %[t1], %[tag], v123\n\tv_or_b32 %[t0], %[t0], %[t1]\n\tv_cmp_eq_u32 vcc, 0, %[t0]\n\ts_mov_b64 exec, vcc\n\tv_mov_b32 %[x], v120\n\tv_mov_b32 %[y], v122\n\ts_andn2_b64 %[pm], %[pm], vcc\n\ts_cbranch_scc0 4f\n\ts_mov_b64 exec, %[pm]\n\tglobal_load_dwordx4 v[120:123], %[off], %[base] sc1\n\ts_waitcnt vmcnt(7)\n\tv_xor_b32 %[t0], %[tag], v125\n\tv_xor_b32 %[t1], %[tag], v127\n\tv_or_b32 %[t0], %[t0], %[t1]\n\tv_cmp_eq_u32 vcc, 0, %[t0]\n\ts_mov_b64 exec, vcc\n\tv_mov_b32 %[x], v124\n\tv_mov_b32 %[y], v126\n\ts_andn2_b64 %[pm], %[pm], vcc\n\ts_cbranch_scc0 4f\n\ts_mov_b64 exec, %[pm]\n\tglobal_load_dwordx4 v[124:127], %[off], %[base] sc1\n\ts_sub_u32 %[cnt], %[cnt], 1\n\ts_cmp_lg_u32 %[cnt], 0\n\ts_cbranch_scc1 1b\n4:\n\ts_mov_b64 exec, -1"
               : [x] "+&v"(x), [y] "+&v"(y), [t0] "=&v"(t0), [t1] "=&v"(t1), [cnt] "+&s"(cnt), [pm] "=&s"(pm)
               : [off] "v"(off), [base] "s"(base), [tag] "s"(tag), [gap] "n"(GAP)
               : "memory", "vcc", "scc", "v96", "v97", "v98", "v99", "v100", "v101", "v102", "v103", "v104", "v105", "v106", "v107", "v108", "v109", "v110", "v111", "v112", "v113", "v114", "v115", "v116", "v117", "v118", "v119", "v120", "v121", "v122", "v123", "v124", "v125", "v126", "v127");
}
template <int GAP> __device__ __forceinline__ void pollm4_2(float (&x)[4], float (&y)[4], const unsigned char* base, unsigned off, unsigned tag) {
  unsigned t0, t1, cnt = SPIN_CAP;
  unsigned long long p[4], pa;
#pragma unroll
  for (int i = 0; i < 4; ++i) { x[i] = 0.f; y[i] = 0.f; }
  asm volatile("s_mov_b64 %[p0], -1\n\ts_mov_b64 %[p1], -1\n\ts_mov_b64 %[p2], -1\n\ts_mov_b64 %[p3], -1\n\tglobal_load_dwordx4 v[96:99], %[off], %[base] sc1\n\tglobal_load_dwordx4 v[100:103], %[off], %[base] offset:1024 sc1\n\tglobal_load_dwordx4 v[104:107], %[off], %[base] offset:2048 sc1\n\tglobal_load_dwordx4 v[108:111], %[off], %[base] offset:3072 sc1\n\ts_sleep %[gap]\n\tglobal_load_dwordx4 v[112:115], %[off], %[base] sc1\n\tglobal_load_dwordx4 v[116:119], %[off], %[base] offset:1024 sc1\n\tglobal_load_dwordx4 v[120:123], %[off], %[base] offset:2048 sc1\n\tglobal_load_dwordx4 v[124:127], %[off], %[base] offset:3072 sc1\n\t\n1:\n\ts_waitcnt vmcnt(4)\n\ts_mov_b64 exec, %[p0]\n\tv_xor_b32 %[t0], %[tag], v97\n\tv_xor_b32 %[t1], %[tag], v99\n\tv_or_b32 %[t0], %[t0], %[t1]\n\tv_cmp_eq_u32 vcc, 0, %[t0]\n\ts_mov_b64 exec, vcc\n\tv_mov_b32 %[x0], v96\n\tv_mov_b32 %[y0], v98\n\ts_andn2_b64 %[p0], %[p0], vcc\n\ts_mov_b64 exec, %[p1]\n\tv_xor_b32 %[t0], %[tag], v101\n\tv_xor_b32 %[t1], %[tag], v103\n\tv_or_b32 %[t0], %[t0], %[t1]\n\tv_cmp_eq_u32 vcc, 0, %[t0]\n\ts_mov_b64 exec, vcc\n\tv_mov_b32 %[x1], v100\n\tv_mov_b32 %[y1], v102\n\ts_andn2_b64 %[p1], %[p1], vcc\n\ts_mov_b64 exec, %[p2]\n\tv_xor_b32 %[t0], %[tag], v105\n\tv_xor_b32 %[t1], %[tag], v107\n\tv_or_b32 %[t0], %[t0], %[t1]\n\tv_cmp_eq_u32 vcc, 0, %[t0]\n\ts_mov_b64 exec, vcc\n\tv_mov_b32 %[x2], v104\n\tv_mov_b32 %[y2], v106\n\ts_andn2_b64 %[p2], %[p2], vcc\n\ts_mov_b64 exec, %[p3]\n\tv_xor_b32 %[t0], %[tag], v109\n\tv_xor_b32 %[t1], %[tag], v111\n\tv_or_b32 %[t0], %[t0], %[t1]\n\tv_cmp_eq_u32 vcc, 0, %[t0]\n\ts_mov_b64 exec, vcc\n\tv_mov_b32 %[x3], v108\n\tv_mov_b32 %[y3], v110\n\ts_andn2_b64 %[p3], %[p3], vcc\n\ts_or_b64 %[pa], %[p0], %[p1]\n\ts_or_b64 %[pa], %[pa], %[p2]\n\ts_or_b64 %[pa], %[pa], %[p3]\n\ts_cbranch_scc0 4f\n\ts_cmp_eq_u64 %[p0], 0\n\ts_cselect_b64 exec, 1, %[p0]\n\tglobal_load_dwordx4 v[96:99], %[off], %[base] sc1\n\ts_cmp_eq_u64 %[p1], 0\n\ts_cselect_b64 exec, 1, %[p1]\n\tglobal_load_dwordx4 v[100:103], %[off], %[base] offset:1024 sc1\n\ts_cmp_eq_u64 %[p2], 0\n\ts_cselect_b64 exec, 1, %[p2]\n\tglobal_load_dwordx4 v[104:107], %[off], %[base] offset:2048 sc1\n\ts_cmp_eq_u64 %[p3], 0\n\ts_cselect_b64 exec, 1, %[p3]\n\tglobal_load_dwordx4 v[108:111], %[off], %[base] offset:3072 sc1\n\ts_waitcnt vmcnt(4)\n\ts_mov_b64 exec, %[p0]\n\tv_xor_b32 %[t0], %[tag], v113\n\tv_xor_b32 %[t1], %[tag], v115\n\tv_or_b32 %[t0], %[t0], %[t1]\n\tv_cmp_eq_u32 vcc, 0, %[t0]\n\ts_mov_b64 exec, vcc\n\tv_mov_b32 %[x0], v112\n\tv_mov_b32 %[y0], v114\n\ts_andn2_b64 %[p0], %[p0], vcc\n\ts_mov_b64 exec, %[p1]\n\tv_xor_b32 %[t0], %[tag], v117\n\tv_xor_b32 %[t1], %[tag], v119\n\tv_or_b32 %[t0], %[t0], %[t1]\n\tv_cmp_eq_u32 vcc, 0, %[t0]\n\ts_mov_b64 exec, vcc\n\tv_mov_b32 %[x1], v116\n\tv_mov_b32 %[y1], v118\n\ts_andn2_b64 %[p1], %[p1], vcc\n\ts_mov_b64 exec, %[p2]\n\tv_xor_b32 %[t0], %[tag], v121\n\tv_xor_b32 %[t1], %[tag], v123\n\tv_or_b32 %[t0], %[t0], %[t1]\n\tv_cmp_eq_u32 vcc, 0, %[t0]\n\ts_mov_b64 exec, vcc\n\tv_mov_b32 %[x2], v120\n\tv_mov_b32 %[y2], v122\n\ts_andn2_b64 %[p2], %[p2], vcc\n\ts_mov_b64 exec, %[p3]\n\tv_xor_b32 %[t0], %[tag], v125\n\tv_xor_b32 %[t1], %[tag], v127\n\tv_or_b32 %[t0], %[t0], %[t1]\n\tv_cmp_eq_u32 vcc, 0, %[t0]\n\ts_mov_b64 exec, vcc\n\tv_mov_b32 %[x3], v124\n\tv_mov_b32 %[y3], v126\n\ts_andn2_b64 %[p3], %[p3], vcc\n\ts_or_b64 %[pa], %[p0], %[p1]\n\ts_or_b64 %[pa], %[pa], %[p2]\n\ts_or_b64 %[pa], %[pa], %[p3]\n\ts_cbranch_scc0 4f\n\ts_cmp_eq_u64 %[p0], 0\n\ts_cselect_b64 exec, 1, %[p0]\n\tglobal_load_dwordx4 v[112:115], %[off], %[base] sc1\n\ts_cmp_eq_u64 %[p1], 0\n\ts_cselect_b64 exec, 1, %[p1]\n\tglobal_load_dwordx4 v[116:119], %[off], %[base] offset:1024 sc1\n\ts_cmp_eq_u64 %[p2], 0\n\ts_cselect_b64 exec, 1, %[p2]\n\tglobal_load_dwordx4 v[120:123], %[off], %[base] offset:2048 sc1\n\ts_cmp_eq_u64 %[p3], 0\n\ts_cselect_b64 exec, 1, %[p3]\n\tglobal_load_dwordx4 v[124:127], %[off], %[base] offset:3072 sc1\n\ts_sub_u32 %[cnt], %[cnt], 1\n\ts_cmp_lg_u32 %[cnt], 0\n\ts_cbranch_scc1 1b\n4:\n\ts_mov_b64 exec, -1"
               : [x0] "+&v"(x[0]), [y0] "+&v"(y[0]), [x1] "+&v"(x[1]), [y1] "+&v"(y[1]), [x2] "+&v"(x[2]), [y2] "+&v"(y[2]), [x3] "+&v"(x[3]), [y3] "+&v"(y[3]), [t0] "=&v"(t0), [t1] "=&v"(t1), [cnt] "+&s"(cnt), [p0] "=&s"(p[0]), [p1] "=&s"(p[1]), [p2] "=&s"(p[2]), [p3] "=&s"(p[3]), [pa] "=&s"(pa)
               : [off] "v"(off), [base] "s"(base), [tag] "s"(tag), [gap] "n"(GAP)
               : "memory", "vcc", "scc", "v96", "v97", "v98", "v99", "v100", "v101", "v102", "v103", "v104", "v105", "v106", "v107", "v108", "v109", "v110", "v111", "v112", "v113", "v114", "v115", "v116", "v117", "v118", "v119", "v120", "v121", "v122", "v123", "v124", "v125", "v126", "v127");
}
template <int GAP> __device__ __forceinline__ void pollm4_3(float (&x)[4], float (&y)[4], const unsigned char* base, unsigned off, unsigned tag) {
  unsigned t0, t1, cnt = SPIN_CAP;
  unsigned long long p[4], pa;
#pragma unroll
  for (int i = 0; i < 4; ++i) { x[i] = 0.f; y[i] = 0.f; }
  asm volatile("s_mov_b64 %[p0], -1\n\ts_mov_b64 %[p1], -1\n\ts_mov_b64 %[p2], -1\n\ts_mov_b64 %[p3], -1\n\tglobal_load_dwordx4 v[80:83], %[off], %[base] sc1\n\tglobal_load_dwordx4 v[84:87], %[off], %[base] offset:1024 sc1\n\tglobal_load_dwordx4 v[88:91], %[off], %[base] offset:2048 sc1\n\tglobal_load_dwordx4 v[92:95], %[off], %[base] offset:3072 sc1\n\ts_sleep %[gap]\n\tglobal_load_dwordx4 v[96:99], %[off], %[base] sc1\n\tglobal_load_dwordx4 v[100:103], %[off], %[base] offset:1024 sc1\n\tglobal_load_dwordx4 v[104:107], %[off], %[base] offset:2048 sc1\n\tglobal_load_dwordx4 v[108:111], %[off], %[base] offset:3072 sc1\n\ts_sleep %[gap]\n\tglobal_load_dwordx4 v[112:115], %[off], %[base] sc1\n\tglobal_load_dwordx4 v[116:119], %[off], %[base] offset:1024 sc1\n\tglobal_load_dwordx4 v[120:123], %[off], %[base] offset:2048 sc1\n\tglobal_load_dwordx4 v[124:127], %[off], %[base] offset:3072 sc1\n\t\n1:\n\ts_waitcnt vmcnt(8)\n\ts_mov_b64 exec, %[p0]\n\tv_xor_b32 %[t0], %[tag], v81\n\tv_xor_b32 %[t1], %[tag], v83\n\tv_or_b32 %[t0], %[t0], %[t1]\n\tv_cmp_eq_u32 vcc, 0, %[t0]\n\ts_mov_b64 exec, vcc\n\tv_mov_b32 %[x0], v80\n\tv_mov_b32 %[y0], v82\n\ts_andn2_b64 %[p0], %[p0], vcc\n\ts_mov_b64 exec, %[p1]\n\tv_xor_b32 %[t0], %[tag], v85\n\tv_xor_b32 %[t1], %[tag], v87\n\tv_or_b32 %[t0], %[t0], %[t1]\n\tv_cmp_eq_u32 vcc, 0, %[t0]\n\ts_mov_b64 exec, vcc\n\tv_mov_b32 %[x1], v84\n\tv_mov_b32 %[y1], v86\n\ts_andn2_b64 %[p1], %[p1], vcc\n\ts_mov_b64 exec, %[p2]\n\tv_xor_b32 %[t0], %[tag], v89\n\tv_xor_b32 %[t1], %[tag], v91\n\tv_or_b32 %[t0], %[t0], %[t1]\n\tv_cmp_eq_u32 vcc, 0, %[t0]\n\ts_mov_b64 exec, vcc\n\tv_mov_b32 %[x2], v88\n\tv_mov_b32 %[y2], v90\n\ts_andn2_b64 %[p2], %[p2], vcc\n\ts_mov_b64 exec, %[p3]\n\tv_xor_b32 %[t0], %[tag], v93\n\tv_xor_b32 %[t1], %[tag], v95\n\tv_or_b32 %[t0], %[t0], %[t1]\n\tv_cmp_eq_u32 vcc, 0, %[t0]\n\ts_mov_b64 exec, vcc\n\tv_mov_b32 %[x3], v92\n\tv_mov_b32 %[y3], v94\n\ts_andn2_b64 %[p3], %[p3], vcc\n\ts_or_b64 %[pa], %[p0], %[p1]\n\ts_or_b64 %[pa], %[pa], %[p2]\n\ts_or_b64 %[pa], %[pa], %[p3]\n\ts_cbranch_scc0 4f\n\ts_cmp_eq_u64 %[p0], 0\n\ts_cselect_b64 exec, 1, %[p0]\n\tglobal_load_dwordx4 v[80:83], %[off], %[base] sc1\n\ts_cmp_eq_u64 %[p1], 0\n\ts_cselect_b64 exec, 1, %[p1]\n\tglobal_load_dwordx4 v[84:87], %[off], %[base] offset:1024 sc1\n\ts_cmp_eq_u64 %[p2], 0\n\ts_cselect_b64 exec, 1, %[p2]\n\tglobal_load_dwordx4 v[88:91], %[off], %[base] offset:2048 sc1\n\ts_cmp_eq_u64 %[p3], 0\n\ts_cselect_b64 exec, 1, %[p3]\n\tglobal_load_dwordx4 v[92:95], %[off], %[base] offset:3072 sc1\n\ts_waitcnt vmcnt(8)\n\ts_mov_b64 exec, %[p0]\n\tv_xor_b32 %[t0], %[tag], v97\n\tv_xor_b32 %[t1], %[tag], v99\n\tv_or_b32 %[t0], %[t0], %[t1]\n\tv_cmp_eq_u32 vcc, 0, %[t0]\n\ts_mov_b64 exec, vcc\n\tv_mov_b32 %[x0], v96\n\tv_mov_b32 %[y0], v98\n\ts_andn2_b64 %[p0], %[p0], vcc\n\ts_mov_b64 exec, %[p1]\n\tv_xor_b32 %[t0], %[tag], v101\n\tv_xor_b32 %[t1], %[tag], v103\n\tv_or_b32 %[t0], %[t0], %[t1]\n\tv_cmp_eq_u32 vcc, 0, %[t0]\n\ts_mov_b64 exec, vcc\n\tv_mov_b32 %[x1], v100\n\tv_mov_b32 %[y1], v102\n\ts_andn2_b64 %[p1], %[p1], vcc\n\ts_mov_b64 exec, %[p2]\n\tv_xor_b32 %[t0], %[tag], v105\n\tv_xor_b32 %[t1], %[tag], v107\n\tv_or_b32 %[t0], %[t0], %[t1]\n\tv_cmp_eq_u32 vcc, 0, %[t0]\n\ts_mov_b64 exec, vcc\n\tv_mov_b32 %[x2], v104\n\tv_mov_b32 %[y2], v106\n\ts_andn2_b64 %[p2], %[p2], vcc\n\ts_mov_b64 exec, %[p3]\n\tv_xor_b32 %[t0], %[tag], v109\n\tv_xor_b32 %[t1], %[tag], v111\n\tv_or_b32 %[t0], %[t0], %[t1]\n\tv_cmp_eq_u32 vcc, 0, %[t0]\n\ts_mov_b64 exec, vcc\n\tv_mov_b32 %[x3], v108\n\tv_mov_b32 %[y3], v110\n\ts_andn2_b64 %[p3], %[p3], vcc\n\ts_or_b64 %[pa], %[p0], %[p1]\n\ts_or_b64 %[pa], %[pa], %[p2]\n\ts_or_b64 %[pa], %[pa], %[p3]\n\ts_cbranch_scc0 4f\n\ts_cmp_eq_u64 %[p0], 0\n\ts_cselect_b64 exec, 1, %[p0]\n\tglobal_load_dwordx4 v[96:99], %[off], %[base] sc1\n\ts_cmp_eq_u64 %[p1], 0\n\ts_cselect_b64 exec, 1, %[p1]\n\tglobal_load_dwordx4 v[100:103], %[off], %[base] offset:1024 sc1\n\ts_cmp_eq_u64 %[p2], 0\n\ts_cselect_b64 exec, 1, %[p2]\n\tglobal_load_dwordx4 v[104:107], %[off], %[base] offset:2048 sc1\n\ts_cmp_eq_u64 %[p3], 0\n\ts_cselect_b64 exec, 1, %[p3]\n\tglobal_load_dwordx4 v[108:111], %[off], %[base] offset:3072 sc1\n\ts_waitcnt vmcnt(8)\n\ts_mov_b64 exec, %[p0]\n\tv_xor_b32 %[t0], %[tag], v113\n\tv_xor_b32 %[t1], %[tag], v115\n\tv_or_b32 %[t0], %[t0], %[t1]\n\tv_cmp_eq_u32 vcc, 0, %[t0]\n\ts_mov_b64 exec, vcc\n\tv_mov_b32 %[x0], v112\n\tv_mov_b32 %[y0], v114\n\ts_andn2_b64 %[p0], %[p0], vcc\n\ts_mov_b64 exec, %[p1]\n\tv_xor_b32 %[t0], %[tag], v117\n\tv_xor_b32 %[t1], %[tag], v119\n\tv_or_b32 %[t0], %[t0], %[t1]\n\tv_cmp_eq_u32 vcc, 0, %[t0]\n\ts_mov_b64 exec, vcc\n\tv_mov_b32 %[x1], v116\n\tv_mov_b32 %[y1], v118\n\ts_andn2_b64 %[p1], %[p1], vcc\n\ts_mov_b64 exec, %[p2]\n\tv_xor_b32 %[t0], %[tag], v121\n\tv_xor_b32 %[t1], %[tag], v123\n\tv_or_b32 %[t0], %[t0], %[t1]\n\tv_cmp_eq_u32 vcc, 0, %[t0]\n\ts_mov_b64 exec, vcc\n\tv_mov_b32 %[x2], v120\n\tv_mov_b32 %[y2], v122\n\ts_andn2_b64 %[p2], %[p2], vcc\n\ts_mov_b64 exec, %[p3]\n\tv_xor_b32 %[t0], %[tag], v125\n\tv_xor_b32 %[t1], %[tag], v127\n\tv_or_b32 %[t0], %[t0], %[t1]\n\tv_cmp_eq_u32 vcc, 0, %[t0]\n\ts_mov_b64 exec, vcc\n\tv_mov_b32 %[x3], v124\n\tv_mov_b32 %[y3], v126\n\ts_andn2_b64 %[p3], %[p3], vcc\n\ts_or_b64 %[pa], %[p0], %[p1]\n\ts_or_b64 %[pa], %[pa], %[p2]\n\ts_or_b64 %[pa], %[pa], %[p3]\n\ts_cbranch_scc0 4f\n\ts_cmp_eq_u64 %[p0], 0\n\ts_cselect_b64 exec, 1, %[p0]\n\tglobal_load_dwordx4 v[112:115], %[off], %[base] sc1\n\ts_cmp_eq_u64 %[p1], 0\n\ts_cselect_b64 exec, 1, %[p1]\n\tglobal_load_dwordx4 v[116:119], %[off], %[base] offset:1024 sc1\n\ts_cmp_eq_u64 %[p2], 0\n\ts_cselect_b64 exec, 1, %[p2]\n\tglobal_load_dwordx4 v[120:123], %[off], %[base] offset:2048 sc1\n\ts_cmp_eq_u64 %[p3], 0\n\ts_cselect_b64 exec, 1, %[p3]\n\tglobal_load_dwordx4 v[124:127], %[off], %[base] offset:3072 sc1\n\ts_sub_u32 %[cnt], %[cnt], 1\n\ts_cmp_lg_u32 %[cnt], 0\n\ts_cbranch_scc1 1b\n4:\n\ts_mov_b64 exec, -1"
               : [x0] "+&v"(x[0]), [y0] "+&v"(y[0]), [x1] "+&v"(x[1]), [y1] "+&v"(y[1]), [x2] "+&v"(x[2]), [y2] "+&v"(y[2]), [x3] "+&v"(x[3]), [y3] "+&v"(y[3]), [t0] "=&v"(t0), [t1] "=&v"(t1), [cnt] "+&s"(cnt), [p0] "=&s"(p[0]), [p1] "=&s"(p[1]), [p2] "=&s"(p[2]), [p3] "=&s"(p[3]), [pa] "=&s"(pa)
               : [off] "v"(off), [base] "s"(base), [tag] "s"(tag), [gap] "n"(GAP)
               : "memory", "vcc", "scc", "v80", "v81", "v82", "v83", "v84", "v85", "v86", "v87", "v88", "v89", "v90", "v91", "v92", "v93", "v94", "v95", "v96", "v97", "v98", "v99", "v100", "v101", "v102", "v103", "v104", "v105", "v106", "v107", "v108", "v109", "v110", "v111", "v112", "v113", "v114", "v115", "v116", "v117", "v118", "v119", "v120", "v121", "v122", "v123", "v124", "v125", "v126", "v127");
}
template <int GAP> __device__ __forceinline__ void pollm4_4(float (&x)[4], float (&y)[4], const unsigned char* base, unsigned off, unsigned tag) {
  unsigned t0, t1, cnt = SPIN_CAP;
  unsigned long long p[4], pa;
#pragma unroll
  for (int i = 0; i < 4; ++i) { x[i] = 0.f; y[i] = 0.f; }
  asm volatile("s_mov_b64 %[p0], -1\n\ts_mov_b64 %[p1], -1\n\ts_mov_b64 %[p2], -1\n\ts_mov_b64 %[p3], -1\n\tglobal_load_dwordx4 v[64:67], %[off], %[base] sc1\n\tglobal_load_dwordx4 v[68:71], %[off], %[base] offset:1024 sc1\n\tglobal_load_dwordx4 v[72:75], %[off], %[base] offset:2048 sc1\n\tglobal_load_dwordx4 v[76:79], %[off], %[base] offset:3072 sc1\n\ts_sleep %[gap]\n\tglobal_load_dwordx4 v[80:83], %[off], %[base] sc1\n\tglobal_load_dwordx4 v[84:87], %[off], %[base] offset:1024 sc1\n\tglobal_load_dwordx4 v[88:91], %[off], %[base] offset:2048 sc1\n\tglobal_load_dwordx4 v[92:95], %[off], %[base] offset:3072 sc1\n\ts_sleep %[gap]\n\tglobal_load_dwordx4 v[96:99], %[off], %[base] sc1\n\tglobal_load_dwordx4 v[100:103], %[off], %[base] offset:1024 sc1\n\tglobal_load_dwordx4 v[104:107], %[off], %[base] offset:2048 sc1\n\tglobal_load_dwordx4 v[108:111], %[off], %[base] offset:3072 sc1\n\ts_sleep %[gap]\n\tglobal_load_dwordx4 v[112:115], %[off], %[base] sc1\n\tglobal_load_dwordx4 v[116:119], %[off], %[base] offset:1024 sc1\n\tglobal_load_dwordx4 v[120:123], %[off], %[base] offset:2048 sc1\n\tglobal_load_dwordx4 v[124:127], %[off], %[base] offset:3072 sc1\n\t\n1:\n\ts_waitcnt vmcnt(12)\n\ts_mov_b64 exec, %[p0]\n\tv_xor_b32 %[t0], %[tag], v65\n\tv_xor_b32 %[t1], %[tag], v67\n\tv_or_b32 %[t0], %[t0], %[t1]\n\tv_cmp_eq_u32 vcc, 0, %[t0]\n\ts_mov_b64 exec, vcc\n\tv_mov_b32 %[x0], v64\n\tv_mov_b32 %[y0], v66\n\ts_andn2_b64 %[p0], %[p0], vcc\n\ts_mov_b64 exec, %[p1]\n\tv_xor_b32 %[t0], %[tag], v69\n\tv_xor_b32 %[t1], %[tag], v71\n\tv_or_b32 %[t0], %[t0], %[t1]\n\tv_cmp_eq_u32 vcc, 0, %[t0]\n\ts_mov_b64 exec, vcc\n\tv_mov_b32 %[x1], v68\n\tv_mov_b32 %[y1], v70\n\ts_andn2_b64 %[p1], %[p1], vcc\n\ts_mov_b64 exec, %[p2]\n\tv_xor_b32 %[t0], %[tag], v73\n\tv_xor_b32 %[t1], %[tag], v75\n\tv_or_b32 %[t0], %[t0], %[t1]\n\tv_cmp_eq_u32 vcc, 0, %[t0]\n\ts_mov_b64 exec, vcc\n\tv_mov_b32 %[x2], v72\n\tv_mov_b32 %[y2], v74\n\ts_andn2_b64 %[p2], %[p2], vcc\n\ts_mov_b64 exec, %[p3]\n\tv_xor_b32 %[t0], %[tag], v77\n\tv_xor_b32 %[t1], %[tag], v79\n\tv_or_b32 %[t0], %[t0], %[t1]\n\tv_cmp_eq_u32 vcc, 0, %[t0]\n\ts_mov_b64 exec, vcc\n\tv_mov_b32 %[x3], v76\n\tv_mov_b32 %[y3], v78\n\ts_andn2_b64 %[p3], %[p3], vcc\n\ts_or_b64 %[pa], %[p0], %[p1]\n\ts_or_b64 %[pa], %[pa], %[p2]\n\ts_or_b64 %[pa], %[pa], %[p3]\n\ts_cbranch_scc0 4f\n\ts_cmp_eq_u64 %[p0], 0\n\ts_cselect_b64 exec, 1, %[p0]\n\tglobal_load_dwordx4 v[64:67], %[off], %[base] sc1\n\ts_cmp_eq_u64 %[p1], 0\n\ts_cselect_b64 exec, 1, %[p1]\n\tglobal_load_dwordx4 v[68:71], %[off], %[base] offset:1024 sc1\n\ts_cmp_eq_u64 %[p2], 0\n\ts_cselect_b64 exec, 1, %[p2]\n\tglobal_load_dwordx4 v[72:75], %[off], %[base] offset:2048 sc1\n\ts_cmp_eq_u64 %[p3], 0\n\ts_cselect_b64 exec, 1, %[p3]\n\tglobal_load_dwordx4 v[76:79], %[off], %[base] offset:3072 sc1\n\ts_waitcnt vmcnt(12)\n\ts_mov_b64 exec, %[p0]\n\tv_xor_b32 %[t0], %[tag], v81\n\tv_xor_b32 %[t1], %[tag], v83\n\tv_or_b32 %[t0], %[t0], %[t1]\n\tv_cmp_eq_u32 vcc, 0, %[t0]\n\ts_mov_b64 exec, vcc\n\tv_mov_b32 %[x0], v80\n\tv_mov_b32 %[y0], v82\n\ts_andn2_b64 %[p0], %[p0], vcc\n\ts_mov_b64 exec, %[p1]\n\tv_xor_b32 %[t0], %[tag], v85\n\tv_xor_b32 %[t1], %[tag], v87\n\tv_or_b32 %[t0], %[t0], %[t1]\n\tv_cmp_eq_u32 vcc, 0, %[t0]\n\ts_mov_b64 exec, vcc\n\tv_mov_b32 %[x1], v84\n\tv_mov_b32 %[y1], v86\n\ts_andn2_b64 %[p1], %[p1], vcc\n\ts_mov_b64 exec, %[p2]\n\tv_xor_b32 %[t0], %[tag], v89\n\tv_xor_b32 %[t1], %[tag], v91\n\tv_or_b32 %[t0], %[t0], %[t1]\n\tv_cmp_eq_u32 vcc, 0, %[t0]\n\ts_mov_b64 exec, vcc\n\tv_mov_b32 %[x2], v88\n\tv_mov_b32 %[y2], v90\n\ts_andn2_b64 %[p2], %[p2], vcc\n\ts_mov_b64 exec, %[p3]\n\tv_xor_b32 %[t0], %[tag], v93\n\tv_xor_b32 %[t1], %[tag], v95\n\tv_or_b32 %[t0], %[t0], %[t1]\n\tv_cmp_eq_u32 vcc, 0, %[t0]\n\ts_mov_b64 exec, vcc\n\tv_mov_b32 %[x3], v92\n\tv_mov_b32 %[y3], v94\n\ts_andn2_b64 %[p3], %[p3], vcc\n\ts_or_b64 %[pa], %[p0], %[p1]\n\ts_or_b64 %[pa], %[pa], %[p2]\n\ts_or_b64 %[pa], %[pa], %[p3]\n\ts_cbranch_scc0 4f\n\ts_cmp_eq_u64 %[p0], 0\n\ts_cselect_b64 exec, 1, %[p0]\n\tglobal_load_dwordx4 v[80:83], %[off], %[base] sc1\n\ts_cmp_eq_u64 %[p1], 0\n\ts_cselect_b64 exec, 1, %[p1]\n\tglobal_load_dwordx4 v[84:87], %[off], %[base] offset:1024 sc1\n\ts_cmp_eq_u64 %[p2], 0\n\ts_cselect_b64 exec, 1, %[p2]\n\tglobal_load_dwordx4 v[88:91], %[off], %[base] offset:2048 sc1\n\ts_cmp_eq_u64 %[p3], 0\n\ts_cselect_b64 exec, 1, %[p3]\n\tglobal_load_dwordx4 v[92:95], %[off], %[base] offset:3072 sc1\n\ts_waitcnt vmcnt(12)\n\ts_mov_b64 exec, %[p0]\n\tv_xor_b32 %[t0], %[tag], v97\n\tv_xor_b32 %[t1], %[tag], v99\n\tv_or_b32 %[t0], %[t0], %[t1]\n\tv_cmp_eq_u32 vcc, 0, %[t0]\n\ts_mov_b64 exec, vcc\n\tv_mov_b32 %[x0], v96\n\tv_mov_b32 %[y0], v98\n\ts_andn2_b64 %[p0], %[p0], vcc\n\ts_mov_b64 exec, %[p1]\n\tv_xor_b32 %[t0], %[tag], v101\n\tv_xor_b32 %[t1], %[tag], v103\n\tv_or_b32 %[t0], %[t0], %[t1]\n\tv_cmp_eq_u32 vcc, 0, %[t0]\n\ts_mov_b64 exec, vcc\n\tv_mov_b32 %[x1], v100\n\tv_mov_b32 %[y1], v102\n\ts_andn2_b64 %[p1], %[p1], vcc\n\ts_mov_b64 exec, %[p2]\n\tv_xor_b32 %[t0], %[tag], v105\n\tv_xor_b32 %[t1], %[tag], v107\n\tv_or_b32 %[t0], %[t0], %[t1]\n\tv_cmp_eq_u32 vcc, 0, %[t0]\n\ts_mov_b64 exec, vcc\n\tv_mov_b32 %[x2], v104\n\tv_mov_b32 %[y2], v106\n\ts_andn2_b64 %[p2], %[p2], vcc\n\ts_mov_b64 exec, %[p3]\n\tv_xor_b32 %[t0], %[tag], v109\n\tv_xor_b32 %[t1], %[tag], v111\n\tv_or_b32 %[t0], %[t0], %[t1]\n\tv_cmp_eq_u32 vcc, 0, %[t0]\n\ts_mov_b64 exec, vcc\n\tv_mov_b32 %[x3], v108\n\tv_mov_b32 %[y3], v110\n\ts_andn2_b64 %[p3], %[p3], vcc\n\ts_or_b64 %[pa], %[p0], %[p1]\n\ts_or_b64 %[pa], %[pa], %[p2]\n\ts_or_b64 %[pa], %[pa], %[p3]\n\ts_cbranch_scc0 4f\n\ts_cmp_eq_u64 %[p0], 0\n\ts_cselect_b64 exec, 1, %[p0]\n\tglobal_load_dwordx4 v[96:99], %[off], %[base] sc1\n\ts_cmp_eq_u64 %[p1], 0\n\ts_cselect_b64 exec, 1, %[p1]\n\tglobal_load_dwordx4 v[100:103], %[off], %[base] offset:1024 sc1\n\ts_cmp_eq_u64 %[p2], 0\n\ts_cselect_b64 exec, 1, %[p2]\n\tglobal_load_dwordx4 v[104:107], %[off], %[base] offset:2048 sc1\n\ts_cmp_eq_u64 %[p3], 0\n\ts_cselect_b64 exec, 1, %[p3]\n\tglobal_load_dwordx4 v[108:111], %[off], %[base] offset:3072 sc1\n\ts_waitcnt vmcnt(12)\n\ts_mov_b64 exec, %[p0]\n\tv_xor_b32 %[t0], %[tag], v113\n\tv_xor_b32 %[t1], %[tag], v115\n\tv_or_b32 %[t0], %[t0], %[t1]\n\tv_cmp_eq_u32 vcc, 0, %[t0]\n\ts_mov_b64 exec, vcc\n\tv_mov_b32 %[x0], v112\n\tv_mov_b32 %[y0], v114\n\ts_andn2_b64 %[p0], %[p0], vcc\n\ts_mov_b64 exec, %[p1]\n\tv_xor_b32 %[t0], %[tag], v117\n\tv_xor_b32 %[t1], %[tag], v119\n\tv_or_b32 %[t0], %[t0], %[t1]\n\tv_cmp_eq_u32 vcc, 0, %[t0]\n\ts_mov_b64 exec, vcc\n\tv_mov_b32 %[x1], v116\n\tv_mov_b32 %[y1], v118\n\ts_andn2_b64 %[p1], %[p1], vcc\n\ts_mov_b64 exec, %[p2]\n\tv_xor_b32 %[t0], %[tag], v121\n\tv_xor_b32 %[t1], %[tag], v123\n\tv_or_b32 %[t0], %[t0], %[t1]\n\tv_cmp_eq_u32 vcc, 0, %[t0]\n\ts_mov_b64 exec, vcc\n\tv_mov_b32 %[x2], v120\n\tv_mov_b32 %[y2], v122\n\ts_andn2_b64 %[p2], %[p2], vcc\n\ts_mov_b64 exec, %[p3]\n\tv_xor_b32 %[t0], %[tag], v125\n\tv_xor_b32 %[t1], %[tag], v127\n\tv_or_b32 %[t0], %[t0], %[t1]\n\tv_cmp_eq_u32 vcc, 0, %[t0]\n\ts_mov_b64 exec, vcc\n\tv_mov_b32 %[x3], v124\n\tv_mov_b32 %[y3], v126\n\ts_andn2_b64 %[p3], %[p3], vcc\n\ts_or_b64 %[pa], %[p0], %[p1]\n\ts_or_b64 %[pa], %[pa], %[p2]\n\ts_or_b64 %[pa], %[pa], %[p3]\n\ts_cbranch_scc0 4f\n\ts_cmp_eq_u64 %[p0], 0\n\ts_cselect_b64 exec, 1, %[p0]\n\tglobal_load_dwordx4 v[112:115], %[off], %[base] sc1\n\ts_cmp_eq_u64 %[p1], 0\n\ts_cselect_b64 exec, 1, %[p1]\n\tglobal_load_dwordx4 v[116:119], %[off], %[base] offset:1024 sc1\n\ts_cmp_eq_u64 %[p2], 0\n\ts_cselect_b64 exec, 1, %[p2]\n\tglobal_load_dwordx4 v[120:123], %[off], %[base] offset:2048 sc1\n\ts_cmp_eq_u64 %[p3], 0\n\ts_cselect_b64 exec, 1, %[p3]\n\tglobal_load_dwordx4 v[124:127], %[off], %[base] offset:3072 sc1\n\ts_sub_u32 %[cnt], %[cnt], 1\n\ts_cmp_lg_u32 %[cnt], 0\n\ts_cbranch_scc1 1b\n4:\n\ts_mov_b64 exec, -1"
               : [x0] "+&v"(x[0]), [y0] "+&v"(y[0]), [x1] "+&v"(x[1]), [y1] "+&v"(y[1]), [x2] "+&v"(x[2]), [y2] "+&v"(y[2]), [x3] "+&v"(x[3]), [y3] "+&v"(y[3]), [t0] "=&v"(t0), [t1] "=&v"(t1), [cnt] "+&s"(cnt), [p0] "=&s"(p[0]), [p1] "=&s"(p[1]), [p2] "=&s"(p[2]), [p3] "=&s"(p[3]), [pa] "=&s"(pa)
               : [off] "v"(off), [base] "s"(base), [tag] "s"(tag), [gap] "n"(GAP)
               : "memory", "vcc", "scc", "v64", "v65", "v66", "v67", "v68", "v69", "v70", "v71", "v72", "v73", "v74", "v75", "v76", "v77", "v78", "v79", "v80", "v81", "v82", "v83", "v84", "v85", "v86", "v87", "v88", "v89", "v90", "v91", "v92", "v93", "v94", "v95", "v96", "v97", "v98", "v99", "v100", "v101", "v102", "v103", "v104", "v105", "v106", "v107", "v108", "v109", "v110", "v111", "v112", "v113", "v114", "v115", "v116", "v117", "v118", "v119", "v120", "v121", "v122", "v123", "v124", "v125", "v126", "v127");
}
template <int GAP> __device__ __forceinline__ void pollm5_2(float (&x)[5], float (&y)[5], const unsigned char* base, unsigned off, unsigned off5, unsigned tag) {
  unsigned t0, t1, cnt = SPIN_CAP;
  unsigned long long p[5], pa;
#pragma unroll
  for (int i = 0; i < 5; ++i) { x[i] = 0.f; y[i] = 0.f; }
  asm volatile("s_mov_b64 %[p0], -1\n\ts_mov_b64 %[p1], -1\n\ts_mov_b64 %[p2], -1\n\ts_mov_b64 %[p3], -1\n\ts_mov_b64 %[p4], -1\n\tglobal_load_dwordx4 v[88:91], %[off], %[base] sc1\n\tglobal_load_dwordx4 v[92:95], %[off], %[base] offset:1024 sc1\n\tglobal_load_dwordx4 v[96:99], %[off], %[base] offset:2048 sc1\n\tglobal_load_dwordx4 v[100:103], %[off], %[base] offset:3072 sc1\n\tglobal_load_dwordx4 v[104:107], %[off5], %[base] sc1\n\ts_sleep %[gap]\n\tglobal_load_dwordx4 v[108:111], %[off], %[base] sc1\n\tglobal_load_dwordx4 v[112:115], %[off], %[base] offset:1024 sc1\n\tglobal_load_dwordx4 v[116:119], %[off], %[base] offset:2048 sc1\n\tglobal_load_dwordx4 v[120:123], %[off], %[base] offset:3072 sc1\n\tglobal_load_dwordx4 v[124:127], %[off5], %[base] sc1\n\t\n1:\n\ts_waitcnt vmcnt(5)\n\ts_mov_b64 exec, %[p0]\n\tv_xor_b32 %[t0], %[tag], v89\n\tv_xor_b32 %[t1], %[tag], v91\n\tv_or_b32 %[t0], %[t0], %[t1]\n\tv_cmp_eq_u32 vcc, 0, %[t0]\n\ts_mov_b64 exec, vcc\n\tv_mov_b32 %[x0], v88\n\tv_mov_b32 %[y0], v90\n\ts_andn2_b64 %[p0], %[p0], vcc\n\ts_mov_b64 exec, %[p1]\n\tv_xor_b32 %[t0], %[tag], v93\n\tv_xor_b32 %[t1], %[tag], v95\n\tv_or_b32 %[t0], %[t0], %[t1]\n\tv_cmp_eq_u32 vcc, 0, %[t0]\n\ts_mov_b64 exec, vcc\n\tv_mov_b32 %[x1], v92\n\tv_mov_b32 %[y1], v94\n\ts_andn2_b64 %[p1], %[p1], vcc\n\ts_mov_b64 exec, %[p2]\n\tv_xor_b32 %[t0], %[tag], v97\n\tv_xor_b32 %[t1], %[tag], v99\n\tv_or_b32 %[t0], %[t0], %[t1]\n\tv_cmp_eq_u32 vcc, 0, %[t0]\n\ts_mov_b64 exec, vcc\n\tv_mov_b32 %[x2], v96\n\tv_mov_b32 %[y2], v98\n\ts_andn2_b64 %[p2], %[p2], vcc\n\ts_mov_b64 exec, %[p3]\n\tv_xor_b32 %[t0], %[tag], v101\n\tv_xor_b32 %[t1], %[tag], v103\n\tv_or_b32 %[t0], %[t0], %[t1]\n\tv_cmp_eq_u32 vcc, 0, %[t0]\n\ts_mov_b64 exec, vcc\n\tv_mov_b32 %[x3], v100\n\tv_mov_b32 %[y3], v102\n\ts_andn2_b64 %[p3], %[p3], vcc\n\ts_mov_b64 exec, %[p4]\n\tv_xor_b32 %[t0], %[tag], v105\n\tv_xor_b32 %[t1], %[tag], v107\n\tv_or_b32 %[t0], %[t0], %[t1]\n\tv_cmp_eq_u32 vcc, 0, %[t0]\n\ts_mov_b64 exec, vcc\n\tv_mov_b32 %[x4], v104\n\tv_mov_b32 %[y4], v106\n\ts_andn2_b64 %[p4], %[p4], vcc\n\ts_or_b64 %[pa], %[p0], %[p1]\n\ts_or_b64 %[pa], %[pa], %[p2]\n\ts_or_b64 %[pa], %[pa], %[p3]\n\ts_or_b64 %[pa], %[pa], %[p4]\n\ts_cbranch_scc0 4f\n\ts_cmp_eq_u64 %[p0], 0\n\ts_cselect_b64 exec, 1, %[p0]\n\tglobal_load_dwordx4 v[88:91], %[off], %[base] sc1\n\ts_cmp_eq_u64 %[p1], 0\n\ts_cselect_b64 exec, 1, %[p1]\n\tglobal_load_dwordx4 v[92:95], %[off], %[base] offset:1024 sc1\n\ts_cmp_eq_u64 %[p2], 0\n\ts_cselect_b64 exec, 1, %[p2]\n\tglobal_load_dwordx4 v[96:99], %[off], %[base] offset:2048 sc1\n\ts_cmp_eq_u64 %[p3], 0\n\ts_cselect_b64 exec, 1, %[p3]\n\tglobal_load_dwordx4 v[100:103], %[off], %[base] offset:3072 sc1\n\ts_cmp_eq_u64 %[p4], 0\n\ts_cselect_b64 exec, 1, %[p4]\n\tglobal_load_dwordx4 v[104:107], %[off5], %[base] sc1\n\ts_waitcnt vmcnt(5)\n\ts_mov_b64 exec, %[p0]\n\tv_xor_b32 %[t0], %[tag], v109\n\tv_xor_b32 %[t1], %[tag], v111\n\tv_or_b32 %[t0], %[t0], %[t1]\n\tv_cmp_eq_u32 vcc, 0, %[t0]\n\ts_mov_b64 exec, vcc\n\tv_mov_b32 %[x0], v108\n\tv_mov_b32 %[y0], v110\n\ts_andn2_b64 %[p0], %[p0], vcc\n\ts_mov_b64 exec, %[p1]\n\tv_xor_b32 %[t0], %[tag], v113\n\tv_xor_b32 %[t1], %[tag], v115\n\tv_or_b32 %[t0], %[t0], %[t1]\n\tv_cmp_eq_u32 vcc, 0, %[t0]\n\ts_mov_b64 exec, vcc\n\tv_mov_b32 %[x1], v112\n\tv_mov_b32 %[y1], v114\n\ts_andn2_b64 %[p1], %[p1], vcc\n\ts_mov_b64 exec, %[p2]\n\tv_xor_b32 %[t0], %[tag], v117\n\tv_xor_b32 %[t1], %[tag], v119\n\tv_or_b32 %[t0], %[t0], %[t1]\n\tv_cmp_eq_u32 vcc, 0, %[t0]\n\ts_mov_b64 exec, vcc\n\tv_mov_b32 %[x2], v116\n\tv_mov_b32 %[y2], v118\n\ts_andn2_b64 %[p2], %[p2], vcc\n\ts_mov_b64 exec, %[p3]\n\tv_xor_b32 %[t0], %[tag], v121\n\tv_xor_b32 %[t1], %[tag], v123\n\tv_or_b32 %[t0], %[t0], %[t1]\n\tv_cmp_eq_u32 vcc, 0, %[t0]\n\ts_mov_b64 exec, vcc\n\tv_mov_b32 %[x3], v120\n\tv_mov_b32 %[y3], v122\n\ts_andn2_b64 %[p3], %[p3], vcc\n\ts_mov_b64 exec, %[p4]\n\tv_xor_b32 %[t0], %[tag], v125\n\tv_xor_b32 %[t1], %[tag], v127\n\tv_or_b32 %[t0], %[t0], %[t1]\n\tv_cmp_eq_u32 vcc, 0, %[t0]\n\ts_mov_b64 exec, vcc\n\tv_mov_b32 %[x4], v124\n\tv_mov_b32 %[y4], v126\n\ts_andn2_b64 %[p4], %[p4], vcc\n\ts_or_b64 %[pa], %[p0], %[p1]\n\ts_or_b64 %[pa], %[pa], %[p2]\n\ts_or_b64 %[pa], %[pa], %[p3]\n\ts_or_b64 %[pa], %[pa], %[p4]\n\ts_cbranch_scc0 4f\n\ts_cmp_eq_u64 %[p0], 0\n\ts_cselect_b64 exec, 1, %[p0]\n\tglobal_load_dwordx4 v[108:111], %[off], %[base] sc1\n\ts_cmp_eq_u64 %[p1], 0\n\ts_cselect_b64 exec, 1, %[p1]\n\tglobal_load_dwordx4 v[112:115], %[off], %[base] offset:1024 sc1\n\ts_cmp_eq_u64 %[p2], 0\n\ts_cselect_b64 exec, 1, %[p2]\n\tglobal_load_dwordx4 v[116:119], %[off], %[base] offset:2048 sc1\n\ts_cmp_eq_u64 %[p3], 0\n\ts_cselect_b64 exec, 1, %[p3]\n\tglobal_load_dwordx4 v[120:123], %[off], %[base] offset:3072 sc1\n\ts_cmp_eq_u64 %[p4], 0\n\ts_cselect_b64 exec, 1, %[p4]\n\tglobal_load_dwordx4 v[124:127], %[off5], %[base] sc1\n\ts_sub_u32 %[cnt], %[cnt], 1\n\ts_cmp_lg_u32 %[cnt], 0\n\ts_cbranch_scc1 1b\n4:\n\ts_mov_b64 exec, -1"
               : [x0] "+&v"(x[0]), [y0] "+&v"(y[0]), [x1] "+&v"(x[1]), [y1] "+&v"(y[1]), [x2] "+&v"(x[2]), [y2] "+&v"(y[2]), [x3] "+&v"(x[3]), [y3] "+&v"(y[3]), [x4] "+&v"(x[4]), [y4] "+&v"(y[4]), [t0] "=&v"(t0), [t1] "=&v"(t1), [cnt] "+&s"(cnt), [p0] "=&s"(p[0]), [p1] "=&s"(p[1]), [p2] "=&s"(p[2]), [p3] "=&s"(p[3]), [p4] "=&s"(p[4]), [pa] "=&s"(pa)
               : [off] "v"(off), [off5] "v"(off5), [base] "s"(base), [tag] "s"(tag), [gap] "n"(GAP)
               : "memory", "vcc", "scc", "v88", "v89", "v90", "v91", "v92", "v93", "v94", "v95", "v96", "v97", "v98", "v99", "v100", "v101", "v102", "v103", "v104", "v105", "v106", "v107", "v108", "v109", "v110", "v111", "v112", "v113", "v114", "v115", "v116", "v117", "v118", "v119", "v120", "v121", "v122", "v123", "v124", "v125", "v126", "v127");
}
template <int GAP> __device__ __forceinline__ void pollm5_3(float (&x)[5], float (&y)[5], const unsigned char* base, unsigned off, unsigned off5, unsigned tag) {
  unsigned t0, t1, cnt = SPIN_CAP;
  unsigned long long p[5], pa;
#pragma unroll
  for (int i = 0; i < 5; ++i) { x[i] = 0.f; y[i] = 0.f; }
  asm volatile("s_mov_b64 %[p0], -1\n\ts_mov_b64 %[p1], -1\n\ts_mov_b64 %[p2], -1\n\ts_mov_b64 %[p3], -1\n\ts_mov_b64 %[p4], -1\n\tglobal_load_dwordx4 v[68:71], %[off], %[base] sc1\n\tglobal_load_dwordx4 v[72:75], %[off], %[base] offset:1024 sc1\n\tglobal_load_dwordx4 v[76:79], %[off], %[base] offset:2048 sc1\n\tglobal_load_dwordx4 v[80:83], %[off], %[base] offset:3072 sc1\n\tglobal_load_dwordx4 v[84:87], %[off5], %[base] sc1\n\ts_sleep %[gap]\n\tglobal_load_dwordx4 v[88:91], %[off], %[base] sc1\n\tglobal_load_dwordx4 v[92:95], %[off], %[base] offset:1024 sc1\n\tglobal_load_dwordx4 v[96:99], %[off], %[base] offset:2048 sc1\n\tglobal_load_dwordx4 v[100:103], %[off], %[base] offset:3072 sc1\n\tglobal_load_dwordx4 v[104:107], %[off5], %[base] sc1\n\ts_sleep %[gap]\n\tglobal_load_dwordx4 v[108:111], %[off], %[base] sc1\n\tglobal_load_dwordx4 v[112:115], %[off], %[base] offset:1024 sc1\n\tglobal_load_dwordx4 v[116:119], %[off], %[base] offset:2048 sc1\n\tglobal_load_dwordx4 v[120:123], %[off], %[base] offset:3072 sc1\n\tglobal_load_dwordx4 v[124:127], %[off5], %[base] sc1\n\t\n1:\n\ts_waitcnt vmcnt(10)\n\ts_mov_b64 exec, %[p0]\n\tv_xor_b32 %[t0], %[tag], v69\n\tv_xor_b32 %[t1], %[tag], v71\n\tv_or_b32 %[t0], %[t0], %[t1]\n\tv_cmp_eq_u32 vcc, 0, %[t0]\n\ts_mov_b64 exec, vcc\n\tv_mov_b32 %[x0], v68\n\tv_mov_b32 %[y0], v70\n\ts_andn2_b64 %[p0], %[p0], vcc\n\ts_mov_b64 exec, %[p1]\n\tv_xor_b32 %[t0], %[tag], v73\n\tv_xor_b32 %[t1], %[tag], v75\n\tv_or_b32 %[t0], %[t0], %[t1]\n\tv_cmp_eq_u32 vcc, 0, %[t0]\n\ts_mov_b64 exec, vcc\n\tv_mov_b32 %[x1], v72\n\tv_mov_b32 %[y1], v74\n\ts_andn2_b64 %[p1], %[p1], vcc\n\ts_mov_b64 exec, %[p2]\n\tv_xor_b32 %[t0], %[tag], v77\n\tv_xor_b32 %[t1], %[tag], v79\n\tv_or_b32 %[t0], %[t0], %[t1]\n\tv_cmp_eq_u32 vcc, 0, %[t0]\n\ts_mov_b64 exec, vcc\n\tv_mov_b32 %[x2], v76\n\tv_mov_b32 %[y2], v78\n\ts_andn2_b64 %[p2], %[p2], vcc\n\ts_mov_b64 exec, %[p3]\n\tv_xor_b32 %[t0], %[tag], v81\n\tv_xor_b32 %[t1], %[tag], v83\n\tv_or_b32 %[t0], %[t0], %[t1]\n\tv_cmp_eq_u32 vcc, 0, %[t0]\n\ts_mov_b64 exec, vcc\n\tv_mov_b32 %[x3], v80\n\tv_mov_b32 %[y3], v82\n\ts_andn2_b64 %[p3], %[p3], vcc\n\ts_mov_b64 exec, %[p4]\n\tv_xor_b32 %[t0], %[tag], v85\n\tv_xor_b32 %[t1], %[tag], v87\n\tv_or_b32 %[t0], %[t0], %[t1]\n\tv_cmp_eq_u32 vcc, 0, %[t0]\n\ts_mov_b64 exec, vcc\n\tv_mov_b32 %[x4], v84\n\tv_mov_b32 %[y4], v86\n\ts_andn2_b64 %[p4], %[p4], vcc\n\ts_or_b64 %[pa], %[p0], %[p1]\n\ts_or_b64 %[pa], %[pa], %[p2]\n\ts_or_b64 %[pa], %[pa], %[p3]\n\ts_or_b64 %[pa], %[pa], %[p4]\n\ts_cbranch_scc0 4f\n\ts_cmp_eq_u64 %[p0], 0\n\ts_cselect_b64 exec, 1, %[p0]\n\tglobal_load_dwordx4 v[68:71], %[off], %[base] sc1\n\ts_cmp_eq_u64 %[p1], 0\n\ts_cselect_b64 exec, 1, %[p1]\n\tglobal_load_dwordx4 v[72:75], %[off], %[base] offset:1024 sc1\n\ts_cmp_eq_u64 %[p2], 0\n\ts_cselect_b64 exec, 1, %[p2]\n\tglobal_load_dwordx4 v[76:79], %[off], %[base] offset:2048 sc1\n\ts_cmp_eq_u64 %[p3], 0\n\ts_cselect_b64 exec, 1, %[p3]\n\tglobal_load_dwordx4 v[80:83], %[off], %[base] offset:3072 sc1\n\ts_cmp_eq_u64 %[p4], 0\n\ts_cselect_b64 exec, 1, %[p4]\n\tglobal_load_dwordx4 v[84:87], %[off5], %[base] sc1\n\ts_waitcnt vmcnt(10)\n\ts_mov_b64 exec, %[p0]\n\tv_xor_b32 %[t0], %[tag], v89\n\tv_xor_b32 %[t1], %[tag], v91\n\tv_or_b32 %[t0], %[t0], %[t1]\n\tv_cmp_eq_u32 vcc, 0, %[t0]\n\ts_mov_b64 exec, vcc\n\tv_mov_b32 %[x0], v88\n\tv_mov_b32 %[y0], v90\n\ts_andn2_b64 %[p0], %[p0], vcc\n\ts_mov_b64 exec, %[p1]\n\tv_xor_b32 %[t0], %[tag], v93\n\tv_xor_b32 %[t1], %[tag], v95\n\tv_or_b32 %[t0], %[t0], %[t1]\n\tv_cmp_eq_u32 vcc, 0, %[t0]\n\ts_mov_b64 exec, vcc\n\tv_mov_b32 %[x1], v92\n\tv_mov_b32 %[y1], v94\n\ts_andn2_b64 %[p1], %[p1], vcc\n\ts_mov_b64 exec, %[p2]\n\tv_xor_b32 %[t0], %[tag], v97\n\tv_xor_b32 %[t1], %[tag], v99\n\tv_or_b32 %[t0], %[t0], %[t1]\n\tv_cmp_eq_u32 vcc, 0, %[t0]\n\ts_mov_b64 exec, vcc\n\tv_mov_b32 %[x2], v96\n\tv_mov_b32 %[y2], v98\n\ts_andn2_b64 %[p2], %[p2], vcc\n\ts_mov_b64 exec, %[p3]\n\tv_xor_b32 %[t0], %[tag], v101\n\tv_xor_b32 %[t1], %[tag], v103\n\tv_or_b32 %[t0], %[t0], %[t1]\n\tv_cmp_eq_u32 vcc, 0, %[t0]\n\ts_mov_b64 exec, vcc\n\tv_mov_b32 %[x3], v100\n\tv_mov_b32 %[y3], v102\n\ts_andn2_b64 %[p3], %[p3], vcc\n\ts_mov_b64 exec, %[p4]\n\tv_xor_b32 %[t0], %[tag], v105\n\tv_xor_b32 %[t1], %[tag], v107\n\tv_or_b32 %[t0], %[t0], %[t1]\n\tv_cmp_eq_u32 vcc, 0, %[t0]\n\ts_mov_b64 exec, vcc\n\tv_mov_b32 %[x4], v104\n\tv_mov_b32 %[y4], v106\n\ts_andn2_b64 %[p4], %[p4], vcc\n\ts_or_b64 %[pa], %[p0], %[p1]\n\ts_or_b64 %[pa], %[pa], %[p2]\n\ts_or_b64 %[pa], %[pa], %[p3]\n\ts_or_b64 %[pa], %[pa], %[p4]\n\ts_cbranch_scc0 4f\n\ts_cmp_eq_u64 %[p0], 0\n\ts_cselect_b64 exec, 1, %[p0]\n\tglobal_load_dwordx4 v[88:91], %[off], %[base] sc1\n\ts_cmp_eq_u64 %[p1], 0\n\ts_cselect_b64 exec, 1, %[p1]\n\tglobal_load_dwordx4 v[92:95], %[off], %[base] offset:1024 sc1\n\ts_cmp_eq_u64 %[p2], 0\n\ts_cselect_b64 exec, 1, %[p2]\n\tglobal_load_dwordx4 v[96:99], %[off], %[base] offset:2048 sc1\n\ts_cmp_eq_u64 %[p3], 0\n\ts_cselect_b64 exec, 1, %[p3]\n\tglobal_load_dwordx4 v[100:103], %[off], %[base] offset:3072 sc1\n\ts_cmp_eq_u64 %[p4], 0\n\ts_cselect_b64 exec, 1, %[p4]\n\tglobal_load_dwordx4 v[104:107], %[off5], %[base] sc1\n\ts_waitcnt vmcnt(10)\n\ts_mov_b64 exec, %[p0]\n\tv_xor_b32 %[t0], %[tag], v109\n\tv_xor_b32 %[t1], %[tag], v111\n\tv_or_b32 %[t0], %[t0], %[t1]\n\tv_cmp_eq_u32 vcc, 0, %[t0]\n\ts_mov_b64 exec, vcc\n\tv_mov_b32 %[x0], v108\n\tv_mov_b32 %[y0], v110\n\ts_andn2_b64 %[p0], %[p0], vcc\n\ts_mov_b64 exec, %[p1]\n\tv_xor_b32 %[t0], %[tag], v113\n\tv_xor_b32 %[t1], %[tag], v115\n\tv_or_b32 %[t0], %[t0], %[t1]\n\tv_cmp_eq_u32 vcc, 0, %[t0]\n\ts_mov_b64 exec, vcc\n\tv_mov_b32 %[x1], v112\n\tv_mov_b32 %[y1], v114\n\ts_andn2_b64 %[p1], %[p1], vcc\n\ts_mov_b64 exec, %[p2]\n\tv_xor_b32 %[t0], %[tag], v117\n\tv_xor_b32 %[t1], %[tag], v119\n\tv_or_b32 %[t0], %[t0], %[t1]\n\tv_cmp_eq_u32 vcc, 0, %[t0]\n\ts_mov_b64 exec, vcc\n\tv_mov_b32 %[x2], v116\n\tv_mov_b32 %[y2], v118\n\ts_andn2_b64 %[p2], %[p2], vcc\n\ts_mov_b64 exec, %[p3]\n\tv_xor_b32 %[t0], %[tag], v121\n\tv_xor_b32 %[t1], %[tag], v123\n\tv_or_b32 %[t0], %[t0], %[t1]\n\tv_cmp_eq_u32 vcc, 0, %[t0]\n\ts_mov_b64 exec, vcc\n\tv_mov_b32 %[x3], v120\n\tv_mov_b32 %[y3], v122\n\ts_andn2_b64 %[p3], %[p3], vcc\n\ts_mov_b64 exec, %[p4]\n\tv_xor_b32 %[t0], %[tag], v125\n\tv_xor_b32 %[t1], %[tag], v127\n\tv_or_b32 %[t0], %[t0], %[t1]\n\tv_cmp_eq_u32 vcc, 0, %[t0]\n\ts_mov_b64 exec, vcc\n\tv_mov_b32 %[x4], v124\n\tv_mov_b32 %[y4], v126\n\ts_andn2_b64 %[p4], %[p4], vcc\n\ts_or_b64 %[pa], %[p0], %[p1]\n\ts_or_b64 %[pa], %[pa], %[p2]\n\ts_or_b64 %[pa], %[pa], %[p3]\n\ts_or_b64 %[pa], %[pa], %[p4]\n\ts_cbranch_scc0 4f\n\ts_cmp_eq_u64 %[p0], 0\n\ts_cselect_b64 exec, 1, %[p0]\n\tglobal_load_dwordx4 v[108:111], %[off], %[base] sc1\n\ts_cmp_eq_u64 %[p1], 0\n\ts_cselect_b64 exec, 1, %[p1]\n\tglobal_load_dwordx4 v[112:115], %[off], %[base] offset:1024 sc1\n\ts_cmp_eq_u64 %[p2], 0\n\ts_cselect_b64 exec, 1, %[p2]\n\tglobal_load_dwordx4 v[116:119], %[off], %[base] offset:2048 sc1\n\ts_cmp_eq_u64 %[p3], 0\n\ts_cselect_b64 exec, 1, %[p3]\n\tglobal_load_dwordx4 v[120:123], %[off], %[base] offset:3072 sc1\n\ts_cmp_eq_u64 %[p4], 0\n\ts_cselect_b64 exec, 1, %[p4]\n\tglobal_load_dwordx4 v[124:127], %[off5], %[base] sc1\n\ts_sub_u32 %[cnt], %[cnt], 1\n\ts_cmp_lg_u32 %[cnt], 0\n\ts_cbranch_scc1 1b\n4:\n\ts_mov_b64 exec, -1"
               : [x0] "+&v"(x[0]), [y0] "+&v"(y[0]), [x1] "+&v"(x[1]), [y1] "+&v"(y[1]), [x2] "+&v"(x[2]), [y2] "+&v"(y[2]), [x3] "+&v"(x[3]), [y3] "+&v"(y[3]), [x4] "+&v"(x[4]), [y4] "+&v"(y[4]), [t0] "=&v"(t0), [t1] "=&v"(t1), [cnt] "+&s"(cnt), [p0] "=&s"(p[0]), [p1] "=&s"(p[1]), [p2] "=&s"(p[2]), [p3] "=&s"(p[3]), [p4] "=&s"(p[4]), [pa] "=&s"(pa)
               : [off] "v"(off), [off5] "v"(off5), [base] "s"(base), [tag] "s"(tag), [gap] "n"(GAP)
               : "memory", "vcc", "scc", "v68", "v69", "v70", "v71", "v72", "v73", "v74", "v75", "v76", "v77", "v78", "v79", "v80", "v81", "v82", "v83", "v84", "v85", "v86", "v87", "v88", "v89", "v90", "v91", "v92", "v93", "v94", "v95", "v96", "v97", "v98", "v99", "v100", "v101", "v102", "v103", "v104", "v105", "v106", "v107", "v108", "v109", "v110", "v111", "v112", "v113", "v114", "v115", "v116", "v117", "v118", "v119", "v120", "v121", "v122", "v123", "v124", "v125", "v126", "v127");
}
template <int GAP> __device__ __forceinline__ void pollf4_1(float (&x)[4], float (&y)[4], const unsigned char* base, unsigned off, unsigned tag) {
  unsigned t0, t1, cnt = SPIN_CAP;
  unsigned long long p[4], pa;
#pragma unroll
  for (int i = 0; i < 4; ++i) { x[i] = 0.f; y[i] = 0.f; }
  asm volatile("s_mov_b64 %[p0], -1\n\ts_mov_b64 %[p1], -1\n\ts_mov_b64 %[p2], -1\n\ts_mov_b64 %[p3], -1\n\tglobal_load_dwordx4 v[112:115], %[off], %[base] sc1\n\tglobal_load_dwordx4 v[116:119], %[off], %[base] offset:1024 sc1\n\tglobal_load_dwordx4 v[120:123], %[off], %[base] offset:2048 sc1\n\tglobal_load_dwordx4 v[124:127], %[off], %[base] offset:3072 sc1\n\ts_waitcnt vmcnt(0)\n\ts_mov_b64 exec, %[p0]\n\tv_xor_b32 %[t0], %[tag], v113\n\tv_xor_b32 %[t1], %[tag], v115\n\tv_or_b32 %[t0], %[t0], %[t1]\n\tv_cmp_eq_u32 vcc, 0, %[t0]\n\ts_mov_b64 exec, vcc\n\tv_mov_b32 %[x0], v112\n\tv_mov_b32 %[y0], v114\n\ts_andn2_b64 %[p0], %[p0], vcc\n\ts_mov_b64 exec, %[p1]\n\tv_xor_b32 %[t0], %[tag], v117\n\tv_xor_b32 %[t1], %[tag], v119\n\tv_or_b32 %[t0], %[t0], %[t1]\n\tv_cmp_eq_u32 vcc, 0, %[t0]\n\ts_mov_b64 exec, vcc\n\tv_mov_b32 %[x1], v116\n\tv_mov_b32 %[y1], v118\n\ts_andn2_b64 %[p1], %[p1], vcc\n\ts_mov_b64 exec, %[p2]\n\tv_xor_b32 %[t0], %[tag], v121\n\tv_xor_b32 %[t1], %[tag], v123\n\tv_or_b32 %[t0], %[t0], %[t1]\n\tv_cmp_eq_u32 vcc, 0, %[t0]\n\ts_mov_b64 exec, vcc\n\tv_mov_b32 %[x2], v120\n\tv_mov_b32 %[y2], v122\n\ts_andn2_b64 %[p2], %[p2], vcc\n\ts_mov_b64 exec, %[p3]\n\tv_xor_b32 %[t0], %[tag], v125\n\tv_xor_b32 %[t1], %[tag], v127\n\tv_or_b32 %[t0], %[t0], %[t1]\n\tv_cmp_eq_u32 vcc, 0, %[t0]\n\ts_mov_b64 exec, vcc\n\tv_mov_b32 %[x3], v124\n\tv_mov_b32 %[y3], v126\n\ts_andn2_b64 %[p3], %[p3], vcc\n\ts_or_b64 %[pa], %[p0], %[p1]\n\ts_or_b64 %[pa], %[pa], %[p2]\n\ts_or_b64 %[pa], %[pa], %[p3]\n\ts_cbranch_scc0 4f\n\ts_cmp_eq_u64 %[p0], 0\n\ts_cselect_b64 exec, 1, %[p0]\n\tglobal_load_dwordx4 v[112:115], %[off], %[base] sc1\n\ts_cmp_eq_u64 %[p1], 0\n\ts_cselect_b64 exec, 1, %[p1]\n\tglobal_load_dwordx4 v[116:119], %[off], %[base] offset:1024 sc1\n\ts_cmp_eq_u64 %[p2], 0\n\ts_cselect_b64 exec, 1, %[p2]\n\tglobal_load_dwordx4 v[120:123], %[off], %[base] offset:2048 sc1\n\ts_cmp_eq_u64 %[p3], 0\n\ts_cselect_b64 exec, 1, %[p3]\n\tglobal_load_dwordx4 v[124:127], %[off], %[base] offset:3072 sc1\n\t\n1:\n\ts_waitcnt vmcnt(0)\n\ts_mov_b64 exec, %[p0]\n\tv_xor_b32 %[t0], %[tag], v113\n\tv_xor_b32 %[t1], %[tag], v115\n\tv_or_b32 %[t0], %[t0], %[t1]\n\tv_cmp_eq_u32 vcc, 0, %[t0]\n\ts_mov_b64 exec, vcc\n\tv_mov_b32 %[x0], v112\n\tv_mov_b32 %[y0], v114\n\ts_andn2_b64 %[p0], %[p0], vcc\n\ts_mov_b64 exec, %[p1]\n\tv_xor_b32 %[t0], %[tag], v117\n\tv_xor_b32 %[t1], %[tag], v119\n\tv_or_b32 %[t0], %[t0], %[t1]\n\tv_cmp_eq_u32 vcc, 0, %[t0]\n\ts_mov_b64 exec, vcc\n\tv_mov_b32 %[x1], v116\n\tv_mov_b32 %[y1], v118\n\ts_andn2_b64 %[p1], %[p1], vcc\n\ts_mov_b64 exec, %[p2]\n\tv_xor_b32 %[t0], %[tag], v121\n\tv_xor_b32 %[t1], %[tag], v123\n\tv_or_b32 %[t0], %[t0], %[t1]\n\tv_cmp_eq_u32 vcc, 0, %[t0]\n\ts_mov_b64 exec, vcc\n\tv_mov_b32 %[x2], v120\n\tv_mov_b32 %[y2], v122\n\ts_andn2_b64 %[p2], %[p2], vcc\n\ts_mov_b64 exec, %[p3]\n\tv_xor_b32 %[t0], %[tag], v125\n\tv_xor_b32 %[t1], %[tag], v127\n\tv_or_b32 %[t0], %[t0], %[t1]\n\tv_cmp_eq_u32 vcc, 0, %[t0]\n\ts_mov_b64 exec, vcc\n\tv_mov_b32 %[x3], v124\n\tv_mov_b32 %[y3], v126\n\ts_andn2_b64 %[p3], %[p3], vcc\n\ts_or_b64 %[pa], %[p0], %[p1]\n\ts_or_b64 %[pa], %[pa], %[p2]\n\ts_or_b64 %[pa], %[pa], %[p3]\n\ts_cbranch_scc0 4f\n\ts_cmp_eq_u64 %[p0], 0\n\ts_cselect_b64 exec, 1, %[p0]\n\tglobal_load_dwordx4 v[112:115], %[off], %[base] sc1\n\ts_cmp_eq_u64 %[p1], 0\n\ts_cselect_b64 exec, 1, %[p1]\n\tglobal_load_dwordx4 v[116:119], %[off], %[base] offset:1024 sc1\n\ts_cmp_eq_u64 %[p2], 0\n\ts_cselect_b64 exec, 1, %[p2]\n\tglobal_load_dwordx4 v[120:123], %[off], %[base] offset:2048 sc1\n\ts_cmp_eq_u64 %[p3], 0\n\ts_cselect_b64 exec, 1, %[p3]\n\tglobal_load_dwordx4 v[124:127], %[off], %[base] offset:3072 sc1\n\ts_sub_u32 %[cnt], %[cnt], 1\n\ts_cmp_lg_u32 %[cnt], 0\n\ts_cbranch_scc1 1b\n4:\n\ts_mov_b64 exec, -1"
               : [x0] "+&v"(x[0]), [y0] "+&v"(y[0]), [x1] "+&v"(x[1]), [y1] "+&v"(y[1]), [x2] "+&v"(x[2]), [y2] "+&v"(y[2]), [x3] "+&v"(x[3]), [y3] "+&v"(y[3]), [t0] "=&v"(t0), [t1] "=&v"(t1), [cnt] "+&s"(cnt), [p0] "=&s"(p[0]), [p1] "=&s"(p[1]), [p2] "=&s"(p[2]), [p3] "=&s"(p[3]), [pa] "=&s"(pa)
               : [off] "v"(off), [base] "s"(base), [tag] "s"(tag), [gap] "n"(GAP)
               : "memory", "vcc", "scc", "v112", "v113", "v114", "v115", "v116", "v117", "v118", "v119", "v120", "v121", "v122", "v123", "v124", "v125", "v126", "v127");
}
template <int GAP> __device__ __forceinline__ void pollf4_2(float (&x)[4], float (&y)[4], const unsigned char* base, unsigned off, unsigned tag) {
  unsigned t0, t1, cnt = SPIN_CAP;
  unsigned long long p[4], pa;
#pragma unroll
  for (int i = 0; i < 4; ++i) { x[i] = 0.f; y[i] = 0.f; }
  asm volatile("s_mov_b64 %[p0], -1\n\ts_mov_b64 %[p1], -1\n\ts_mov_b64 %[p2], -1\n\ts_mov_b64 %[p3], -1\n\tglobal_load_dwordx4 v[96:99], %[off], %[base] sc1\n\tglobal_load_dwordx4 v[100:103], %[off], %[base] offset:1024 sc1\n\tglobal_load_dwordx4 v[104:107], %[off], %[base] offset:2048 sc1\n\tglobal_load_dwordx4 v[108:111], %[off], %[base] offset:3072 sc1\n\ts_waitcnt vmcnt(0)\n\ts_mov_b64 exec, %[p0]\n\tv_xor_b32 %[t0], %[tag], v97\n\tv_xor_b32 %[t1], %[tag], v99\n\tv_or_b32 %[t0], %[t0], %[t1]\n\tv_cmp_eq_u32 vcc, 0, %[t0]\n\ts_mov_b64 exec, vcc\n\tv_mov_b32 %[x0], v96\n\tv_mov_b32 %[y0], v98\n\ts_andn2_b64 %[p0], %[p0], vcc\n\ts_mov_b64 exec, %[p1]\n\tv_xor_b32 %[t0], %[tag], v101\n\tv_xor_b32 %[t1], %[tag], v103\n\tv_or_b32 %[t0], %[t0], %[t1]\n\tv_cmp_eq_u32 vcc, 0, %[t0]\n\ts_mov_b64 exec, vcc\n\tv_mov_b32 %[x1], v100\n\tv_mov_b32 %[y1], v102\n\ts_andn2_b64 %[p1], %[p1], vcc\n\ts_mov_b64 exec, %[p2]\n\tv_xor_b32 %[t0], %[tag], v105\n\tv_xor_b32 %[t1], %[tag], v107\n\tv_or_b32 %[t0], %[t0], %[t1]\n\tv_cmp_eq_u32 vcc, 0, %[t0]\n\ts_mov_b64 exec, vcc\n\tv_mov_b32 %[x2], v104\n\tv_mov_b32 %[y2], v106\n\ts_andn2_b64 %[p2], %[p2], vcc\n\ts_mov_b64 exec, %[p3]\n\tv_xor_b32 %[t0], %[tag], v109\n\tv_xor_b32 %[t1], %[tag], v111\n\tv_or_b32 %[t0], %[t0], %[t1]\n\tv_cmp_eq_u32 vcc, 0, %[t0]\n\ts_mov_b64 exec, vcc\n\tv_mov_b32 %[x3], v108\n\tv_mov_b32 %[y3], v110\n\ts_andn2_b64 %[p3], %[p3], vcc\n\ts_or_b64 %[pa], %[p0], %[p1]\n\ts_or_b64 %[pa], %[pa], %[p2]\n\ts_or_b64 %[pa], %[pa], %[p3]\n\ts_cbranch_scc0 4f\n\ts_cmp_eq_u64 %[p0], 0\n\ts_cselect_b64 exec, 1, %[p0]\n\tglobal_load_dwordx4 v[96:99], %[off], %[base] sc1\n\ts_cmp_eq_u64 %[p1], 0\n\ts_cselect_b64 exec, 1, %[p1]\n\tglobal_load_dwordx4 v[100:103], %[off], %[base] offset:1024 sc1\n\ts_cmp_eq_u64 %[p2], 0\n\ts_cselect_b64 exec, 1, %[p2]\n\tglobal_load_dwordx4 v[104:107], %[off], %[base] offset:2048 sc1\n\ts_cmp_eq_u64 %[p3], 0\n\ts_cselect_b64 exec, 1, %[p3]\n\tglobal_load_dwordx4 v[108:111], %[off], %[base] offset:3072 sc1\n\ts_sleep %[gap]\n\ts_cmp_eq_u64 %[p0], 0\n\ts_cselect_b64 exec, 1, %[p0]\n\tglobal_load_dwordx4 v[112:115], %[off], %[base] sc1\n\ts_cmp_eq_u64 %[p1], 0\n\ts_cselect_b64 exec, 1, %[p1]\n\tglobal_load_dwordx4 v[116:119], %[off], %[base] offset:1024 sc1\n\ts_cmp_eq_u64 %[p2], 0\n\ts_cselect_b64 exec, 1, %[p2]\n\tglobal_load_dwordx4 v[120:123], %[off], %[base] offset:2048 sc1\n\ts_cmp_eq_u64 %[p3], 0\n\ts_cselect_b64 exec, 1, %[p3]\n\tglobal_load_dwordx4 v[124:127], %[off], %[base] offset:3072 sc1\n\t\n1:\n\ts_waitcnt vmcnt(4)\n\ts_mov_b64 exec, %[p0]\n\tv_xor_b32 %[t0], %[tag], v97\n\tv_xor_b32 %[t1], %[tag], v99\n\tv_or_b32 %[t0], %[t0], %[t1]\n\tv_cmp_eq_u32 vcc, 0, %[t0]\n\ts_mov_b64 exec, vcc\n\tv_mov_b32 %[x0], v96\n\tv_mov_b32 %[y0], v98\n\ts_andn2_b64 %[p0], %[p0], vcc\n\ts_mov_b64 exec, %[p1]\n\tv_xor_b32 %[t0], %[tag], v101\n\tv_xor_b32 %[t1], %[tag], v103\n\tv_or_b32 %[t0], %[t0], %[t1]\n\tv_cmp_eq_u32 vcc, 0, %[t0]\n\ts_mov_b64 exec, vcc\n\tv_mov_b32 %[x1], v100\n\tv_mov_b32 %[y1], v102\n\ts_andn2_b64 %[p1], %[p1], vcc\n\ts_mov_b64 exec, %[p2]\n\tv_xor_b32 %[t0], %[tag], v105\n\tv_xor_b32 %[t1], %[tag], v107\n\tv_or_b32 %[t0], %[t0], %[t1]\n\tv_cmp_eq_u32 vcc, 0, %[t0]\n\ts_mov_b64 exec, vcc\n\tv_mov_b32 %[x2], v104\n\tv_mov_b32 %[y2], v106\n\ts_andn2_b64 %[p2], %[p2], vcc\n\ts_mov_b64 exec, %[p3]\n\tv_xor_b32 %[t0], %[tag], v109\n\tv_xor_b32 %[t1], %[tag], v111\n\tv_or_b32 %[t0], %[t0], %[t1]\n\tv_cmp_eq_u32 vcc, 0, %[t0]\n\ts_mov_b64 exec, vcc\n\tv_mov_b32 %[x3], v108\n\tv_mov_b32 %[y3], v110\n\ts_andn2_b64 %[p3], %[p3], vcc\n\ts_or_b64 %[pa], %[p0], %[p1]\n\ts_or_b64 %[pa], %[pa], %[p2]\n\ts_or_b64 %[pa], %[pa], %[p3]\n\ts_cbranch_scc0 4f\n\ts_cmp_eq_u64 %[p0], 0\n\ts_cselect_b64 exec, 1, %[p0]\n\tglobal_load_dwordx4 v[96:99], %[off], %[base] sc1\n\ts_cmp_eq_u64 %[p1], 0\n\ts_cselect_b64 exec, 1, %[p1]\n\tglobal_load_dwordx4 v[100:103], %[off], %[base] offset:1024 sc1\n\ts_cmp_eq_u64 %[p2], 0\n\ts_cselect_b64 exec, 1, %[p2]\n\tglobal_load_dwordx4 v[104:107], %[off], %[base] offset:2048 sc1\n\ts_cmp_eq_u64 %[p3], 0\n\ts_cselect_b64 exec, 1, %[p3]\n\tglobal_load_dwordx4 v[108:111], %[off], %[base] offset:3072 sc1\n\ts_waitcnt vmcnt(4)\n\ts_mov_b64 exec, %[p0]\n\tv_xor_b32 %[t0], %[tag], v113\n\tv_xor_b32 %[t1], %[tag], v115\n\tv_or_b32 %[t0], %[t0], %[t1]\n\tv_cmp_eq_u32 vcc, 0, %[t0]\n\ts_mov_b64 exec, vcc\n\tv_mov_b32 %[x0], v112\n\tv_mov_b32 %[y0], v114\n\ts_andn2_b64 %[p0], %[p0], vcc\n\ts_mov_b64 exec, %[p1]\n\tv_xor_b32 %[t0], %[tag], v117\n\tv_xor_b32 %[t1], %[tag], v119\n\tv_or_b32 %[t0], %[t0], %[t1]\n\tv_cmp_eq_u32 vcc, 0, %[t0]\n\ts_mov_b64 exec, vcc\n\tv_mov_b32 %[x1], v116\n\tv_mov_b32 %[y1], v118\n\ts_andn2_b64 %[p1], %[p1], vcc\n\ts_mov_b64 exec, %[p2]\n\tv_xor_b32 %[t0], %[tag], v121\n\tv_xor_b32 %[t1], %[tag], v123\n\tv_or_b32 %[t0], %[t0], %[t1]\n\tv_cmp_eq_u32 vcc, 0, %[t0]\n\ts_mov_b64 exec, vcc\n\tv_mov_b32 %[x2], v120\n\tv_mov_b32 %[y2], v122\n\ts_andn2_b64 %[p2], %[p2], vcc\n\ts_mov_b64 exec, %[p3]\n\tv_xor_b32 %[t0], %[tag], v125\n\tv_xor_b32 %[t1], %[tag], v127\n\tv_or_b32 %[t0], %[t0], %[t1]\n\tv_cmp_eq_u32 vcc, 0, %[t0]\n\ts_mov_b64 exec, vcc\n\tv_mov_b32 %[x3], v124\n\tv_mov_b32 %[y3], v126\n\ts_andn2_b64 %[p3], %[p3], vcc\n\ts_or_b64 %[pa], %[p0], %[p1]\n\ts_or_b64 %[pa], %[pa], %[p2]\n\ts_or_b64 %[pa], %[pa], %[p3]\n\ts_cbranch_scc0 4f\n\ts_cmp_eq_u64 %[p0], 0\n\ts_cselect_b64 exec, 1, %[p0]\n\tglobal_load_dwordx4 v[112:115], %[off], %[base] sc1\n\ts_cmp_eq_u64 %[p1], 0\n\ts_cselect_b64 exec, 1, %[p1]\n\tglobal_load_dwordx4 v[116:119], %[off], %[base] offset:1024 sc1\n\ts_cmp_eq_u64 %[p2], 0\n\ts_cselect_b64 exec, 1, %[p2]\n\tglobal_load_dwordx4 v[120:123], %[off], %[base] offset:2048 sc1\n\ts_cmp_eq_u64 %[p3], 0\n\ts_cselect_b64 exec, 1, %[p3]\n\tglobal_load_dwordx4 v[124:127], %[off], %[base] offset:3072 sc1\n\ts_sub_u32 %[cnt], %[cnt], 1\n\ts_cmp_lg_u32 %[cnt], 0\n\ts_cbranch_scc1 1b\n4:\n\ts_mov_b64 exec, -1"
               : [x0] "+&v"(x[0]), [y0] "+&v"(y[0]), [x1] "+&v"(x[1]), [y1] "+&v"(y[1]), [x2] "+&v"(x[2]), [y2] "+&v"(y[2]), [x3] "+&v"(x[3]), [y3] "+&v"(y[3]), [t0] "=&v"(t0), [t1] "=&v"(t1), [cnt] "+&s"(cnt), [p0] "=&s"(p[0]), [p1] "=&s"(p[1]), [p2] "=&s"(p[2]), [p3] "=&s"(p[3]), [pa] "=&s"(pa)
               : [off] "v"(off), [base] "s"(base), [tag] "s"(tag), [gap] "n"(GAP)
               : "memory", "vcc", "scc", "v96", "v97", "v98", "v99", "v100", "v101", "v102", "v103", "v104", "v105", "v106", "v107", "v108", "v109", "v110", "v111", "v112", "v113", "v114", "v115", "v116", "v117", "v118", "v119", "v120", "v121", "v122", "v123", "v124", "v125", "v126", "v127");
}
template <int GAP> __device__ __forceinline__ void pollf4_3(float (&x)[4], float (&y)[4], const unsigned char* base, unsigned off, unsigned tag) {
  unsigned t0, t1, cnt = SPIN_CAP;
  unsigned long long p[4], pa;
#pragma unroll
  for (int i = 0; i < 4; ++i) { x[i] = 0.f; y[i] = 0.f; }
  asm volatile("s_mov_b64 %[p0], -1\n\ts_mov_b64 %[p1], -1\n\ts_mov_b64 %[p2], -1\n\ts_mov_b64 %[p3], -1\n\tglobal_load_dwordx4 v[80:83], %[off], %[base] sc1\n\tglobal_load_dwordx4 v[84:87], %[off], %[base] offset:1024 sc1\n\tglobal_load_dwordx4 v[88:91], %[off], %[base] offset:2048 sc1\n\tglobal_load_dwordx4 v[92:95], %[off], %[base] offset:3072 sc1\n\ts_waitcnt vmcnt(0)\n\ts_mov_b64 exec, %[p0]\n\tv_xor_b32 %[t0], %[tag], v81\n\tv_xor_b32 %[t1], %[tag], v83\n\tv_or_b32 %[t0], %[t0], %[t1]\n\tv_cmp_eq_u32 vcc, 0, %[t0]\n\ts_mov_b64 exec, vcc\n\tv_mov_b32 %[x0], v80\n\tv_mov_b32 %[y0], v82\n\ts_andn2_b64 %[p0], %[p0], vcc\n\ts_mov_b64 exec, %[p1]\n\tv_xor_b32 %[t0], %[tag], v85\n\tv_xor_b32 %[t1], %[tag], v87\n\tv_or_b32 %[t0], %[t0], %[t1]\n\tv_cmp_eq_u32 vcc, 0, %[t0]\n\ts_mov_b64 exec, vcc\n\tv_mov_b32 %[x1], v84\n\tv_mov_b32 %[y1], v86\n\ts_andn2_b64 %[p1], %[p1], vcc\n\ts_mov_b64 exec, %[p2]\n\tv_xor_b32 %[t0], %[tag], v89\n\tv_xor_b32 %[t1], %[tag], v91\n\tv_or_b32 %[t0], %[t0], %[t1]\n\tv_cmp_eq_u32 vcc, 0, %[t0]\n\ts_mov_b64 exec, vcc\n\tv_mov_b32 %[x2], v88\n\tv_mov_b32 %[y2], v90\n\ts_andn2_b64 %[p2], %[p2], vcc\n\ts_mov_b64 exec, %[p3]\n\tv_xor_b32 %[t0], %[tag], v93\n\tv_xor_b32 %[t1], %[tag], v95\n\tv_or_b32 %[t0], %[t0], %[t1]\n\tv_cmp_eq_u32 vcc, 0, %[t0]\n\ts_mov_b64 exec, vcc\n\tv_mov_b32 %[x3], v92\n\tv_mov_b32 %[y3], v94\n\ts_andn2_b64 %[p3], %[p3], vcc\n\ts_or_b64 %[pa], %[p0], %[p1]\n\ts_or_b64 %[pa], %[pa], %[p2]\n\ts_or_b64 %[pa], %[pa], %[p3]\n\ts_cbranch_scc0 4f\n\ts_cmp_eq_u64 %[p0], 0\n\ts_cselect_b64 exec, 1, %[p0]\n\tglobal_load_dwordx4 v[80:83], %[off], %[base] sc1\n\ts_cmp_eq_u64 %[p1], 0\n\ts_cselect_b64 exec, 1, %[p1]\n\tglobal_load_dwordx4 v[84:87], %[off], %[base] offset:1024 sc1\n\ts_cmp_eq_u64 %[p2], 0\n\ts_cselect_b64 exec, 1, %[p2]\n\tglobal_load_dwordx4 v[88:91], %[off], %[base] offset:2048 sc1\n\ts_cmp_eq_u64 %[p3], 0\n\ts_cselect_b64 exec, 1, %[p3]\n\tglobal_load_dwordx4 v[92:95], %[off], %[base] offset:3072 sc1\n\ts_sleep %[gap]\n\ts_cmp_eq_u64 %[p0], 0\n\ts_cselect_b64 exec, 1, %[p0]\n\tglobal_load_dwordx4 v[96:99], %[off], %[base] sc1\n\ts_cmp_eq_u64 %[p1], 0\n\ts_cselect_b64 exec, 1, %[p1]\n\tglobal_load_dwordx4 v[100:103], %[off], %[base] offset:1024 sc1\n\ts_cmp_eq_u64 %[p2], 0\n\ts_cselect_b64 exec, 1, %[p2]\n\tglobal_load_dwordx4 v[104:107], %[off], %[base] offset:2048 sc1\n\ts_cmp_eq_u64 %[p3], 0\n\ts_cselect_b64 exec, 1, %[p3]\n\tglobal_load_dwordx4 v[108:111], %[off], %[base] offset:3072 sc1\n\ts_sleep %[gap]\n\ts_cmp_eq_u64 %[p0], 0\n\ts_cselect_b64 exec, 1, %[p0]\n\tglobal_load_dwordx4 v[112:115], %[off], %[base] sc1\n\ts_cmp_eq_u64 %[p1], 0\n\ts_cselect_b64 exec, 1, %[p1]\n\tglobal_load_dwordx4 v[116:119], %[off], %[base] offset:1024 sc1\n\ts_cmp_eq_u64 %[p2], 0\n\ts_cselect_b64 exec, 1, %[p2]\n\tglobal_load_dwordx4 v[120:123], %[off], %[base] offset:2048 sc1\n\ts_cmp_eq_u64 %[p3], 0\n\ts_cselect_b64 exec, 1, %[p3]\n\tglobal_load_dwordx4 v[124:127], %[off], %[base] offset:3072 sc1\n\t\n1:\n\ts_waitcnt vmcnt(8)\n\ts_mov_b64 exec, %[p0]\n\tv_xor_b32 %[t0], %[tag], v81\n\tv_xor_b32 %[t1], %[tag], v83\n\tv_or_b32 %[t0], %[t0], %[t1]\n\tv_cmp_eq_u32 vcc, 0, %[t0]\n\ts_mov_b64 exec, vcc\n\tv_mov_b32 %[x0], v80\n\tv_mov_b32 %[y0], v82\n\ts_andn2_b64 %[p0], %[p0], vcc\n\ts_mov_b64 exec, %[p1]\n\tv_xor_b32 %[t0], %[tag], v85\n\tv_xor_b32 %[t1], %[tag], v87\n\tv_or_b32 %[t0], %[t0], %[t1]\n\tv_cmp_eq_u32 vcc, 0, %[t0]\n\ts_mov_b64 exec, vcc\n\tv_mov_b32 %[x1], v84\n\tv_mov_b32 %[y1], v86\n\ts_andn2_b64 %[p1], %[p1], vcc\n\ts_mov_b64 exec, %[p2]\n\tv_xor_b32 %[t0], %[tag], v89\n\tv_xor_b32 %[t1], %[tag], v91\n\tv_or_b32 %[t0], %[t0], %[t1]\n\tv_cmp_eq_u32 vcc, 0, %[t0]\n\ts_mov_b64 exec, vcc\n\tv_mov_b32 %[x2], v88\n\tv_mov_b32 %[y2], v90\n\ts_andn2_b64 %[p2], %[p2], vcc\n\ts_mov_b64 exec, %[p3]\n\tv_xor_b32 %[t0], %[tag], v93\n\tv_xor_b32 %[t1], %[tag], v95\n\tv_or_b32 %[t0], %[t0], %[t1]\n\tv_cmp_eq_u32 vcc, 0, %[t0]\n\ts_mov_b64 exec, vcc\n\tv_mov_b32 %[x3], v92\n\tv_mov_b32 %[y3], v94\n\ts_andn2_b64 %[p3], %[p3], vcc\n\ts_or_b64 %[pa], %[p0], %[p1]\n\ts_or_b64 %[pa], %[pa], %[p2]\n\ts_or_b64 %[pa], %[pa], %[p3]\n\ts_cbranch_scc0 4f\n\ts_cmp_eq_u64 %[p0], 0\n\ts_cselect_b64 exec, 1, %[p0]\n\tglobal_load_dwordx4 v[80:83], %[off], %[base] sc1\n\ts_cmp_eq_u64 %[p1], 0\n\ts_cselect_b64 exec, 1, %[p1]\n\tglobal_load_dwordx4 v[84:87], %[off], %[base] offset:1024 sc1\n\ts_cmp_eq_u64 %[p2], 0\n\ts_cselect_b64 exec, 1, %[p2]\n\tglobal_load_dwordx4 v[88:91], %[off], %[base] offset:2048 sc1\n\ts_cmp_eq_u64 %[p3], 0\n\ts_cselect_b64 exec, 1, %[p3]\n\tglobal_load_dwordx4 v[92:95], %[off], %[base] offset:3072 sc1\n\ts_waitcnt vmcnt(8)\n\ts_mov_b64 exec, %[p0]\n\tv_xor_b32 %[t0], %[tag], v97\n\tv_xor_b32 %[t1], %[tag], v99\n\tv_or_b32 %[t0], %[t0], %[t1]\n\tv_cmp_eq_u32 vcc, 0, %[t0]\n\ts_mov_b64 exec, vcc\n\tv_mov_b32 %[x0], v96\n\tv_mov_b32 %[y0], v98\n\ts_andn2_b64 %[p0], %[p0], vcc\n\ts_mov_b64 exec, %[p1]\n\tv_xor_b32 %[t0], %[tag], v101\n\tv_xor_b32 %[t1], %[tag], v103\n\tv_or_b32 %[t0], %[t0], %[t1]\n\tv_cmp_eq_u32 vcc, 0, %[t0]\n\ts_mov_b64 exec, vcc\n\tv_mov_b32 %[x1], v100\n\tv_mov_b32 %[y1], v102\n\ts_andn2_b64 %[p1], %[p1], vcc\n\ts_mov_b64 exec, %[p2]\n\tv_xor_b32 %[t0], %[tag], v105\n\tv_xor_b32 %[t1], %[tag], v107\n\tv_or_b32 %[t0], %[t0], %[t1]\n\tv_cmp_eq_u32 vcc, 0, %[t0]\n\ts_mov_b64 exec, vcc\n\tv_mov_b32 %[x2], v104\n\tv_mov_b32 %[y2], v106\n\ts_andn2_b64 %[p2], %[p2], vcc\n\ts_mov_b64 exec, %[p3]\n\tv_xor_b32 %[t0], %[tag], v109\n\tv_xor_b32 %[t1], %[tag], v111\n\tv_or_b32 %[t0], %[t0], %[t1]\n\tv_cmp_eq_u32 vcc, 0, %[t0]\n\ts_mov_b64 exec, vcc\n\tv_mov_b32 %[x3], v108\n\tv_mov_b32 %[y3], v110\n\ts_andn2_b64 %[p3], %[p3], vcc\n\ts_or_b64 %[pa], %[p0], %[p1]\n\ts_or_b64 %[pa], %[pa], %[p2]\n\ts_or_b64 %[pa], %[pa], %[p3]\n\ts_cbranch_scc0 4f\n\ts_cmp_eq_u64 %[p0], 0\n\ts_cselect_b64 exec, 1, %[p0]\n\tglobal_load_dwordx4 v[96:99], %[off], %[base] sc1\n\ts_cmp_eq_u64 %[p1], 0\n\ts_cselect_b64 exec, 1, %[p1]\n\tglobal_load_dwordx4 v[100:103], %[off], %[base] offset:1024 sc1\n\ts_cmp_eq_u64 %[p2], 0\n\ts_cselect_b64 exec, 1, %[p2]\n\tglobal_load_dwordx4 v[104:107], %[off], %[base] offset:2048 sc1\n\ts_cmp_eq_u64 %[p3], 0\n\ts_cselect_b64 exec, 1, %[p3]\n\tglobal_load_dwordx4 v[108:111], %[off], %[base] offset:3072 sc1\n\ts_waitcnt vmcnt(8)\n\ts_mov_b64 exec, %[p0]\n\tv_xor_b32 %[t0], %[tag], v113\n\tv_xor_b32 %[t1], %[tag], v115\n\tv_or_b32 %[t0], %[t0], %[t1]\n\tv_cmp_eq_u32 vcc, 0, %[t0]\n\ts_mov_b64 exec, vcc\n\tv_mov_b32 %[x0], v112\n\tv_mov_b32 %[y0], v114\n\ts_andn2_b64 %[p0], %[p0], vcc\n\ts_mov_b64 exec, %[p1]\n\tv_xor_b32 %[t0], %[tag], v117\n\tv_xor_b32 %[t1], %[tag], v119\n\tv_or_b32 %[t0], %[t0], %[t1]\n\tv_cmp_eq_u32 vcc, 0, %[t0]\n\ts_mov_b64 exec, vcc\n\tv_mov_b32 %[x1], v116\n\tv_mov_b32 %[y1], v118\n\ts_andn2_b64 %[p1], %[p1], vcc\n\ts_mov_b64 exec, %[p2]\n\tv_xor_b32 %[t0], %[tag], v121\n\tv_xor_b32 %[t1], %[tag], v123\n\tv_or_b32 %[t0], %[t0], %[t1]\n\tv_cmp_eq_u32 vcc, 0, %[t0]\n\ts_mov_b64 exec, vcc\n\tv_mov_b32 %[x2], v120\n\tv_mov_b32 %[y2], v122\n\ts_andn2_b64 %[p2], %[p2], vcc\n\ts_mov_b64 exec, %[p3]\n\tv_xor_b32 %[t0], %[tag], v125\n\tv_xor_b32 %[t1], %[tag], v127\n\tv_or_b32 %[t0], %[t0], %[t1]\n\tv_cmp_eq_u32 vcc, 0, %[t0]\n\ts_mov_b64 exec, vcc\n\tv_mov_b32 %[x3], v124\n\tv_mov_b32 %[y3], v126\n\ts_andn2_b64 %[p3], %[p3], vcc\n\ts_or_b64 %[pa], %[p0], %[p1]\n\ts_or_b64 %[pa], %[pa], %[p2]\n\ts_or_b64 %[pa], %[pa], %[p3]\n\ts_cbranch_scc0 4f\n\ts_cmp_eq_u64 %[p0], 0\n\ts_cselect_b64 exec, 1, %[p0]\n\tglobal_load_dwordx4 v[112:115], %[off], %[base] sc1\n\ts_cmp_eq_u64 %[p1], 0\n\ts_cselect_b64 exec, 1, %[p1]\n\tglobal_load_dwordx4 v[116:119], %[off], %[base] offset:1024 sc1\n\ts_cmp_eq_u64 %[p2], 0\n\ts_cselect_b64 exec, 1, %[p2]\n\tglobal_load_dwordx4 v[120:123], %[off], %[base] offset:2048 sc1\n\ts_cmp_eq_u64 %[p3], 0\n\ts_cselect_b64 exec, 1, %[p3]\n\tglobal_load_dwordx4 v[124:127], %[off], %[base] offset:3072 sc1\n\ts_sub_u32 %[cnt], %[cnt], 1\n\ts_cmp_lg_u32 %[cnt], 0\n\ts_cbranch_scc1 1b\n4:\n\ts_mov_b64 exec, -1"
               : [x0] "+&v"(x[0]), [y0] "+&v"(y[0]), [x1] "+&v"(x[1]), [y1] "+&v"(y[1]), [x2] "+&v"(x[2]), [y2] "+&v"(y[2]), [x3] "+&v"(x[3]), [y3] "+&v"(y[3]), [t0] "=&v"(t0), [t1] "=&v"(t1), [cnt] "+&s"(cnt), [p0] "=&s"(p[0]), [p1] "=&s"(p[1]), [p2] "=&s"(p[2]), [p3] "=&s"(p[3]), [pa] "=&s"(pa)
               : [off] "v"(off), [base] "s"(base), [tag] "s"(tag), [gap] "n"(GAP)
               : "memory", "vcc", "scc", "v80", "v81", "v82", "v83", "v84", "v85", "v86", "v87", "v88", "v89", "v90", "v91", "v92", "v93", "v94", "v95", "v96", "v97", "v98", "v99", "v100", "v101", "v102", "v103", "v104", "v105", "v106", "v107", "v108", "v109", "v110", "v111", "v112", "v113", "v114", "v115", "v116", "v117", "v118", "v119", "v120", "v121", "v122", "v123", "v124", "v125", "v126", "v127");
}
template <int GAP> __device__ __forceinline__ void pollf4_4(float (&x)[4], float (&y)[4], const unsigned char* base, unsigned off, unsigned tag) {
  unsigned t0, t1, cnt = SPIN_CAP;
  unsigned long long p[4], pa;
#pragma unroll
  for (int i = 0; i < 4; ++i) { x[i] = 0.f; y[i] = 0.f; }
  asm volatile("s_mov_b64 %[p0], -1\n\ts_mov_b64 %[p1], -1\n\ts_mov_b64 %[p2], -1\n\ts_mov_b64 %[p3], -1\n\tglobal_load_dwordx4 v[64:67], %[off], %[base] sc1\n\tglobal_load_dwordx4 v[68:71], %[off], %[base] offset:1024 sc1\n\tglobal_load_dwordx4 v[72:75], %[off], %[base] offset:2048 sc1\n\tglobal_load_dwordx4 v[76:79], %[off], %[base] offset:3072 sc1\n\ts_waitcnt vmcnt(0)\n\ts_mov_b64 exec, %[p0]\n\tv_xor_b32 %[t0], %[tag], v65\n\tv_xor_b32 %[t1], %[tag], v67\n\tv_or_b32 %[t0], %[t0], %[t1]\n\tv_cmp_eq_u32 vcc, 0, %[t0]\n\ts_mov_b64 exec, vcc\n\tv_mov_b32 %[x0], v64\n\tv_mov_b32 %[y0], v66\n\ts_andn2_b64 %[p0], %[p0], vcc\n\ts_mov_b64 exec, %[p1]\n\tv_xor_b32 %[t0], %[tag], v69\n\tv_xor_b32 %[t1], %[tag], v71\n\tv_or_b32 %[t0], %[t0], %[t1]\n\tv_cmp_eq_u32 vcc, 0, %[t0]\n\ts_mov_b64 exec, vcc\n\tv_mov_b32 %[x1], v68\n\tv_mov_b32 %[y1], v70\n\ts_andn2_b64 %[p1], %[p1], vcc\n\ts_mov_b64 exec, %[p2]\n\tv_xor_b32 %[t0], %[tag], v73\n\tv_xor_b32 %[t1], %[tag], v75\n\tv_or_b32 %[t0], %[t0], %[t1]\n\tv_cmp_eq_u32 vcc, 0, %[t0]\n\ts_mov_b64 exec, vcc\n\tv_mov_b32 %[x2], v72\n\tv_mov_b32 %[y2], v74\n\ts_andn2_b64 %[p2], %[p2], vcc\n\ts_mov_b64 exec, %[p3]\n\tv_xor_b32 %[t0], %[tag], v77\n\tv_xor_b32 %[t1], %[tag], v79\n\tv_or_b32 %[t0], %[t0], %[t1]\n\tv_cmp_eq_u32 vcc, 0, %[t0]\n\ts_mov_b64 exec, vcc\n\tv_mov_b32 %[x3], v76\n\tv_mov_b32 %[y3], v78\n\ts_andn2_b64 %[p3], %[p3], vcc\n\ts_or_b64 %[pa], %[p0], %[p1]\n\ts_or_b64 %[pa], %[pa], %[p2]\n\ts_or_b64 %[pa], %[pa], %[p3]\n\ts_cbranch_scc0 4f\n\ts_cmp_eq_u64 %[p0], 0\n\ts_cselect_b64 exec, 1, %[p0]\n\tglobal_load_dwordx4 v[64:67], %[off], %[base] sc1\n\ts_cmp_eq_u64 %[p1], 0\n\ts_cselect_b64 exec, 1, %[p1]\n\tglobal_load_dwordx4 v[68:71], %[off], %[base] offset:1024 sc1\n\ts_cmp_eq_u64 %[p2], 0\n\ts_cselect_b64 exec, 1, %[p2]\n\tglobal_load_dwordx4 v[72:75], %[off], %[base] offset:2048 sc1\n\ts_cmp_eq_u64 %[p3], 0\n\ts_cselect_b64 exec, 1, %[p3]\n\tglobal_load_dwordx4 v[76:79], %[off], %[base] offset:3072 sc1\n\ts_sleep %[gap]\n\ts_cmp_eq_u64 %[p0], 0\n\ts_cselect_b64 exec, 1, %[p0]\n\tglobal_load_dwordx4 v[80:83], %[off], %[base] sc1\n\ts_cmp_eq_u64 %[p1], 0\n\ts_cselect_b64 exec, 1, %[p1]\n\tglobal_load_dwordx4 v[84:87], %[off], %[base] offset:1024 sc1\n\ts_cmp_eq_u64 %[p2], 0\n\ts_cselect_b64 exec, 1, %[p2]\n\tglobal_load_dwordx4 v[88:91], %[off], %[base] offset:2048 sc1\n\ts_cmp_eq_u64 %[p3], 0\n\ts_cselect_b64 exec, 1, %[p3]\n\tglobal_load_dwordx4 v[92:95], %[off], %[base] offset:3072 sc1\n\ts_sleep %[gap]\n\ts_cmp_eq_u64 %[p0], 0\n\ts_cselect_b64 exec, 1, %[p0]\n\tglobal_load_dwordx4 v[96:99], %[off], %[base] sc1\n\ts_cmp_eq_u64 %[p1], 0\n\ts_cselect_b64 exec, 1, %[p1]\n\tglobal_load_dwordx4 v[100:103], %[off], %[base] offset:1024 sc1\n\ts_cmp_eq_u64 %[p2], 0\n\ts_cselect_b64 exec, 1, %[p2]\n\tglobal_load_dwordx4 v[104:107], %[off], %[base] offset:2048 sc1\n\ts_cmp_eq_u64 %[p3], 0\n\ts_cselect_b64 exec, 1, %[p3]\n\tglobal_load_dwordx4 v[108:111], %[off], %[base] offset:3072 sc1\n\ts_sleep %[gap]\n\ts_cmp_eq_u64 %[p0], 0\n\ts_cselect_b64 exec, 1, %[p0]\n\tglobal_load_dwordx4 v[112:115], %[off], %[base] sc1\n\ts_cmp_eq_u64 %[p1], 0\n\ts_cselect_b64 exec, 1, %[p1]\n\tglobal_load_dwordx4 v[116:119], %[off], %[base] offset:1024 sc1\n\ts_cmp_eq_u64 %[p2], 0\n\ts_cselect_b64 exec, 1, %[p2]\n\tglobal_load_dwordx4 v[120:123], %[off], %[base] offset:2048 sc1\n\ts_cmp_eq_u64 %[p3], 0\n\ts_cselect_b64 exec, 1, %[p3]\n\tglobal_load_dwordx4 v[124:127], %[off], %[base] offset:3072 sc1\n\t\n1:\n\ts_waitcnt vmcnt(12)\n\ts_mov_b64 exec, %[p0]\n\tv_xor_b32 %[t0], %[tag], v65\n\tv_xor_b32 %[t1], %[tag], v67\n\tv_or_b32 %[t0], %[t0], %[t1]\n\tv_cmp_eq_u32 vcc, 0, %[t0]\n\ts_mov_b64 exec, vcc\n\tv_mov_b32 %[x0], v64\n\tv_mov_b32 %[y0], v66\n\ts_andn2_b64 %[p0], %[p0], vcc\n\ts_mov_b64 exec, %[p1]\n\tv_xor_b32 %[t0], %[tag], v69\n\tv_xor_b32 %[t1], %[tag], v71\n\tv_or_b32 %[t0], %[t0], %[t1]\n\tv_cmp_eq_u32 vcc, 0, %[t0]\n\ts_mov_b64 exec, vcc\n\tv_mov_b32 %[x1], v68\n\tv_mov_b32 %[y1], v70\n\ts_andn2_b64 %[p1], %[p1], vcc\n\ts_mov_b64 exec, %[p2]\n\tv_xor_b32 %[t0], %[tag], v73\n\tv_xor_b32 %[t1], %[tag], v75\n\tv_or_b32 %[t0], %[t0], %[t1]\n\tv_cmp_eq_u32 vcc, 0, %[t0]\n\ts_mov_b64 exec, vcc\n\tv_mov_b32 %[x2], v72\n\tv_mov_b32 %[y2], v74\n\ts_andn2_b64 %[p2], %[p2], vcc\n\ts_mov_b64 exec, %[p3]\n\tv_xor_b32 %[t0], %[tag], v77\n\tv_xor_b32 %[t1], %[tag], v79\n\tv_or_b32 %[t0], %[t0], %[t1]\n\tv_cmp_eq_u32 vcc, 0, %[t0]\n\ts_mov_b64 exec, vcc\n\tv_mov_b32 %[x3], v76\n\tv_mov_b32 %[y3], v78\n\ts_andn2_b64 %[p3], %[p3], vcc\n\ts_or_b64 %[pa], %[p0], %[p1]\n\ts_or_b64 %[pa], %[pa], %[p2]\n\ts_or_b64 %[pa], %[pa], %[p3]\n\ts_cbranch_scc0 4f\n\ts_cmp_eq_u64 %[p0], 0\n\ts_cselect_b64 exec, 1, %[p0]\n\tglobal_load_dwordx4 v[64:67], %[off], %[base] sc1\n\ts_cmp_eq_u64 %[p1], 0\n\ts_cselect_b64 exec, 1, %[p1]\n\tglobal_load_dwordx4 v[68:71], %[off], %[base] offset:1024 sc1\n\ts_cmp_eq_u64 %[p2], 0\n\ts_cselect_b64 exec, 1, %[p2]\n\tglobal_load_dwordx4 v[72:75], %[off], %[base] offset:2048 sc1\n\ts_cmp_eq_u64 %[p3], 0\n\ts_cselect_b64 exec, 1, %[p3]\n\tglobal_load_dwordx4 v[76:79], %[off], %[base] offset:3072 sc1\n\ts_waitcnt vmcnt(12)\n\ts_mov_b64 exec, %[p0]\n\tv_xor_b32 %[t0], %[tag], v81\n\tv_xor_b32 %[t1], %[tag], v83\n\tv_or_b32 %[t0], %[t0], %[t1]\n\tv_cmp_eq_u32 vcc, 0, %[t0]\n\ts_mov_b64 exec, vcc\n\tv_mov_b32 %[x0], v80\n\tv_mov_b32 %[y0], v82\n\ts_andn2_b64 %[p0], %[p0], vcc\n\ts_mov_b64 exec, %[p1]\n\tv_xor_b32 %[t0], %[tag], v85\n\tv_xor_b32 %[t1], %[tag], v87\n\tv_or_b32 %[t0], %[t0], %[t1]\n\tv_cmp_eq_u32 vcc, 0, %[t0]\n\ts_mov_b64 exec, vcc\n\tv_mov_b32 %[x1], v84\n\tv_mov_b32 %[y1], v86\n\ts_andn2_b64 %[p1], %[p1], vcc\n\ts_mov_b64 exec, %[p2]\n\tv_xor_b32 %[t0], %[tag], v89\n\tv_xor_b32 %[t1], %[tag], v91\n\tv_or_b32 %[t0], %[t0], %[t1]\n\tv_cmp_eq_u32 vcc, 0, %[t0]\n\ts_mov_b64 exec, vcc\n\tv_mov_b32 %[x2], v88\n\tv_mov_b32 %[y2], v90\n\ts_andn2_b64 %[p2], %[p2], vcc\n\ts_mov_b64 exec, %[p3]\n\tv_xor_b32 %[t0], %[tag], v93\n\tv_xor_b32 %[t1], %[tag], v95\n\tv_or_b32 %[t0], %[t0], %[t1]\n\tv_cmp_eq_u32 vcc, 0, %[t0]\n\ts_mov_b64 exec, vcc\n\tv_mov_b32 %[x3], v92\n\tv_mov_b32 %[y3], v94\n\ts_andn2_b64 %[p3], %[p3], vcc\n\ts_or_b64 %[pa], %[p0], %[p1]\n\ts_or_b64 %[pa], %[pa], %[p2]\n\ts_or_b64 %[pa], %[pa], %[p3]\n\ts_cbranch_scc0 4f\n\ts_cmp_eq_u64 %[p0], 0\n\ts_cselect_b64 exec, 1, %[p0]\n\tglobal_load_dwordx4 v[80:83], %[off], %[base] sc1\n\ts_cmp_eq_u64 %[p1], 0\n\ts_cselect_b64 exec, 1, %[p1]\n\tglobal_load_dwordx4 v[84:87], %[off], %[base] offset:1024 sc1\n\ts_cmp_eq_u64 %[p2], 0\n\ts_cselect_b64 exec, 1, %[p2]\n\tglobal_load_dwordx4 v[88:91], %[off], %[base] offset:2048 sc1\n\ts_cmp_eq_u64 %[p3], 0\n\ts_cselect_b64 exec, 1, %[p3]\n\tglobal_load_dwordx4 v[92:95], %[off], %[base] offset:3072 sc1\n\ts_waitcnt vmcnt(12)\n\ts_mov_b64 exec, %[p0]\n\tv_xor_b32 %[t0], %[tag], v97\n\tv_xor_b32 %[t1], %[tag], v99\n\tv_or_b32 %[t0], %[t0], %[t1]\n\tv_cmp_eq_u32 vcc, 0, %[t0]\n\ts_mov_b64 exec, vcc\n\tv_mov_b32 %[x0], v96\n\tv_mov_b32 %[y0], v98\n\ts_andn2_b64 %[p0], %[p0], vcc\n\ts_mov_b64 exec, %[p1]\n\tv_xor_b32 %[t0], %[tag], v101\n\tv_xor_b32 %[t1], %[tag], v103\n\tv_or_b32 %[t0], %[t0], %[t1]\n\tv_cmp_eq_u32 vcc, 0, %[t0]\n\ts_mov_b64 exec, vcc\n\tv_mov_b32 %[x1], v100\n\tv_mov_b32 %[y1], v102\n\ts_andn2_b64 %[p1], %[p1], vcc\n\ts_mov_b64 exec, %[p2]\n\tv_xor_b32 %[t0], %[tag], v105\n\tv_xor_b32 %[t1], %[tag], v107\n\tv_or_b32 %[t0], %[t0], %[t1]\n\tv_cmp_eq_u32 vcc, 0, %[t0]\n\ts_mov_b64 exec, vcc\n\tv_mov_b32 %[x2], v104\n\tv_mov_b32 %[y2], v106\n\ts_andn2_b64 %[p2], %[p2], vcc\n\ts_mov_b64 exec, %[p3]\n\tv_xor_b32 %[t0], %[tag], v109\n\tv_xor_b32 %[t1], %[tag], v111\n\tv_or_b32 %[t0], %[t0], %[t1]\n\tv_cmp_eq_u32 vcc, 0, %[t0]\n\ts_mov_b64 exec, vcc\n\tv_mov_b32 %[x3], v108\n\tv_mov_b32 %[y3], v110\n\ts_andn2_b64 %[p3], %[p3], vcc\n\ts_or_b64 %[pa], %[p0], %[p1]\n\ts_or_b64 %[pa], %[pa], %[p2]\n\ts_or_b64 %[pa], %[pa], %[p3]\n\ts_cbranch_scc0 4f\n\ts_cmp_eq_u64 %[p0], 0\n\ts_cselect_b64 exec, 1, %[p0]\n\tglobal_load_dwordx4 v[96:99], %[off], %[base] sc1\n\ts_cmp_eq_u64 %[p1], 0\n\ts_cselect_b64 exec, 1, %[p1]\n\tglobal_load_dwordx4 v[100:103], %[off], %[base] offset:1024 sc1\n\ts_cmp_eq_u64 %[p2], 0\n\ts_cselect_b64 exec, 1, %[p2]\n\tglobal_load_dwordx4 v[104:107], %[off], %[base] offset:2048 sc1\n\ts_cmp_eq_u64 %[p3], 0\n\ts_cselect_b64 exec, 1, %[p3]\n\tglobal_load_dwordx4 v[108:111], %[off], %[base] offset:3072 sc1\n\ts_waitcnt vmcnt(12)\n\ts_mov_b64 exec, %[p0]\n\tv_xor_b32 %[t0], %[tag], v113\n\tv_xor_b32 %[t1], %[tag], v115\n\tv_or_b32 %[t0], %[t0], %[t1]\n\tv_cmp_eq_u32 vcc, 0, %[t0]\n\ts_mov_b64 exec, vcc\n\tv_mov_b32 %[x0], v112\n\tv_mov_b32 %[y0], v114\n\ts_andn2_b64 %[p0], %[p0], vcc\n\ts_mov_b64 exec, %[p1]\n\tv_xor_b32 %[t0], %[tag], v117\n\tv_xor_b32 %[t1], %[tag], v119\n\tv_or_b32 %[t0], %[t0], %[t1]\n\tv_cmp_eq_u32 vcc, 0, %[t0]\n\ts_mov_b64 exec, vcc\n\tv_mov_b32 %[x1], v116\n\tv_mov_b32 %[y1], v118\n\ts_andn2_b64 %[p1], %[p1], vcc\n\ts_mov_b64 exec, %[p2]\n\tv_xor_b32 %[t0], %[tag], v121\n\tv_xor_b32 %[t1], %[tag], v123\n\tv_or_b32 %[t0], %[t0], %[t1]\n\tv_cmp_eq_u32 vcc, 0, %[t0]\n\ts_mov_b64 exec, vcc\n\tv_mov_b32 %[x2], v120\n\tv_mov_b32 %[y2], v122\n\ts_andn2_b64 %[p2], %[p2], vcc\n\ts_mov_b64 exec, %[p3]\n\tv_xor_b32 %[t0], %[tag], v125\n\tv_xor_b32 %[t1], %[tag], v127\n\tv_or_b32 %[t0], %[t0], %[t1]\n\tv_cmp_eq_u32 vcc, 0, %[t0]\n\ts_mov_b64 exec, vcc\n\tv_mov_b32 %[x3], v124\n\tv_mov_b32 %[y3], v126\n\ts_andn2_b64 %[p3], %[p3], vcc\n\ts_or_b64 %[pa], %[p0], %[p1]\n\ts_or_b64 %[pa], %[pa], %[p2]\n\ts_or_b64 %[pa], %[pa], %[p3]\n\ts_cbranch_scc0 4f\n\ts_cmp_eq_u64 %[p0], 0\n\ts_cselect_b64 exec, 1, %[p0]\n\tglobal_load_dwordx4 v[112:115], %[off], %[base] sc1\n\ts_cmp_eq_u64 %[p1], 0\n\ts_cselect_b64 exec, 1, %[p1]\n\tglobal_load_dwordx4 v[116:119], %[off], %[base] offset:1024 sc1\n\ts_cmp_eq_u64 %[p2], 0\n\ts_cselect_b64 exec, 1, %[p2]\n\tglobal_load_dwordx4 v[120:123], %[off], %[base] offset:2048 sc1\n\ts_cmp_eq_u64 %[p3], 0\n\ts_cselect_b64 exec, 1, %[p3]\n\tglobal_load_dwordx4 v[124:127], %[off], %[base] offset:3072 sc1\n\ts_sub_u32 %[cnt], %[cnt], 1\n\ts_cmp_lg_u32 %[cnt], 0\n\ts_cbranch_scc1 1b\n4:\n\ts_mov_b64 exec, -1"
               : [x0] "+&v"(x[0]), [y0] "+&v"(y[0]), [x1] "+&v"(x[1]), [y1] "+&v"(y[1]), [x2] "+&v"(x[2]), [y2] "+&v"(y[2]), [x3] "+&v"(x[3]), [y3] "+&v"(y[3]), [t0] "=&v"(t0), [t1] "=&v"(t1), [cnt] "+&s"(cnt), [p0] "=&s"(p[0]), [p1] "=&s"(p[1]), [p2] "=&s"(p[2]), [p3] "=&s"(p[3]), [pa] "=&s"(pa)
               : [off] "v"(off), [base] "s"(base), [tag] "s"(tag), [gap] "n"(GAP)
               : "memory", "vcc", "scc", "v64", "v65", "v66", "v67", "v68", "v69", "v70", "v71", "v72", "v73", "v74", "v75", "v76", "v77", "v78", "v79", "v80", "v81", "v82", "v83", "v84", "v85", "v86", "v87", "v88", "v89", "v90", "v91", "v92", "v93", "v94", "v95", "v96", "v97", "v98", "v99", "v100", "v101", "v102", "v103", "v104", "v105", "v106", "v107", "v108", "v109", "v110", "v111", "v112", "v113", "v114", "v115", "v116", "v117", "v118", "v119", "v120", "v121", "v122", "v123", "v124", "v125", "v126", "v127");
}
template <int GAP> __device__ __forceinline__ void pollf5_1(float (&x)[5], float (&y)[5], const unsigned char* base, unsigned off, unsigned off5, unsigned tag) {
  unsigned t0, t1, cnt = SPIN_CAP;
  unsigned long long p[5], pa;
#pragma unroll
  for (int i = 0; i < 5; ++i) { x[i] = 0.f; y[i] = 0.f; }
  asm volatile("s_mov_b64 %[p0], -1\n\ts_mov_b64 %[p1], -1\n\ts_mov_b64 %[p2], -1\n\ts_mov_b64 %[p3], -1\n\ts_mov_b64 %[p4], -1\n\tglobal_load_dwordx4 v[108:111], %[off], %[base] sc1\n\tglobal_load_dwordx4 v[112:115], %[off], %[base] offset:1024 sc1\n\tglobal_load_dwordx4 v[116:119], %[off], %[base] offset:2048 sc1\n\tglobal_load_dwordx4 v[120:123], %[off], %[base] offset:3072 sc1\n\tglobal_load_dwordx4 v[124:127], %[off5], %[base] sc1\n\ts_waitcnt vmcnt(0)\n\ts_mov_b64 exec, %[p0]\n\tv_xor_b32 %[t0], %[tag], v109\n\tv_xor_b32 %[t1], %[tag], v111\n\tv_or_b32 %[t0], %[t0], %[t1]\n\tv_cmp_eq_u32 vcc, 0, %[t0]\n\ts_mov_b64 exec, vcc\n\tv_mov_b32 %[x0], v108\n\tv_mov_b32 %[y0], v110\n\ts_andn2_b64 %[p0], %[p0], vcc\n\ts_mov_b64 exec, %[p1]\n\tv_xor_b32 %[t0], %[tag], v113\n\tv_xor_b32 %[t1], %[tag], v115\n\tv_or_b32 %[t0], %[t0], %[t1]\n\tv_cmp_eq_u32 vcc, 0, %[t0]\n\ts_mov_b64 exec, vcc\n\tv_mov_b32 %[x1], v112\n\tv_mov_b32 %[y1], v114\n\ts_andn2_b64 %[p1], %[p1], vcc\n\ts_mov_b64 exec, %[p2]\n\tv_xor_b32 %[t0], %[tag], v117\n\tv_xor_b32 %[t1], %[tag], v119\n\tv_or_b32 %[t0], %[t0], %[t1]\n\tv_cmp_eq_u32 vcc, 0, %[t0]\n\ts_mov_b64 exec, vcc\n\tv_mov_b32 %[x2], v116\n\tv_mov_b32 %[y2], v118\n\ts_andn2_b64 %[p2], %[p2], vcc\n\ts_mov_b64 exec, %[p3]\n\tv_xor_b32 %[t0], %[tag], v121\n\tv_xor_b32 %[t1], %[tag], v123\n\tv_or_b32 %[t0], %[t0], %[t1]\n\tv_cmp_eq_u32 vcc, 0, %[t0]\n\ts_mov_b64 exec, vcc\n\tv_mov_b32 %[x3], v120\n\tv_mov_b32 %[y3], v122\n\ts_andn2_b64 %[p3], %[p3], vcc\n\ts_mov_b64 exec, %[p4]\n\tv_xor_b32 %[t0], %[tag], v125\n\tv_xor_b32 %[t1], %[tag], v127\n\tv_or_b32 %[t0], %[t0], %[t1]\n\tv_cmp_eq_u32 vcc, 0, %[t0]\n\ts_mov_b64 exec, vcc\n\tv_mov_b32 %[x4], v124\n\tv_mov_b32 %[y4], v126\n\ts_andn2_b64 %[p4], %[p4], vcc\n\ts_or_b64 %[pa], %[p0], %[p1]\n\ts_or_b64 %[pa], %[pa], %[p2]\n\ts_or_b64 %[pa], %[pa], %[p3]\n\ts_or_b64 %[pa], %[pa], %[p4]\n\ts_cbranch_scc0 4f\n\ts_cmp_eq_u64 %[p0], 0\n\ts_cselect_b64 exec, 1, %[p0]\n\tglobal_load_dwordx4 v[108:111], %[off], %[base] sc1\n\ts_cmp_eq_u64 %[p1], 0\n\ts_cselect_b64 exec, 1, %[p1]\n\tglobal_load_dwordx4 v[112:115], %[off], %[base] offset:1024 sc1\n\ts_cmp_eq_u64 %[p2], 0\n\ts_cselect_b64 exec, 1, %[p2]\n\tglobal_load_dwordx4 v[116:119], %[off], %[base] offset:2048 sc1\n\ts_cmp_eq_u64 %[p3], 0\n\ts_cselect_b64 exec, 1, %[p3]\n\tglobal_load_dwordx4 v[120:123], %[off], %[base] offset:3072 sc1\n\ts_cmp_eq_u64 %[p4], 0\n\ts_cselect_b64 exec, 1, %[p4]\n\tglobal_load_dwordx4 v[124:127], %[off5], %[base] sc1\n\t\n1:\n\ts_waitcnt vmcnt(0)\n\ts_mov_b64 exec, %[p0]\n\tv_xor_b32 %[t0], %[tag], v109\n\tv_xor_b32 %[t1], %[tag], v111\n\tv_or_b32 %[t0], %[t0], %[t1]\n\tv_cmp_eq_u32 vcc, 0, %[t0]\n\ts_mov_b64 exec, vcc\n\tv_mov_b32 %[x0], v108\n\tv_mov_b32 %[y0], v110\n\ts_andn2_b64 %[p0], %[p0], vcc\n\ts_mov_b64 exec, %[p1]\n\tv_xor_b32 %[t0], %[tag], v113\n\tv_xor_b32 %[t1], %[tag], v115\n\tv_or_b32 %[t0], %[t0], %[t1]\n\tv_cmp_eq_u32 vcc, 0, %[t0]\n\ts_mov_b64 exec, vcc\n\tv_mov_b32 %[x1], v112\n\tv_mov_b32 %[y1], v114\n\ts_andn2_b64 %[p1], %[p1], vcc\n\ts_mov_b64 exec, %[p2]\n\tv_xor_b32 %[t0], %[tag], v117\n\tv_xor_b32 %[t1], %[tag], v119\n\tv_or_b32 %[t0], %[t0], %[t1]\n\tv_cmp_eq_u32 vcc, 0, %[t0]\n\ts_mov_b64 exec, vcc\n\tv_mov_b32 %[x2], v116\n\tv_mov_b32 %[y2], v118\n\ts_andn2_b64 %[p2], %[p2], vcc\n\ts_mov_b64 exec, %[p3]\n\tv_xor_b32 %[t0], %[tag], v121\n\tv_xor_b32 %[t1], %[tag], v123\n\tv_or_b32 %[t0], %[t0], %[t1]\n\tv_cmp_eq_u32 vcc, 0, %[t0]\n\ts_mov_b64 exec, vcc\n\tv_mov_b32 %[x3], v120\n\tv_mov_b32 %[y3], v122\n\ts_andn2_b64 %[p3], %[p3], vcc\n\ts_mov_b64 exec, %[p4]\n\tv_xor_b32 %[t0], %[tag], v125\n\tv_xor_b32 %[t1], %[tag], v127\n\tv_or_b32 %[t0], %[t0], %[t1]\n\tv_cmp_eq_u32 vcc, 0, %[t0]\n\ts_mov_b64 exec, vcc\n\tv_mov_b32 %[x4], v124\n\tv_mov_b32 %[y4], v126\n\ts_andn2_b64 %[p4], %[p4], vcc\n\ts_or_b64 %[pa], %[p0], %[p1]\n\ts_or_b64 %[pa], %[pa], %[p2]\n\ts_or_b64 %[pa], %[pa], %[p3]\n\ts_or_b64 %[pa], %[pa], %[p4]\n\ts_cbranch_scc0 4f\n\ts_cmp_eq_u64 %[p0], 0\n\ts_cselect_b64 exec, 1, %[p0]\n\tglobal_load_dwordx4 v[108:111], %[off], %[base] sc1\n\ts_cmp_eq_u64 %[p1], 0\n\ts_cselect_b64 exec, 1, %[p1]\n\tglobal_load_dwordx4 v[112:115], %[off], %[base] offset:1024 sc1\n\ts_cmp_eq_u64 %[p2], 0\n\ts_cselect_b64 exec, 1, %[p2]\n\tglobal_load_dwordx4 v[116:119], %[off], %[base] offset:2048 sc1\n\ts_cmp_eq_u64 %[p3], 0\n\ts_cselect_b64 exec, 1, %[p3]\n\tglobal_load_dwordx4 v[120:123], %[off], %[base] offset:3072 sc1\n\ts_cmp_eq_u64 %[p4], 0\n\ts_cselect_b64 exec, 1, %[p4]\n\tglobal_load_dwordx4 v[124:127], %[off5], %[base] sc1\n\ts_sub_u32 %[cnt], %[cnt], 1\n\ts_cmp_lg_u32 %[cnt], 0\n\ts_cbranch_scc1 1b\n4:\n\ts_mov_b64 exec, -1"
               : [x0] "+&v"(x[0]), [y0] "+&v"(y[0]), [x1] "+&v"(x[1]), [y1] "+&v"(y[1]), [x2] "+&v"(x[2]), [y2] "+&v"(y[2]), [x3] "+&v"(x[3]), [y3] "+&v"(y[3]), [x4] "+&v"(x[4]), [y4] "+&v"(y[4]), [t0] "=&v"(t0), [t1] "=&v"(t1), [cnt] "+&s"(cnt), [p0] "=&s"(p[0]), [p1] "=&s"(p[1]), [p2] "=&s"(p[2]), [p3] "=&s"(p[3]), [p4] "=&s"(p[4]), [pa] "=&s"(pa)
               : [off] "v"(off), [off5] "v"(off5), [base] "s"(base), [tag] "s"(tag), [gap] "n"(GAP)
               : "memory", "vcc", "scc", "v108", "v109", "v110", "v111", "v112", "v113", "v114", "v115", "v116", "v117", "v118", "v119", "v120", "v121", "v122", "v123", "v124", "v125", "v126", "v127");
}
template <int GAP> __device__ __forceinline__ void pollf5_2(float (&x)[5], float (&y)[5], const unsigned char* base, unsigned off, unsigned off5, unsigned tag) {
  unsigned t0, t1, cnt = SPIN_CAP;
  unsigned long long p[5], pa;
#pragma unroll
  for (int i = 0; i < 5; ++i) { x[i] = 0.f; y[i] = 0.f; }
  asm volatile("s_mov_b64 %[p0], -1\n\ts_mov_b64 %[p1], -1\n\ts_mov_b64 %[p2], -1\n\ts_mov_b64 %[p3], -1\n\ts_mov_b64 %[p4], -1\n\tglobal_load_dwordx4 v[88:91], %[off], %[base] sc1\n\tglobal_load_dwordx4 v[92:95], %[off], %[base] offset:1024 sc1\n\tglobal_load_dwordx4 v[96:99], %[off], %[base] offset:2048 sc1\n\tglobal_load_dwordx4 v[100:103], %[off], %[base] offset:3072 sc1\n\tglobal_load_dwordx4 v[104:107], %[off5], %[base] sc1\n\ts_waitcnt vmcnt(0)\n\ts_mov_b64 exec, %[p0]\n\tv_xor_b32 %[t0], %[tag], v89\n\tv_xor_b32 %[t1], %[tag], v91\n\tv_or_b32 %[t0], %[t0], %[t1]\n\tv_cmp_eq_u32 vcc, 0, %[t0]\n\ts_mov_b64 exec, vcc\n\tv_mov_b32 %[x0], v88\n\tv_mov_b32 %[y0], v90\n\ts_andn2_b64 %[p0], %[p0], vcc\n\ts_mov_b64 exec, %[p1]\n\tv_xor_b32 %[t0], %[tag], v93\n\tv_xor_b32 %[t1], %[tag], v95\n\tv_or_b32 %[t0], %[t0], %[t1]\n\tv_cmp_eq_u32 vcc, 0, %[t0]\n\ts_mov_b64 exec, vcc\n\tv_mov_b32 %[x1], v92\n\tv_mov_b32 %[y1], v94\n\ts_andn2_b64 %[p1], %[p1], vcc\n\ts_mov_b64 exec, %[p2]\n\tv_xor_b32 %[t0], %[tag], v97\n\tv_xor_b32 %[t1], %[tag], v99\n\tv_or_b32 %[t0], %[t0], %[t1]\n\tv_cmp_eq_u32 vcc, 0, %[t0]\n\ts_mov_b64 exec, vcc\n\tv_mov_b32 %[x2], v96\n\tv_mov_b32 %[y2], v98\n\ts_andn2_b64 %[p2], %[p2], vcc\n\ts_mov_b64 exec, %[p3]\n\tv_xor_b32 %[t0], %[tag], v101\n\tv_xor_b32 %[t1], %[tag], v103\n\tv_or_b32 %[t0], %[t0], %[t1]\n\tv_cmp_eq_u32 vcc, 0, %[t0]\n\ts_mov_b64 exec, vcc\n\tv_mov_b32 %[x3], v100\n\tv_mov_b32 %[y3], v102\n\ts_andn2_b64 %[p3], %[p3], vcc\n\ts_mov_b64 exec, %[p4]\n\tv_xor_b32 %[t0], %[tag], v105\n\tv_xor_b32 %[t1], %[tag], v107\n\tv_or_b32 %[t0], %[t0], %[t1]\n\tv_cmp_eq_u32 vcc, 0, %[t0]\n\ts_mov_b64 exec, vcc\n\tv_mov_b32 %[x4], v104\n\tv_mov_b32 %[y4], v106\n\ts_andn2_b64 %[p4], %[p4], vcc\n\ts_or_b64 %[pa], %[p0], %[p1]\n\ts_or_b64 %[pa], %[pa], %[p2]\n\ts_or_b64 %[pa], %[pa], %[p3]\n\ts_or_b64 %[pa], %[pa], %[p4]\n\ts_cbranch_scc0 4f\n\ts_cmp_eq_u64 %[p0], 0\n\ts_cselect_b64 exec, 1, %[p0]\n\tglobal_load_dwordx4 v[88:91], %[off], %[base] sc1\n\ts_cmp_eq_u64 %[p1], 0\n\ts_cselect_b64 exec, 1, %[p1]\n\tglobal_load_dwordx4 v[92:95], %[off], %[base] offset:1024 sc1\n\ts_cmp_eq_u64 %[p2], 0\n\ts_cselect_b64 exec, 1, %[p2]\n\tglobal_load_dwordx4 v[96:99], %[off], %[base] offset:2048 sc1\n\ts_cmp_eq_u64 %[p3], 0\n\ts_cselect_b64 exec, 1, %[p3]\n\tglobal_load_dwordx4 v[100:103], %[off], %[base] offset:3072 sc1\n\ts_cmp_eq_u64 %[p4], 0\n\ts_cselect_b64 exec, 1, %[p4]\n\tglobal_load_dwordx4 v[104:107], %[off5], %[base] sc1\n\ts_sleep %[gap]\n\ts_cmp_eq_u64 %[p0], 0\n\ts_cselect_b64 exec, 1, %[p0]\n\tglobal_load_dwordx4 v[108:111], %[off], %[base] sc1\n\ts_cmp_eq_u64 %[p1], 0\n\ts_cselect_b64 exec, 1, %[p1]\n\tglobal_load_dwordx4 v[112:115], %[off], %[base] offset:1024 sc1\n\ts_cmp_eq_u64 %[p2], 0\n\ts_cselect_b64 exec, 1, %[p2]\n\tglobal_load_dwordx4 v[116:119], %[off], %[base] offset:2048 sc1\n\ts_cmp_eq_u64 %[p3], 0\n\ts_cselect_b64 exec, 1, %[p3]\n\tglobal_load_dwordx4 v[120:123], %[off], %[base] offset:3072 sc1\n\ts_cmp_eq_u64 %[p4], 0\n\ts_cselect_b64 exec, 1, %[p4]\n\tglobal_load_dwordx4 v[124:127], %[off5], %[base] sc1\n\t\n1:\n\ts_waitcnt vmcnt(5)\n\ts_mov_b64 exec, %[p0]\n\tv_xor_b32 %[t0], %[tag], v89\n\tv_xor_b32 %[t1], %[tag], v91\n\tv_or_b32 %[t0], %[t0], %[t1]\n\tv_cmp_eq_u32 vcc, 0, %[t0]\n\ts_mov_b64 exec, vcc\n\tv_mov_b32 %[x0], v88\n\tv_mov_b32 %[y0], v90\n\ts_andn2_b64 %[p0], %[p0], vcc\n\ts_mov_b64 exec, %[p1]\n\tv_xor_b32 %[t0], %[tag], v93\n\tv_xor_b32 %[t1], %[tag], v95\n\tv_or_b32 %[t0], %[t0], %[t1]\n\tv_cmp_eq_u32 vcc, 0, %[t0]\n\ts_mov_b64 exec, vcc\n\tv_mov_b32 %[x1], v92\n\tv_mov_b32 %[y1], v94\n\ts_andn2_b64 %[p1], %[p1], vcc\n\ts_mov_b64 exec, %[p2]\n\tv_xor_b32 %[t0], %[tag], v97\n\tv_xor_b32 %[t1], %[tag], v99\n\tv_or_b32 %[t0], %[t0], %[t1]\n\tv_cmp_eq_u32 vcc, 0, %[t0]\n\ts_mov_b64 exec, vcc\n\tv_mov_b32 %[x2], v96\n\tv_mov_b32 %[y2], v98\n\ts_andn2_b64 %[p2], %[p2], vcc\n\ts_mov_b64 exec, %[p3]\n\tv_xor_b32 %[t0], %[tag], v101\n\tv_xor_b32 %[t1], %[tag], v103\n\tv_or_b32 %[t0], %[t0], %[t1]\n\tv_cmp_eq_u32 vcc, 0, %[t0]\n\ts_mov_b64 exec, vcc\n\tv_mov_b32 %[x3], v100\n\tv_mov_b32 %[y3], v102\n\ts_andn2_b64 %[p3], %[p3], vcc\n\ts_mov_b64 exec, %[p4]\n\tv_xor_b32 %[t0], %[tag], v105\n\tv_xor_b32 %[t1], %[tag], v107\n\tv_or_b32 %[t0], %[t0], %[t1]\n\tv_cmp_eq_u32 vcc, 0, %[t0]\n\ts_mov_b64 exec, vcc\n\tv_mov_b32 %[x4], v104\n\tv_mov_b32 %[y4], v106\n\ts_andn2_b64 %[p4], %[p4], vcc\n\ts_or_b64 %[pa], %[p0], %[p1]\n\ts_or_b64 %[pa], %[pa], %[p2]\n\ts_or_b64 %[pa], %[pa], %[p3]\n\ts_or_b64 %[pa], %[pa], %[p4]\n\ts_cbranch_scc0 4f\n\ts_cmp_eq_u64 %[p0], 0\n\ts_cselect_b64 exec, 1, %[p0]\n\tglobal_load_dwordx4 v[88:91], %[off], %[base] sc1\n\ts_cmp_eq_u64 %[p1], 0\n\ts_cselect_b64 exec, 1, %[p1]\n\tglobal_load_dwordx4 v[92:95], %[off], %[base] offset:1024 sc1\n\ts_cmp_eq_u64 %[p2], 0\n\ts_cselect_b64 exec, 1, %[p2]\n\tglobal_load_dwordx4 v[96:99], %[off], %[base] offset:2048 sc1\n\ts_cmp_eq_u64 %[p3], 0\n\ts_cselect_b64 exec, 1, %[p3]\n\tglobal_load_dwordx4 v[100:103], %[off], %[base] offset:3072 sc1\n\ts_cmp_eq_u64 %[p4], 0\n\ts_cselect_b64 exec, 1, %[p4]\n\tglobal_load_dwordx4 v[104:107], %[off5], %[base] sc1\n\ts_waitcnt vmcnt(5)\n\ts_mov_b64 exec, %[p0]\n\tv_xor_b32 %[t0], %[tag], v109\n\tv_xor_b32 %[t1], %[tag], v111\n\tv_or_b32 %[t0], %[t0], %[t1]\n\tv_cmp_eq_u32 vcc, 0, %[t0]\n\ts_mov_b64 exec, vcc\n\tv_mov_b32 %[x0], v108\n\tv_mov_b32 %[y0], v110\n\ts_andn2_b64 %[p0], %[p0], vcc\n\ts_mov_b64 exec, %[p1]\n\tv_xor_b32 %[t0], %[tag], v113\n\tv_xor_b32 %[t1], %[tag], v115\n\tv_or_b32 %[t0], %[t0], %[t1]\n\tv_cmp_eq_u32 vcc, 0, %[t0]\n\ts_mov_b64 exec, vcc\n\tv_mov_b32 %[x1], v112\n\tv_mov_b32 %[y1], v114\n\ts_andn2_b64 %[p1], %[p1], vcc\n\ts_mov_b64 exec, %[p2]\n\tv_xor_b32 %[t0], %[tag], v117\n\tv_xor_b32 %[t1], %[tag], v119\n\tv_or_b32 %[t0], %[t0], %[t1]\n\tv_cmp_eq_u32 vcc, 0, %[t0]\n\ts_mov_b64 exec, vcc\n\tv_mov_b32 %[x2], v116\n\tv_mov_b32 %[y2], v118\n\ts_andn2_b64 %[p2], %[p2], vcc\n\ts_mov_b64 exec, %[p3]\n\tv_xor_b32 %[t0], %[tag], v121\n\tv_xor_b32 %[t1], %[tag], v123\n\tv_or_b32 %[t0], %[t0], %[t1]\n\tv_cmp_eq_u32 vcc, 0, %[t0]\n\ts_mov_b64 exec, vcc\n\tv_mov_b32 %[x3], v120\n\tv_mov_b32 %[y3], v122\n\ts_andn2_b64 %[p3], %[p3], vcc\n\ts_mov_b64 exec, %[p4]\n\tv_xor_b32 %[t0], %[tag], v125\n\tv_xor_b32 %[t1], %[tag], v127\n\tv_or_b32 %[t0], %[t0], %[t1]\n\tv_cmp_eq_u32 vcc, 0, %[t0]\n\ts_mov_b64 exec, vcc\n\tv_mov_b32 %[x4], v124\n\tv_mov_b32 %[y4], v126\n\ts_andn2_b64 %[p4], %[p4], vcc\n\ts_or_b64 %[pa], %[p0], %[p1]\n\ts_or_b64 %[pa], %[pa], %[p2]\n\ts_or_b64 %[pa], %[pa], %[p3]\n\ts_or_b64 %[pa], %[pa], %[p4]\n\ts_cbranch_scc0 4f\n\ts_cmp_eq_u64 %[p0], 0\n\ts_cselect_b64 exec, 1, %[p0]\n\tglobal_load_dwordx4 v[108:111], %[off], %[base] sc1\n\ts_cmp_eq_u64 %[p1], 0\n\ts_cselect_b64 exec, 1, %[p1]\n\tglobal_load_dwordx4 v[112:115], %[off], %[base] offset:1024 sc1\n\ts_cmp_eq_u64 %[p2], 0\n\ts_cselect_b64 exec, 1, %[p2]\n\tglobal_load_dwordx4 v[116:119], %[off], %[base] offset:2048 sc1\n\ts_cmp_eq_u64 %[p3], 0\n\ts_cselect_b64 exec, 1, %[p3]\n\tglobal_load_dwordx4 v[120:123], %[off], %[base] offset:3072 sc1\n\ts_cmp_eq_u64 %[p4], 0\n\ts_cselect_b64 exec, 1, %[p4]\n\tglobal_load_dwordx4 v[124:127], %[off5], %[base] sc1\n\ts_sub_u32 %[cnt], %[cnt], 1\n\ts_cmp_lg_u32 %[cnt], 0\n\ts_cbranch_scc1 1b\n4:\n\ts_mov_b64 exec, -1"
               : [x0] "+&v"(x[0]), [y0] "+&v"(y[0]), [x1] "+&v"(x[1]), [y1] "+&v"(y[1]), [x2] "+&v"(x[2]), [y2] "+&v"(y[2]), [x3] "+&v"(x[3]), [y3] "+&v"(y[3]), [x4] "+&v"(x[4]), [y4] "+&v"(y[4]), [t0] "=&v"(t0), [t1] "=&v"(t1), [cnt] "+&s"(cnt), [p0] "=&s"(p[0]), [p1] "=&s"(p[1]), [p2] "=&s"(p[2]), [p3] "=&s"(p[3]), [p4] "=&s"(p[4]), [pa] "=&s"(pa)
               : [off] "v"(off), [off5] "v"(off5), [base] "s"(base), [tag] "s"(tag), [gap] "n"(GAP)
               : "memory", "vcc", "scc", "v88", "v89", "v90", "v91", "v92", "v93", "v94", "v95", "v96", "v97", "v98", "v99", "v100", "v101", "v102", "v103", "v104", "v105", "v106", "v107", "v108", "v109", "v110", "v111", "v112", "v113", "v114", "v115", "v116", "v117", "v118", "v119", "v120", "v121", "v122", "v123", "v124", "v125", "v126", "v127");
}
template <int GAP> __device__ __forceinline__ void pollf5_3(float (&x)[5], float (&y)[5], const unsigned char* base, unsigned off, unsigned off5, unsigned tag) {
  unsigned t0, t1, cnt = SPIN_CAP;
  unsigned long long p[5], pa;
#pragma unroll
  for (int i = 0; i < 5; ++i) { x[i] = 0.f; y[i] = 0.f; }
  asm volatile("s_mov_b64 %[p0], -1\n\ts_mov_b64 %[p1], -1\n\ts_mov_b64 %[p2], -1\n\ts_mov_b64 %[p3], -1\n\ts_mov_b64 %[p4], -1\n\tglobal_load_dwordx4 v[68:71], %[off], %[base] sc1\n\tglobal_load_dwordx4 v[72:75], %[off], %[base] offset:1024 sc1\n\tglobal_load_dwordx4 v[76:79], %[off], %[base] offset:2048 sc1\n\tglobal_load_dwordx4 v[80:83], %[off], %[base] offset:3072 sc1\n\tglobal_load_dwordx4 v[84:87], %[off5], %[base] sc1\n\ts_waitcnt vmcnt(0)\n\ts_mov_b64 exec, %[p0]\n\tv_xor_b32 %[t0], %[tag], v69\n\tv_xor_b32 %[t1], %[tag], v71\n\tv_or_b32 %[t0], %[t0], %[t1]\n\tv_cmp_eq_u32 vcc, 0, %[t0]\n\ts_mov_b64 exec, vcc\n\tv_mov_b32 %[x0], v68\n\tv_mov_b32 %[y0], v70\n\ts_andn2_b64 %[p0], %[p0], vcc\n\ts_mov_b64 exec, %[p1]\n\tv_xor_b32 %[t0], %[tag], v73\n\tv_xor_b32 %[t1], %[tag], v75\n\tv_or_b32 %[t0], %[t0], %[t1]\n\tv_cmp_eq_u32 vcc, 0, %[t0]\n\ts_mov_b64 exec, vcc\n\tv_mov_b32 %[x1], v72\n\tv_mov_b32 %[y1], v74\n\ts_andn2_b64 %[p1], %[p1], vcc\n\ts_mov_b64 exec, %[p2]\n\tv_xor_b32 %[t0], %[tag], v77\n\tv_xor_b32 %[t1], %[tag], v79\n\tv_or_b32 %[t0], %[t0], %[t1]\n\tv_cmp_eq_u32 vcc, 0, %[t0]\n\ts_mov_b64 exec, vcc\n\tv_mov_b32 %[x2], v76\n\tv_mov_b32 %[y2], v78\n\ts_andn2_b64 %[p2], %[p2], vcc\n\ts_mov_b64 exec, %[p3]\n\tv_xor_b32 %[t0], %[tag], v81\n\tv_xor_b32 %[t1], %[tag], v83\n\tv_or_b32 %[t0], %[t0], %[t1]\n\tv_cmp_eq_u32 vcc, 0, %[t0]\n\ts_mov_b64 exec, vcc\n\tv_mov_b32 %[x3], v80\n\tv_mov_b32 %[y3], v82\n\ts_andn2_b64 %[p3], %[p3], vcc\n\ts_mov_b64 exec, %[p4]\n\tv_xor_b32 %[t0], %[tag], v85\n\tv_xor_b32 %[t1], %[tag], v87\n\tv_or_b32 %[t0], %[t0], %[t1]\n\tv_cmp_eq_u32 vcc, 0, %[t0]\n\ts_mov_b64 exec, vcc\n\tv_mov_b32 %[x4], v84\n\tv_mov_b32 %[y4], v86\n\ts_andn2_b64 %[p4], %[p4], vcc\n\ts_or_b64 %[pa], %[p0], %[p1]\n\ts_or_b64 %[pa], %[pa], %[p2]\n\ts_or_b64 %[pa], %[pa], %[p3]\n\ts_or_b64 %[pa], %[pa], %[p4]\n\ts_cbranch_scc0 4f\n\ts_cmp_eq_u64 %[p0], 0\n\ts_cselect_b64 exec, 1, %[p0]\n\tglobal_load_dwordx4 v[68:71], %[off], %[base] sc1\n\ts_cmp_eq_u64 %[p1], 0\n\ts_cselect_b64 exec, 1, %[p1]\n\tglobal_load_dwordx4 v[72:75], %[off], %[base] offset:1024 sc1\n\ts_cmp_eq_u64 %[p2], 0\n\ts_cselect_b64 exec, 1, %[p2]\n\tglobal_load_dwordx4 v[76:79], %[off], %[base] offset:2048 sc1\n\ts_cmp_eq_u64 %[p3], 0\n\ts_cselect_b64 exec, 1, %[p3]\n\tglobal_load_dwordx4 v[80:83], %[off], %[base] offset:3072 sc1\n\ts_cmp_eq_u64 %[p4], 0\n\ts_cselect_b64 exec, 1, %[p4]\n\tglobal_load_dwordx4 v[84:87], %[off5], %[base] sc1\n\ts_sleep %[gap]\n\ts_cmp_eq_u64 %[p0], 0\n\ts_cselect_b64 exec, 1, %[p0]\n\tglobal_load_dwordx4 v[88:91], %[off], %[base] sc1\n\ts_cmp_eq_u64 %[p1], 0\n\ts_cselect_b64 exec, 1, %[p1]\n\tglobal_load_dwordx4 v[92:95], %[off], %[base] offset:1024 sc1\n\ts_cmp_eq_u64 %[p2], 0\n\ts_cselect_b64 exec, 1, %[p2]\n\tglobal_load_dwordx4 v[96:99], %[off], %[base] offset:2048 sc1\n\ts_cmp_eq_u64 %[p3], 0\n\ts_cselect_b64 exec, 1, %[p3]\n\tglobal_load_dwordx4 v[100:103], %[off], %[base] offset:3072 sc1\n\ts_cmp_eq_u64 %[p4], 0\n\ts_cselect_b64 exec, 1, %[p4]\n\tglobal_load_dwordx4 v[104:107], %[off5], %[base] sc1\n\ts_sleep %[gap]\n\ts_cmp_eq_u64 %[p0], 0\n\ts_cselect_b64 exec, 1, %[p0]\n\tglobal_load_dwordx4 v[108:111], %[off], %[base] sc1\n\ts_cmp_eq_u64 %[p1], 0\n\ts_cselect_b64 exec, 1, %[p1]\n\tglobal_load_dwordx4 v[112:115], %[off], %[base] offset:1024 sc1\n\ts_cmp_eq_u64 %[p2], 0\n\ts_cselect_b64 exec, 1, %[p2]\n\tglobal_load_dwordx4 v[116:119], %[off], %[base] offset:2048 sc1\n\ts_cmp_eq_u64 %[p3], 0\n\ts_cselect_b64 exec, 1, %[p3]\n\tglobal_load_dwordx4 v[120:123], %[off], %[base] offset:3072 sc1\n\ts_cmp_eq_u64 %[p4], 0\n\ts_cselect_b64 exec, 1, %[p4]\n\tglobal_load_dwordx4 v[124:127], %[off5], %[base] sc1\n\t\n1:\n\ts_waitcnt vmcnt(10)\n\ts_mov_b64 exec, %[p0]\n\tv_xor_b32 %[t0], %[tag], v69\n\tv_xor_b32 %[t1], %[tag], v71\n\tv_or_b32 %[t0], %[t0], %[t1]\n\tv_cmp_eq_u32 vcc, 0, %[t0]\n\ts_mov_b64 exec, vcc\n\tv_mov_b32 %[x0], v68\n\tv_mov_b32 %[y0], v70\n\ts_andn2_b64 %[p0], %[p0], vcc\n\ts_mov_b64 exec, %[p1]\n\tv_xor_b32 %[t0], %[tag], v73\n\tv_xor_b32 %[t1], %[tag], v75\n\tv_or_b32 %[t0], %[t0], %[t1]\n\tv_cmp_eq_u32 vcc, 0, %[t0]\n\ts_mov_b64 exec, vcc\n\tv_mov_b32 %[x1], v72\n\tv_mov_b32 %[y1], v74\n\ts_andn2_b64 %[p1], %[p1], vcc\n\ts_mov_b64 exec, %[p2]\n\tv_xor_b32 %[t0], %[tag], v77\n\tv_xor_b32 %[t1], %[tag], v79\n\tv_or_b32 %[t0], %[t0], %[t1]\n\tv_cmp_eq_u32 vcc, 0, %[t0]\n\ts_mov_b64 exec, vcc\n\tv_mov_b32 %[x2], v76\n\tv_mov_b32 %[y2], v78\n\ts_andn2_b64 %[p2], %[p2], vcc\n\ts_mov_b64 exec, %[p3]\n\tv_xor_b32 %[t0], %[tag], v81\n\tv_xor_b32 %[t1], %[tag], v83\n\tv_or_b32 %[t0], %[t0], %[t1]\n\tv_cmp_eq_u32 vcc, 0, %[t0]\n\ts_mov_b64 exec, vcc\n\tv_mov_b32 %[x3], v80\n\tv_mov_b32 %[y3], v82\n\ts_andn2_b64 %[p3], %[p3], vcc\n\ts_mov_b64 exec, %[p4]\n\tv_xor_b32 %[t0], %[tag], v85\n\tv_xor_b32 %[t1], %[tag], v87\n\tv_or_b32 %[t0], %[t0], %[t1]\n\tv_cmp_eq_u32 vcc, 0, %[t0]\n\ts_mov_b64 exec, vcc\n\tv_mov_b32 %[x4], v84\n\tv_mov_b32 %[y4], v86\n\ts_andn2_b64 %[p4], %[p4], vcc\n\ts_or_b64 %[pa], %[p0], %[p1]\n\ts_or_b64 %[pa], %[pa], %[p2]\n\ts_or_b64 %[pa], %[pa], %[p3]\n\ts_or_b64 %[pa], %[pa], %[p4]\n\ts_cbranch_scc0 4f\n\ts_cmp_eq_u64 %[p0], 0\n\ts_cselect_b64 exec, 1, %[p0]\n\tglobal_load_dwordx4 v[68:71], %[off], %[base] sc1\n\ts_cmp_eq_u64 %[p1], 0\n\ts_cselect_b64 exec, 1, %[p1]\n\tglobal_load_dwordx4 v[72:75], %[off], %[base] offset:1024 sc1\n\ts_cmp_eq_u64 %[p2], 0\n\ts_cselect_b64 exec, 1, %[p2]\n\tglobal_load_dwordx4 v[76:79], %[off], %[base] offset:2048 sc1\n\ts_cmp_eq_u64 %[p3], 0\n\ts_cselect_b64 exec, 1, %[p3]\n\tglobal_load_dwordx4 v[80:83], %[off], %[base] offset:3072 sc1\n\ts_cmp_eq_u64 %[p4], 0\n\ts_cselect_b64 exec, 1, %[p4]\n\tglobal_load_dwordx4 v[84:87], %[off5], %[base] sc1\n\ts_waitcnt vmcnt(10)\n\ts_mov_b64 exec, %[p0]\n\tv_xor_b32 %[t0], %[tag], v89\n\tv_xor_b32 %[t1], %[tag], v91\n\tv_or_b32 %[t0], %[t0], %[t1]\n\tv_cmp_eq_u32 vcc, 0, %[t0]\n\ts_mov_b64 exec, vcc\n\tv_mov_b32 %[x0], v88\n\tv_mov_b32 %[y0], v90\n\ts_andn2_b64 %[p0], %[p0], vcc\n\ts_mov_b64 exec, %[p1]\n\tv_xor_b32 %[t0], %[tag], v93\n\tv_xor_b32 %[t1], %[tag], v95\n\tv_or_b32 %[t0], %[t0], %[t1]\n\tv_cmp_eq_u32 vcc, 0, %[t0]\n\ts_mov_b64 exec, vcc\n\tv_mov_b32 %[x1], v92\n\tv_mov_b32 %[y1], v94\n\ts_andn2_b64 %[p1], %[p1], vcc\n\ts_mov_b64 exec, %[p2]\n\tv_xor_b32 %[t0], %[tag], v97\n\tv_xor_b32 %[t1], %[tag], v99\n\tv_or_b32 %[t0], %[t0], %[t1]\n\tv_cmp_eq_u32 vcc, 0, %[t0]\n\ts_mov_b64 exec, vcc\n\tv_mov_b32 %[x2], v96\n\tv_mov_b32 %[y2], v98\n\ts_andn2_b64 %[p2], %[p2], vcc\n\ts_mov_b64 exec, %[p3]\n\tv_xor_b32 %[t0], %[tag], v101\n\tv_xor_b32 %[t1], %[tag], v103\n\tv_or_b32 %[t0], %[t0], %[t1]\n\tv_cmp_eq_u32 vcc, 0, %[t0]\n\ts_mov_b64 exec, vcc\n\tv_mov_b32 %[x3], v100\n\tv_mov_b32 %[y3], v102\n\ts_andn2_b64 %[p3], %[p3], vcc\n\ts_mov_b64 exec, %[p4]\n\tv_xor_b32 %[t0], %[tag], v105\n\tv_xor_b32 %[t1], %[tag], v107\n\tv_or_b32 %[t0], %[t0], %[t1]\n\tv_cmp_eq_u32 vcc, 0, %[t0]\n\ts_mov_b64 exec, vcc\n\tv_mov_b32 %[x4], v104\n\tv_mov_b32 %[y4], v106\n\ts_andn2_b64 %[p4], %[p4], vcc\n\ts_or_b64 %[pa], %[p0], %[p1]\n\ts_or_b64 %[pa], %[pa], %[p2]\n\ts_or_b64 %[pa], %[pa], %[p3]\n\ts_or_b64 %[pa], %[pa], %[p4]\n\ts_cbranch_scc0 4f\n\ts_cmp_eq_u64 %[p0], 0\n\ts_cselect_b64 exec, 1, %[p0]\n\tglobal_load_dwordx4 v[88:91], %[off], %[base] sc1\n\ts_cmp_eq_u64 %[p1], 0\n\ts_cselect_b64 exec, 1, %[p1]\n\tglobal_load_dwordx4 v[92:95], %[off], %[base] offset:1024 sc1\n\ts_cmp_eq_u64 %[p2], 0\n\ts_cselect_b64 exec, 1, %[p2]\n\tglobal_load_dwordx4 v[96:99], %[off], %[base] offset:2048 sc1\n\ts_cmp_eq_u64 %[p3], 0\n\ts_cselect_b64 exec, 1, %[p3]\n\tglobal_load_dwordx4 v[100:103], %[off], %[base] offset:3072 sc1\n\ts_cmp_eq_u64 %[p4], 0\n\ts_cselect_b64 exec, 1, %[p4]\n\tglobal_load_dwordx4 v[104:107], %[off5], %[base] sc1\n\ts_waitcnt vmcnt(10)\n\ts_mov_b64 exec, %[p0]\n\tv_xor_b32 %[t0], %[tag], v109\n\tv_xor_b32 %[t1], %[tag], v111\n\tv_or_b32 %[t0], %[t0], %[t1]\n\tv_cmp_eq_u32 vcc, 0, %[t0]\n\ts_mov_b64 exec, vcc\n\tv_mov_b32 %[x0], v108\n\tv_mov_b32 %[y0], v110\n\ts_andn2_b64 %[p0], %[p0], vcc\n\ts_mov_b64 exec, %[p1]\n\tv_xor_b32 %[t0], %[tag], v113\n\tv_xor_b32 %[t1], %[tag], v115\n\tv_or_b32 %[t0], %[t0], %[t1]\n\tv_cmp_eq_u32 vcc, 0, %[t0]\n\ts_mov_b64 exec, vcc\n\tv_mov_b32 %[x1], v112\n\tv_mov_b32 %[y1], v114\n\ts_andn2_b64 %[p1], %[p1], vcc\n\ts_mov_b64 exec, %[p2]\n\tv_xor_b32 %[t0], %[tag], v117\n\tv_xor_b32 %[t1], %[tag], v119\n\tv_or_b32 %[t0], %[t0], %[t1]\n\tv_cmp_eq_u32 vcc, 0, %[t0]\n\ts_mov_b64 exec, vcc\n\tv_mov_b32 %[x2], v116\n\tv_mov_b32 %[y2], v118\n\ts_andn2_b64 %[p2], %[p2], vcc\n\ts_mov_b64 exec, %[p3]\n\tv_xor_b32 %[t0], %[tag], v121\n\tv_xor_b32 %[t1], %[tag], v123\n\tv_or_b32 %[t0], %[t0], %[t1]\n\tv_cmp_eq_u32 vcc, 0, %[t0]\n\ts_mov_b64 exec, vcc\n\tv_mov_b32 %[x3], v120\n\tv_mov_b32 %[y3], v122\n\ts_andn2_b64 %[p3], %[p3], vcc\n\ts_mov_b64 exec, %[p4]\n\tv_xor_b32 %[t0], %[tag], v125\n\tv_xor_b32 %[t1], %[tag], v127\n\tv_or_b32 %[t0], %[t0], %[t1]\n\tv_cmp_eq_u32 vcc, 0, %[t0]\n\ts_mov_b64 exec, vcc\n\tv_mov_b32 %[x4], v124\n\tv_mov_b32 %[y4], v126\n\ts_andn2_b64 %[p4], %[p4], vcc\n\ts_or_b64 %[pa], %[p0], %[p1]\n\ts_or_b64 %[pa], %[pa], %[p2]\n\ts_or_b64 %[pa], %[pa], %[p3]\n\ts_or_b64 %[pa], %[pa], %[p4]\n\ts_cbranch_scc0 4f\n\ts_cmp_eq_u64 %[p0], 0\n\ts_cselect_b64 exec, 1, %[p0]\n\tglobal_load_dwordx4 v[108:111], %[off], %[base] sc1\n\ts_cmp_eq_u64 %[p1], 0\n\ts_cselect_b64 exec, 1, %[p1]\n\tglobal_load_dwordx4 v[112:115], %[off], %[base] offset:1024 sc1\n\ts_cmp_eq_u64 %[p2], 0\n\ts_cselect_b64 exec, 1, %[p2]\n\tglobal_load_dwordx4 v[116:119], %[off], %[base] offset:2048 sc1\n\ts_cmp_eq_u64 %[p3], 0\n\ts_cselect_b64 exec, 1, %[p3]\n\tglobal_load_dwordx4 v[120:123], %[off], %[base] offset:3072 sc1\n\ts_cmp_eq_u64 %[p4], 0\n\ts_cselect_b64 exec, 1, %[p4]\n\tglobal_load_dwordx4 v[124:127], %[off5], %[base] sc1\n\ts_sub_u32 %[cnt], %[cnt], 1\n\ts_cmp_lg_u32 %[cnt], 0\n\ts_cbranch_scc1 1b\n4:\n\ts_mov_b64 exec, -1"
               : [x0] "+&v"(x[0]), [y0] "+&v"(y[0]), [x1] "+&v"(x[1]), [y1] "+&v"(y[1]), [x2] "+&v"(x[2]), [y2] "+&v"(y[2]), [x3] "+&v"(x[3]), [y3] "+&v"(y[3]), [x4] "+&v"(x[4]), [y4] "+&v"(y[4]), [t0] "=&v"(t0), [t1] "=&v"(t1), [cnt] "+&s"(cnt), [p0] "=&s"(p[0]), [p1] "=&s"(p[1]), [p2] "=&s"(p[2]), [p3] "=&s"(p[3]), [p4] "=&s"(p[4]), [pa] "=&s"(pa)
               : [off] "v"(off), [off5] "v"(off5), [base] "s"(base), [tag] "s"(tag), [gap] "n"(GAP)
               : "memory", "vcc", "scc", "v68", "v69", "v70", "v71", "v72", "v73", "v74", "v75", "v76", "v77", "v78", "v79", "v80", "v81", "v82", "v83", "v84", "v85", "v86", "v87", "v88", "v89", "v90", "v91", "v92", "v93", "v94", "v95", "v96", "v97", "v98", "v99", "v100", "v101", "v102", "v103", "v104", "v105", "v106", "v107", "v108", "v109", "v110", "v111", "v112", "v113", "v114", "v115", "v116", "v117", "v118", "v119", "v120", "v121", "v122", "v123", "v124", "v125", "v126", "v127");
}
template <int CTRL> __device__ __forceinline__ float dppx(float x) { return __builtin_bit_cast(float, __builtin_amdgcn_update_dpp(0, __builtin_bit_cast(int, x), CTRL, 0xf, 0xf, true)); }
__device__ __forceinline__ bool none(unsigned bad) { return __builtin_amdgcn_ballot_w64(bad != 0u) == 0ull; }

__global__ void __launch_bounds__(NTHR, 4) fused_decode(Params p) {
  __shared__ __attribute__((aligned(16))) unsigned char lds_raw[L_END + LDS_EXTRA];
  LAS unsigned char* L = (LAS unsigned char*)lds_raw;
  unsigned long long pc0, tt0;
  asm volatile("s_memrealtime %0" : "=s"(tt0));
  asm volatile("s_getpc_b64 %0" : "=s"(pc0));
  const int tid = threadIdx.x, lane = tid & 63, wave = __builtin_amdgcn_readfirstlane(tid >> 6);
  const int b = blockIdx.x, g = b & 15, s = b >> 4, br = g >> 3, kvh = g & 7;
  const int go = g ^ XG, kvo = kvh ^ XG;
  const int lr = lane >> 4, lc = lane & 15;
  const size_t kvoff = ((size_t)kvh * SK + (SK - WIN) + s * 256) * DH;
  const u16 *px = p.x, *pgw = p.gw, *pwq = p.wq[br], *pk = p.k[br], *pv = p.v[br], *pwo = p.wo[br];
  u16* pout = p.out; unsigned char* pws = p.ws;
  asm volatile("s_waitcnt lgkmcnt(0)" : "+s"(px), "+s"(pgw), "+s"(pwq), "+s"(pk), "+s"(pv), "+s"(pwo), "+s"(pout), "+s"(pws), "+s"(tt0));
  const unsigned t0 = (unsigned)tt0;

  if (wave >= NCW && wave < NCW + NLW) {
    const int j = wave - NCW;
    if (j == 0) {
      *(LAS unsigned*)(L + L_CNT + 4 * lane) = (L_CNT + 4 * lane == C_TG || L_CNT + 4 * lane == C_TQ) ? t0 : 0u;
      if (lane < 32) { *(LAS unsigned*)(L + L_MX + 4 * lane) = EMPTY; *(LAS unsigned*)(L + L_O + 4 * lane) = lane < 16 ? EMPTY : 0u; }
    }
    const unsigned char* kb = (const unsigned char*)(pk + kvoff) + j * 1024;
    const unsigned char* vb = (const unsigned char*)(pv + kvoff) + j * 1024;
    const GAS v4u* wb = (const GAS v4u*)(pwo + ((size_t)kvo * 512 + s * 32 + j * 8) * DM) + lane;
    const unsigned ko = lr * 256 + ((lc ^ (4 * j + lr)) << 4);
    const unsigned vo = lr * 256 + ((lc ^ ((lr | ((j & 2) << 1)) << 1)) << 4);
    const unsigned ldum = L_DUM + 8 * lane;
    BARRIER();
    if (KDELAY) __builtin_amdgcn_s_sleep(KDELAY);
    const unsigned rot = RA * g + RB * j + RC * s + ((RD * g) >> 1);
    const unsigned rw = ROTW ? rot : 0;
    dma_run_rot<KWND, DK>(kb, ko, L_K + j * 1024, 0, 16, rot, C_TG, TK0);
    dma_run_rot<VWND, DV>(vb, vo, L_V + j * 1024, 0, VWND - 1, rot, C_TG, TV0);
    asm volatile("s_waitcnt vmcnt(%0)" :: "n"(VWND - 1) : "memory");
    *(volatile LAS unsigned*)(L + (lane == 0 ? C_KL + 4 * j : ldum)) = 1u;
    STAMPX(1);
    dma_run_rot<VWND, DV>(vb, vo, L_V + j * 1024, VWND - 1, 8, rot, C_TG, TV0 + (VWND - 1) * DV);
    asm volatile("s_waitcnt vmcnt(%0)" :: "n"(VWND - 1) : "memory");
    *(volatile LAS unsigned*)(L + (lane == 0 ? C_VH + 4 * j : ldum)) = 1u;
    dma_run_rot<VWND, DV>(vb, vo, L_V + j * 1024, VWND + 7, 9 - VWND, rot, C_TG, TV0 + (VWND + 7) * DV);
    v4u w[8];
    if (WOA) {
      if (WOGATE) wait_until(C_TG, WOGATE);
#pragma unroll
      for (int i = 0; i < WOA; ++i) {
        if (WW) asm volatile("s_waitcnt vmcnt(%0)" :: "n"(WW ? WW - 1 : 0) : "memory");
        w[i] = __builtin_nontemporal_load(wb + ((i + rw) & 7) * 64);
        if (WW) RELOAD();
      }
      RELOAD();
    }
    asm volatile("s_waitcnt vmcnt(%0)" :: "n"(WOA) : "memory");
    STAMPX(8);
    BARRIER();
    if (WOM) {
#pragma unroll
      for (int i = WOA; i < WOA + WOM; ++i) w[i] = __builtin_nontemporal_load(wb + ((i + rw) & 7) * 64);
      RELOAD();
    }
    if (WOA + WOM < 8) {
#pragma clang loop unroll(disable)
      for (unsigned i = 0; i < SPIN_CAP; ++i) if (none(*(volatile LAS unsigned*)(L + C_OP + 4 * (lane & 7)) == 0u)) break;
      RELOAD();
#pragma unroll
      for (int i = WOA + WOM; i < 8; ++i) w[i] = __builtin_nontemporal_load(wb + ((i + rw) & 7) * 64);
      RELOAD();
    } else if (WODELAY) __builtin_amdgcn_s_sleep(WODELAY);
    STAMPX(4);
#pragma unroll
    for (int i = 0; i < 8; ++i) {
      { const unsigned r = (i + rw) & 7; *(LAS v4u*)(L + L_WO + (8 * j + r) * 1024 + ((lane ^ (2 * r)) << 4)) = w[i]; }
      if (i == WOTRIG - 1) { RELOAD(); *(volatile LAS unsigned*)(L + (lane == 0 ? C_WT + 4 * j : ldum)) = 1u; RELOAD(); }

    }
    *(volatile LAS unsigned*)(L + (lane == 0 ? C_WO + 4 * j : ldum)) = 1u;
    STAMPX(7);
    return;
  }

  const __amdgpu_buffer_rsrc_t wsr = __builtin_amdgcn_make_buffer_rsrc(pws, 0, W_END, 0x00020000);
  if (wave == NCW + NLW) {
    unsigned c0 = 0, c1 = 0;
    if (CODEPF && b < CODEPF) {
      const GAS unsigned char* cp = (const GAS unsigned char*)(pc0 & ~127ull) + lane * 128;
      c0 = *(const GAS unsigned*)cp; if (lane < CODEPF_TAIL) c1 = *(const GAS unsigned*)(cp + 8192);
    }
    asm volatile("s_barrier" ::: "memory");
    unsigned ep;
    asm volatile("s_load_dword %0, %1, 0x0\n\ts_waitcnt lgkmcnt(0)" : "=s"(ep) : "s"(pws + W_E) : "memory");
    const unsigned tag = ep + 1u;
    {
    __builtin_amdgcn_raw_buffer_store_b64(v2u{t0, tag}, wsr, (b < 8 && lane == 0) ? W_T + 8 * b : W_END, 0, 16);
    asm volatile("" :: "v"(c0), "v"(c1));
    __builtin_amdgcn_s_sleep(TSLEEP);
    v2u ts;
#pragma clang loop unroll(disable)
    for (unsigned i = 0; i < SPIN_CAP; ++i) { RELOAD(); ts = __builtin_amdgcn_raw_buffer_load_b64(wsr, W_T + 8 * (lane & 7), 0, 16); if (none(ts[1] ^ tag)) break; }
    int d = (int)(ts[0] - t0);
    const unsigned tq = t0 + (unsigned)__builtin_amdgcn_readlane(d, kvo);
    unsigned tg;
    if (TGMEAN) {
      int dm = d;
      dm = max(dm, __builtin_amdgcn_mov_dpp(dm, 0xB1, 0xf, 0xf, true)); dm = max(dm, __builtin_amdgcn_mov_dpp(dm, 0x4E, 0xf, 0xf, true)); dm = max(dm, __builtin_amdgcn_mov_dpp(dm, 0x124, 0xf, 0xf, true));
      d += __builtin_amdgcn_mov_dpp(d, 0xB1, 0xf, 0xf, true); d += __builtin_amdgcn_mov_dpp(d, 0x4E, 0xf, 0xf, true); d += __builtin_amdgcn_mov_dpp(d, 0x124, 0xf, 0xf, true);
      tg = t0 + (unsigned)(((4 - TGMAX) * (__builtin_amdgcn_readfirstlane(d) >> 3) + TGMAX * __builtin_amdgcn_readfirstlane(dm)) >> 2);
    } else {
      d = min(d, __builtin_amdgcn_mov_dpp(d, 0xB1, 0xf, 0xf, true)); d = min(d, __builtin_amdgcn_mov_dpp(d, 0x4E, 0xf, 0xf, true)); d = min(d, __builtin_amdgcn_mov_dpp(d, 0x124, 0xf, 0xf, true));
      tg = t0 + (unsigned)min(__builtin_amdgcn_readfirstlane(d), 0);
    }
    *(volatile LAS unsigned*)(L + (lane == 0 ? C_TG : lane == 1 ? C_TQ : L_DUM + 8 * lane)) = lane == 1 ? tq : tg;
    }
    unsigned moff = W_ML + ((go * 16 + ((lc & 12) | ((lc + lr) & 3))) * 4 + (s >> 2)) * 16;
    unsigned oaddr = lane < 16 ? L_O + 16 * ((lane >> 1) & 3) + 8 * (lane >> 3) + 4 * (lane & 1) : L_DUM + 8 * lane;
    unsigned ooff = W_O + (go * 16 + s) * 4096 + lane * 16;
    asm volatile("" : "+v"(moff), "+v"(oaddr), "+v"(ooff));
    BARRIER();
    __builtin_amdgcn_s_setprio(3);
    float x[4], y[4], m, l;
    if (WOTRIG) {
#pragma clang loop unroll(disable)
      for (unsigned i = 0; i < SPIN_CAP; ++i) if (none(*(volatile LAS unsigned*)(L + C_WT + 4 * (lane & 3)) == 0u)) break;
      RELOAD();
    } else if (TO) wait_until(C_TG, TO); else if (TUNE1) __builtin_amdgcn_s_sleep(TUNE1);
    poll5<GAP1>(x, y, m, l, pws, ooff, moff, tag);
    float M = m;
    M = fmaxf(M, dppx<0x128>(M)); M = fmaxf(M, dppx<0x124>(M)); M = fmaxf(M, dppx<0x122>(M)); M = fmaxf(M, dppx<0x121>(M));
    const float e = __builtin_amdgcn_exp2f(m - M);
    float Ls = e * l;
    Ls += dppx<0x128>(Ls); Ls += dppx<0x124>(Ls); Ls += dppx<0x122>(Ls); Ls += dppx<0x121>(Ls);
    const float wn = e * __builtin_amdgcn_rcpf(Ls);
    const float wgt[4] = {dppx<0x150>(wn), dppx<0x154>(wn), dppx<0x158>(wn), dppx<0x15C>(wn)};
    float ov = rs32(fmaf(wgt[0], x[0], wgt[1] * x[1]) + fmaf(wgt[2], x[2], wgt[3] * x[3]), fmaf(wgt[0], y[0], wgt[1] * y[1]) + fmaf(wgt[2], y[2], wgt[3] * y[3]));
    ov = rs16(ov, ov);
    const auto sw = __builtin_amdgcn_permlane32_swap(f2u(ov), f2u(ov), false, false);
    *(LAS unsigned*)(L + oaddr) = cvtpk(u2f(sw[0]), u2f(sw[1]));
    STAMP(10);
    return;
  }

  const int cw = wave;
  __builtin_amdgcn_s_setprio(2);
  STAMP0(0);
  unsigned tag;
  float gate = 0.f;
  const int q4 = lc >> 2, p4 = lane & 3;
  const bool qb0 = (lane & 4) != 0, qb1 = (lane & 8) != 0;
  const unsigned dum = L_DUM + 8 * lane;
  {
    const int kq = s >> 2, hq = s & 3, row0 = 128 * kq + 16 * cw;
    const GAS v4u* wp = (const GAS v4u*)(pwq + (size_t)(row0 + lr) * NQ + kvo * 512 + 128 * hq + 8 * lc);
    v4u w[4];
#pragma unroll
    for (int i = 0; i < 4; ++i) w[i] = __builtin_nontemporal_load(wp + (size_t)i * (NQ / 2));
    typedef unsigned s8u __attribute__((ext_vector_type(8)));
    s8u xs;
    asm volatile("s_load_dwordx8 %0, %1, 0x0" : "=s"(xs) : "s"(px + row0) : "memory");
    const bool gw = wave == 0 && b < NRED;
    const v4u gx = *(gw ? (const GAS v4u*)(px + 8 * lane) : wp), gg = *(gw ? (const GAS v4u*)(pgw + 8 * lane) : wp);
    RELOAD();
    unsigned tlb = 0;
    if (TLBPF) {
      const unsigned char *a0 = (const unsigned char*)(pk + kvoff), *a1 = (const unsigned char*)(pv + kvoff), *a2 = (const unsigned char*)(pwo + ((size_t)kvo * 512 + s * 32) * DM), *a3 = pws + W_F + b * 4096;
      asm volatile("" : "+s"(a0), "+s"(a1), "+s"(a2), "+s"(a3));
      const unsigned char* a = (wave & 2) ? ((wave & 1) ? a3 : a2) : ((wave & 1) ? a1 : a0);
      asm volatile("s_load_dword %0, %1, 0x0" : "=s"(tlb) : "s"(a) : "memory");
    }
    asm volatile("s_barrier" ::: "memory");
    unsigned ep;
    asm volatile("s_load_dword %0, %1, 0x0" : "=s"(ep) : "s"(pws + W_E) : "memory");
    asm volatile("s_waitcnt lgkmcnt(0)" : "+s"(xs), "+s"(ep), "+s"(tlb), "+v"(w[0]), "+v"(w[1]) :: "memory");
    tag = ep + 1u;
    const unsigned xd[4] = {lr & 2 ? xs[1] : xs[0], lr & 2 ? xs[3] : xs[2], lr & 2 ? xs[5] : xs[4], lr & 2 ? xs[7] : xs[6]};
    const unsigned xsel = lr & 1 ? 0x07060302u : 0x05040100u;
    float acc[8];
#pragma unroll
    for (int k = 0; k < 8; ++k) acc[k] = 0.f;
#pragma unroll
    for (int i = 0; i < 4; i += 2) {
      const unsigned xp = __builtin_amdgcn_perm(xd[i + 1], xd[i], xsel);
#pragma unroll
      for (int d = 0; d < 4; ++d) {
        acc[2 * d] = dot2(__builtin_amdgcn_perm(w[i + 1][d], w[i][d], 0x05040100u), xp, acc[2 * d]);
        acc[2 * d + 1] = dot2(__builtin_amdgcn_perm(w[i + 1][d], w[i][d], 0x07060302u), xp, acc[2 * d + 1]);
      }
    }
    const float r0 = rs32(acc[0], acc[4]), r1 = rs32(acc[1], acc[5]), r2 = rs32(acc[2], acc[6]), r3 = rs32(acc[3], acc[7]);
    const float u0 = rs16(r0, r2), u1 = rs16(r1, r3);
    *(LAS f32x2*)(L + L_QP + (cw * 128 + 8 * lc + 4 * (lane >> 5) + 2 * (lr & 1)) * 4) = f32x2{u0, u1};
    if (__builtin_expect(lds_add(C_QP) == NCW - 1, 1)) {
      f32x2 qs = {0.f, 0.f};
#pragma unroll
      for (int i = 0; i < 8; i += 2) qs += *(const LAS f32x2*)(L + L_QP + (i * 128 + 2 * lane) * 4) + *(const LAS f32x2*)(L + L_QP + ((i + 1) * 128 + 2 * lane) * 4);
      __builtin_amdgcn_raw_buffer_store_b128(v4u{f2u(qs[0]), tag, f2u(qs[1]), tag}, wsr, W_Q + (go * 4 + kq) * 4096 + (128 * hq + 2 * lane) * 8, 0, 16);
      STAMP(2);
    }
    v4u gx_ = gx, gg_ = gg;
    asm volatile("" : "+v"(gx_), "+v"(gg_));
    if (__builtin_expect(gw, 1)) {
      float z = 0.f;
#pragma unroll
      for (int d = 0; d < 4; ++d) z += rbf(bflo(gx_[d]) * bflo(gg_[d])) + rbf(bfhi(gx_[d]) * bfhi(gg_[d]));
      z += dpp<0xB1>(z); z += dpp<0x4E>(z); z += dpp<0x124>(z); z += dpp<0x128>(z); z = xrow_sum(z);
      gate = rbf(__builtin_amdgcn_rcpf(1.f + __expf(-rbf(z))));
    }
  }
  {
    const int off = W_Q + g * 16384 + (lane >> 5) * 4096 + (64 * cw + 2 * (lane & 31)) * 8;
    v4u a, a2;
    unsigned pia = lane == 0 ? C_PI + 4 * cw : dum;
    unsigned pca = lane == 0 ? C_PC : dum;
    asm volatile("" : "+v"(pia), "+v"(pca));
    if (TQ) wait_until(C_TQ, TQ); else if (TUNEQ) __builtin_amdgcn_s_sleep(TUNEQ);
#pragma clang loop unroll(disable)
    for (unsigned i = 0; i < SPIN_CAP; ++i) {
      RELOAD(); a = ld_sc1(wsr, off); a2 = ld_sc1(wsr, off + 8192);
      if (GATE1 | GATE2) { RELOAD(); *(volatile LAS unsigned*)(L + pia) = i + 1; RELOAD(); }
      if (KOPEN) { asm volatile("ds_add_u32 %0, %1" :: "v"(pca), "v"(1u) : "memory"); pca = dum; }
      if (none(tag_bad(a, tag) | tag_bad(a2, tag))) break;
    }
    if (GATE1 | GATE2) *(volatile LAS unsigned*)(L + pia) = SPIN_CAP;
    const float qv = rs32(u2f(a[0]) + u2f(a2[0]), u2f(a[2]) + u2f(a2[2]));
    *(LAS u16*)(L + L_Q + (cw >> 1) * Q_STR + ((cw & 1) * 64 + 2 * (lane & 31) + (lane >> 5)) * 2) = (u16)cvtpk(qv, qv);
    *(volatile LAS unsigned*)(L + (lane == 0 ? C_FLAG + 4 * cw : dum)) = 1u;
    STAMPX(3);
  }
  unsigned ka = L_K + (32 * cw + lc) * 256 + ((lr ^ lc) << 4), qa = L_Q + p4 * Q_STR + 16 * lr, pa = L_P + p4 * P_STR + 16 * lr;
  unsigned pw = (lane & 12) == 0 ? L_P + p4 * P_STR + (32 * cw + 4 * lr) * 2 : dum, pw2 = (lane & 12) == 0 ? pw + 32 : dum;
  unsigned va = L_V + (8 * lr + q4) * 256 + (((2 * cw + (p4 >> 1)) ^ ((q4 | ((lr & 1) << 2)) << 1)) << 4) + 8 * (p4 & 1);
  unsigned wa = L_WO + (4 * lr + q4) * 1024 + (((8 * cw + (p4 >> 1)) ^ ((q4 | ((lr & 1) << 2)) << 1)) << 4) + 8 * (p4 & 1);
  unsigned oo = W_O + ((g * 16 + 4 * p4 + (cw >> 1)) * 16 + s) * 256 + (16 * (cw & 1) + 4 * lr + q4) * 8;
  unsigned fo = NRED == 128 ? W_F + (16 * cw + 4 * p4 + lr) * 8192 + (go * 16 + s) * 32 + q4 * 8 : W_F + ((4 * cw + p4) * 256 + go * 16 + s) * 128 + (4 * lr + q4) * 8;
  unsigned opa = lane == 0 ? C_OP + 4 * cw : dum;
  asm volatile("" : "+v"(opa));
  unsigned ba0 = p4 == 0 ? L_O + 16 * lr : L_Z, ba1 = p4 == 1 ? L_O + 16 * lr : L_Z, ba2 = p4 == 2 ? L_O + 16 * lr : L_Z, ba3 = p4 == 3 ? L_O + 16 * lr : L_Z;
  asm volatile("" : "+v"(ka), "+v"(qa), "+v"(pw), "+v"(pw2), "+v"(pa), "+v"(va), "+v"(wa), "+v"(oo), "+v"(fo), "+v"(ba0), "+v"(ba1), "+v"(ba2), "+v"(ba3));
#pragma clang loop unroll(disable)
  for (unsigned i = 0; i < SPIN_CAP; ++i) if (none(*(volatile LAS unsigned*)(L + C_FLAG + 4 * (lane < 12 ? lane : 0)) == 0u)) break;
  RELOAD();
  STAMP0(5);
  float mc;
  {
    v4u qf[4], kf[2][4];
#pragma unroll
    for (int ks = 0; ks < 4; ++ks) { kf[0][ks] = *(const LAS v4u*)(L + (ka ^ (64 * ks))); qf[ks] = *(const LAS v4u*)(L + qa + 64 * ks); kf[1][ks] = *(const LAS v4u*)(L + (ka ^ (64 * ks)) + 4096); }
    __builtin_amdgcn_sched_barrier(0);
    f32x4 sc[2] = {{0.f, 0.f, 0.f, 0.f}, {0.f, 0.f, 0.f, 0.f}};
#pragma unroll
    for (int ks = 0; ks < 4; ++ks) { sc[0] = mfma(kf[0][ks], qf[ks], sc[0]); sc[1] = mfma(kf[1][ks], qf[ks], sc[1]); }
    float m = fmaxf(fmaxf(fmaxf(sc[0][0], sc[0][1]), fmaxf(sc[0][2], sc[0][3])), fmaxf(fmaxf(sc[1][0], sc[1][1]), fmaxf(sc[1][2], sc[1][3])));
    m = xrow_max(m);
    *(volatile LAS float*)(L + (lane < 4 ? L_MX + (lane * 8 + cw) * 4 : dum)) = m;
    constexpr float C2 = 0.08838834764831845f * 1.4426950408889634f;
    v4u mx0, mx1;
#pragma clang loop unroll(disable)
    for (unsigned i = 0; i < SPIN_CAP; ++i) {
      mx0 = *(volatile LAS v4u*)(L + L_MX + 32 * p4); mx1 = *(volatile LAS v4u*)(L + L_MX + 32 * p4 + 16);
      const unsigned top = max(max(max(mx0[0], mx0[1]), max(mx0[2], mx0[3])), max(max(mx1[0], mx1[1]), max(mx1[2], mx1[3])));
      if (none(top == EMPTY)) break;
    }
    mc = fmaxf(fmaxf(fmaxf(u2f(mx0[0]), u2f(mx0[1])), fmaxf(u2f(mx0[2]), u2f(mx0[3]))), fmaxf(fmaxf(u2f(mx1[0]), u2f(mx1[1])), fmaxf(u2f(mx1[2]), u2f(mx1[3])))) * C2;
    float ls = 0.f;
    v2u pk[2];
#pragma unroll
    for (int t = 0; t < 2; ++t) {
      float e[4];
#pragma unroll
      for (int r = 0; r < 4; ++r) { e[r] = __builtin_amdgcn_exp2f(fmaf(sc[t][r], C2, -mc)); ls += e[r]; }
      pk[t] = v2u{cvtpk(e[0], e[1]), cvtpk(e[2], e[3])};
    }
    ls = xrow_sum(ls);
    *(LAS v2u*)(L + pw) = pk[0]; *(LAS v2u*)(L + pw2) = pk[1];
    *(LAS float*)(L + (lane < 4 ? L_LS + (lane * 8 + cw) * 4 : dum)) = ls;
    *(volatile LAS unsigned*)(L + (lane == 0 ? C_PF + 4 * cw : dum)) = 1u;
    STAMPX(6);
  }
  f32x4 o = {0.f, 0.f, 0.f, 0.f};
  v4u pf[8];
  {
#pragma clang loop unroll(disable)
    for (unsigned i = 0; i < SPIN_CAP; ++i) if (none(*(volatile LAS unsigned*)(L + C_PF + 4 * (lane < 12 ? lane : 0)) == 0u)) break;
    RELOAD();
    v4u vf[4];
#pragma unroll
    for (int ks = 0; ks < 4; ++ks) { vf[ks] = tr_pair(L + va + 8192 * ks, 1024); pf[ks] = *(const LAS v4u*)(L + pa + 64 * ks); }
#pragma unroll
    for (int ks = 4; ks < 8; ++ks) pf[ks] = *(const LAS v4u*)(L + pa + 64 * ks);
    if (__builtin_expect(cw == NCW - 1, 1)) {
      const f32x4 l0 = *(const LAS f32x4*)(L + L_LS + 32 * p4), l1 = *(const LAS f32x4*)(L + L_LS + 32 * p4 + 16);
      const v4u ml = {f2u(mc), tag, f2u(((l0[0] + l0[1]) + (l0[2] + l0[3])) + ((l1[0] + l1[1]) + (l1[2] + l1[3]))), tag};
      __builtin_amdgcn_raw_buffer_store_b128(ml, wsr, lane < 4 ? W_ML + ((g * 16 + s) * 4 + lane) * 16 : W_END, 0, 16);
    }
#pragma unroll
    for (int ks = 0; ks < 4; ++ks) o = mfma(vf[ks], pf[ks], o);
    asm volatile("" : "+v"(o), "+v"(pf[4]), "+v"(pf[5]), "+v"(pf[6]), "+v"(pf[7]));
  }
  BARRIER();
  {
    v4u vf[4];
#pragma unroll
    for (int ks = 0; ks < 4; ++ks) vf[ks] = tr_pair(L + va + 8192 * (ks + 4), 1024);
    __builtin_amdgcn_sched_barrier(0);
#pragma unroll
    for (int ks = 0; ks < 4; ++ks) o = mfma(vf[ks], pf[ks + 4], o);
    st_granule(pws, oo, pick4(o, qb0, qb1), tag);
    if (WOA + WOM < 8) { RELOAD(); *(volatile LAS unsigned*)(L + opa) = 1u; }
    STAMPX(9);
  }
  {
#pragma clang loop unroll(disable)
    for (unsigned i = 0; i < SPIN_CAP; ++i) if (none(*(volatile LAS unsigned*)(L + C_WO + 4 * (lane & 3)) == 0u)) break;
    RELOAD();
    v4u wf[4];
#pragma unroll
    for (int t = 0; t < 4; ++t) wf[t] = tr_pair(L + (wa ^ (32 * t)), 16384);
    asm volatile("" : "+v"(wf[0]), "+v"(wf[1]), "+v"(wf[2]), "+v"(wf[3]));
    v4u b0, b1, b2, b3;
#pragma clang loop unroll(disable)
    for (unsigned i = 0; i < SPIN_CAP; ++i) {
      b0 = *(volatile LAS v4u*)(L + ba0); b1 = *(volatile LAS v4u*)(L + ba1); b2 = *(volatile LAS v4u*)(L + ba2); b3 = *(volatile LAS v4u*)(L + ba3);
      if (none(((b0[0] | b1[0]) | (b2[0] | b3[0])) == EMPTY)) break;
    }
    f32x4 r = {0.f, 0.f, 0.f, 0.f};
    r = mfma(wf[0], b0, r); r = mfma(wf[1], b1, r); r = mfma(wf[2], b2, r); r = mfma(wf[3], b3, r);
    st_granule(pws, fo, pick4(r, qb0, qb1), tag);
    STAMPX(12);
  }
  if (__builtin_expect(b < NRED, 1)) {
    unsigned ra_ = NRED == 128 ? ((lane & 30) == 0 ? L_RED + ((2 * (lane & 1) + (lane >> 5)) * 8 + cw) * 4 : dum) : ((lane & 24) == 0 ? L_RED + ((2 * (lane & 7) + (lane >> 5)) * 8 + cw) * 4 : dum);
    float g1 = rbf(1.f - gate);
    asm volatile("" : "+v"(ra_), "+v"(g1));
    if (TF) wait_until(C_TG, TF); else if (TUNE2) __builtin_amdgcn_s_sleep(TUNE2);
    float t;
    if (NRED == 128) {
      float x, y;
      POLL1(NSF)<GAPF>(x, y, pws, W_F + b * 8192 + cw * 1024 + lane * 16, tag);
      t = rs32(x, y);
      t = rs16(t, t); t += dpp<0x128>(t); t += dpp<0x124>(t); t += dpp<0x4E>(t);
    } else {
      float x[4], y[4];
      poll4<GAP2>(x, y, pws, W_F + b * 32768 + cw * 4096 + lane * 16, tag);
      t = rs32((x[0] + x[1]) + (x[2] + x[3]), (y[0] + y[1]) + (y[2] + y[3]));
      t = rs16(t, t); t += dpp<0x128>(t);
    }
    *(LAS float*)(L + ra_) = t;
    STAMPX(13);
    BARRIER();
    if (__builtin_expect(cw == 0, 1)) {
      const f32x4 ra = *(const LAS f32x4*)(L + L_RED + 32 * lc), rb = *(const LAS f32x4*)(L + L_RED + 32 * lc + 16);
      const float A = rbf((ra[0] + ra[1]) + (ra[2] + ra[3])), B = rbf((rb[0] + rb[1]) + (rb[2] + rb[3]));
      const u16 res = (u16)cvtpk(rbf(gate * A) + rbf(g1 * B), 0.f);
      __builtin_amdgcn_raw_buffer_store_b16(res, __builtin_amdgcn_make_buffer_rsrc(pout, 0, 2 * DM, 0x00020000), lane < 512 / NRED ? (512 / NRED * b + lane) * 2 : 2 * DM, 0, 0);
      __builtin_amdgcn_raw_buffer_store_b32(tag, wsr, (b | lane) == 0 ? W_E : W_END, 0, 16);
      STAMP(14);
    }
  }
}

extern "C" void kernel_launch(void* const* d_in, const int* in_sizes, int n_in, void* d_out, int out_size, void* d_ws, size_t ws_size, hipStream_t stream) {
  Params p;
  p.x = (const u16*)d_in[0]; p.gw = (const u16*)d_in[10];
  p.wq[0] = (const u16*)d_in[3]; p.wq[1] = (const u16*)d_in[8];
  p.k[0] = (const u16*)d_in[1]; p.k[1] = (const u16*)d_in[6];
  p.v[0] = (const u16*)d_in[2]; p.v[1] = (const u16*)d_in[7];
  p.wo[0] = (const u16*)d_in[4]; p.wo[1] = (const u16*)d_in[9];
  p.out = (u16*)d_out; p.ws = (unsigned char*)d_ws;
  fused_decode<<<NWG, NTHR, 0, stream>>>(p);
}
```

```cpp
#include <hip/hip_runtime.h>
#include <hip/hip_bf16.h>

typedef unsigned short u16;
typedef unsigned v4u __attribute__((ext_vector_type(4)));
typedef unsigned v2u __attribute__((ext_vector_type(2)));
typedef float f32x2 __attribute__((ext_vector_type(2)));
typedef float f32x4 __attribute__((ext_vector_type(4)));
typedef short s16x4 __attribute__((ext_vector_type(4)));
typedef __bf16 bf16x2 __attribute__((ext_vector_type(2)));
typedef __bf16 bf16x8 __attribute__((ext_vector_type(8)));
#define GAS __attribute__((address_space(1)))
#define LAS __attribute__((address_space(3)))
#define RLX_AGENT __ATOMIC_RELAXED, __HIP_MEMORY_SCOPE_AGENT

constexpr int DM = 512, SK = 32768, DH = 128, WIN = 4096, NQ = 4096;
constexpr int NWG = 256, NCW = 8, NLW = 4, NTHR = 64 * (NCW + NLW + 1);
constexpr int L_K = 0, L_WO = 0, L_V = 65536, L_P = 131072, P_STR = 576, L_Q = L_P + 4 * P_STR, Q_STR = 320, L_QP = L_Q + 4 * Q_STR,
              L_MX = L_QP + 4096, L_LS = L_MX + 128, L_O = L_LS + 128, L_Z = L_O + 64, L_RED = L_Z + 64, L_CNT = L_RED + 512, L_DUM = L_CNT + 256, L_END = L_DUM + 512;
enum { C_FLAG = L_CNT, C_KL = L_CNT + 32, C_QP = L_CNT + 48, C_PF = L_CNT + 64, C_VH = L_CNT + 96, C_WO = L_CNT + 112, C_TG = L_CNT + 160, C_TQ = L_CNT + 164, C_OP = L_CNT + 192 };
constexpr unsigned EMPTY = 0xFFFFFFFFu;
constexpr unsigned W_E = 0, W_Q = 4096, W_ML = W_Q + 16 * 16384, W_O = W_ML + 16384, W_F = W_O + (1u << 20), W_T = W_F + (1u << 20), W_END = W_T + 4096;
constexpr unsigned SPIN_CAP = 1u << 20;

constexpr int XG = 4;
constexpr int KWND = 7, VWND = 8, WWND = 7;
constexpr int TK0 = 90, DK = 15, TV0 = 330, DV = 15;
constexpr int TW0 = 580, DW = 18, WOA = 6;
constexpr int TQ = 370, TUNEQ = 36;
constexpr int TUNE1 = 24, GAP1 = 8;
constexpr int TF = 895, TUNE2 = 18, GAP2 = 8;
constexpr int TSLEEP = 40, KDELAY = 8, CODEPF = 8, CODEPF_TAIL = 16;

struct Params { const u16* x; const u16* gw; const u16* wq[2]; const u16* k[2]; const u16* v[2]; const u16* wo[2]; u16* out; unsigned char* ws; };

__device__ __forceinline__ float bflo(unsigned w) { return __builtin_bit_cast(float, w << 16); }
__device__ __forceinline__ float bfhi(unsigned w) { return __builtin_bit_cast(float, w & 0xffff0000u); }
__device__ __forceinline__ unsigned cvtpk(float lo, float hi) { return __builtin_bit_cast(unsigned, __builtin_convertvector(f32x2{lo, hi}, bf16x2)); }
__device__ __forceinline__ float rbf(float f) { return bflo(cvtpk(f, f)); }
__device__ __forceinline__ float dot2(unsigned a, unsigned b, float acc) { return __builtin_amdgcn_fdot2_f32_bf16(__builtin_bit_cast(bf16x2, a), __builtin_bit_cast(bf16x2, b), acc, false); }
template <int CTRL> __device__ __forceinline__ float dpp(float x) { return __builtin_bit_cast(float, __builtin_amdgcn_mov_dpp(__builtin_bit_cast(int, x), CTRL, 0xf, 0xf, true)); }
__device__ __forceinline__ float u2f(unsigned u) { return __builtin_bit_cast(float, u); }
__device__ __forceinline__ unsigned f2u(float f) { return __builtin_bit_cast(unsigned, f); }
__device__ __forceinline__ float rs32(float a, float b) { auto r = __builtin_amdgcn_permlane32_swap(f2u(a), f2u(b), false, false); return u2f(r[0]) + u2f(r[1]); }
__device__ __forceinline__ float rs16(float a, float b) { auto r = __builtin_amdgcn_permlane16_swap(f2u(a), f2u(b), false, false); return u2f(r[0]) + u2f(r[1]); }
__device__ __forceinline__ float xrow_max(float x) {
  auto s = __builtin_amdgcn_permlane16_swap(f2u(x), f2u(x), false, false); x = fmaxf(u2f(s[0]), u2f(s[1]));
  auto t = __builtin_amdgcn_permlane32_swap(f2u(x), f2u(x), false, false); return fmaxf(u2f(t[0]), u2f(t[1]));
}
__device__ __forceinline__ float xrow_sum(float x) { x = rs16(x, x); return rs32(x, x); }
__device__ __forceinline__ f32x4 mfma(v4u a, v4u b, f32x4 c) { return __builtin_amdgcn_mfma_f32_16x16x32_bf16(__builtin_bit_cast(bf16x8, a), __builtin_bit_cast(bf16x8, b), c, 0, 0, 0); }
__device__ __forceinline__ v4u tr_pair(const LAS unsigned char* a0, int off2) {
  const s16x4 lo = __builtin_amdgcn_ds_read_tr16_b64_v4i16((LAS s16x4*)a0), hi = __builtin_amdgcn_ds_read_tr16_b64_v4i16((LAS s16x4*)(a0 + off2));
  return __builtin_bit_cast(v4u, __builtin_shufflevector(lo, hi, 0, 1, 2, 3, 4, 5, 6, 7));
}
__device__ __forceinline__ float pick4(f32x4 o, bool b0, bool b1) { const float x = b0 ? o[1] : o[0], y = b0 ? o[3] : o[2]; return b1 ? y : x; }
template <int WND, int DELTA> __device__ __forceinline__ void dma_run(const void* sbase, unsigned voff, unsigned ldst, unsigned p0, unsigned n, unsigned rot, unsigned taddr, unsigned tnext) {
  unsigned f, sf, vt;
  asm volatile("s_lshl_b32 %4, %0, 12\n\ts_add_u32 m0, %8, %4\n\ts_add_u32 %0, %0, %9\n"
               "1:\n\ts_waitcnt vmcnt(%10)\n"
               "2:\n\tds_read_b32 %3, %12\n\ts_memrealtime s[60:61]\n\ts_waitcnt lgkmcnt(0)\n\tv_readfirstlane_b32 %4, %3\n\ts_sub_u32 %4, s60, %4\n\ts_cmp_ge_i32 %4, %2\n\ts_cbranch_scc1 3f\n\ts_sleep 1\n\ts_branch 2b\n"
               "3:\n\ts_and_b32 %4, %0, 15\n\ts_lshl_b32 %4, %4, 12\n\tv_add_u32 %5, %4, %7\n\t"
               "global_load_lds_dwordx4 %5, %6 nt\n\ts_add_u32 m0, m0, 4096\n\ts_add_u32 %0, %0, 1\n\ts_add_u32 %2, %2, %11\n\ts_sub_u32 %1, %1, 1\n\ts_cmp_lg_u32 %1, 0\n\ts_cbranch_scc1 1b"
               : "+&s"(p0), "+&s"(n), "+&s"(tnext), "=&v"(f), "=&s"(sf), "=&v"(vt)
               : "s"(sbase), "v"(voff), "s"(ldst), "s"(rot), "n"(WND - 1), "n"(DELTA), "v"(taddr) : "memory", "scc", "s60", "s61");
}
template <int SL = 1> __device__ __forceinline__ void wait_until(unsigned taddr, unsigned t) {
  unsigned f, sf;
  asm volatile("2:\n\tds_read_b32 %0, %2\n\ts_memrealtime s[60:61]\n\ts_waitcnt lgkmcnt(0)\n\tv_readfirstlane_b32 %1, %0\n\ts_sub_u32 %1, s60, %1\n\ts_cmp_ge_i32 %1, %3\n\ts_cbranch_scc1 3f\n\ts_sleep %4\n\ts_branch 2b\n3:"
               : "=&v"(f), "=&s"(sf) : "v"(taddr), "s"(t), "n"(SL) : "memory", "scc", "s60", "s61");
}
template <int N> __device__ __forceinline__ void wait_vm() { asm volatile("s_waitcnt vmcnt(%0)" :: "n"(N) : "memory"); }
#define BARRIER() asm volatile("s_waitcnt lgkmcnt(0)\n\ts_barrier" ::: "memory")
#define RELOAD() asm volatile("" ::: "memory")
__device__ __forceinline__ unsigned lds_add(unsigned addr) {
  unsigned old, one = 1u;
  asm volatile("s_waitcnt lgkmcnt(0)\n\ts_mov_b64 exec, 1\n\tds_add_rtn_u32 %0, %1, %2\n\ts_waitcnt lgkmcnt(0)\n\ts_mov_b64 exec, -1" : "=&v"(old) : "v"(addr), "v"(one) : "memory");
  return __builtin_amdgcn_readfirstlane(old);
}
__device__ __forceinline__ void st_granule(unsigned char* ws, unsigned off, float v, unsigned tag) {
  __hip_atomic_store((GAS unsigned long long*)(ws + off), ((unsigned long long)tag << 32) | f2u(v), RLX_AGENT);
}
__device__ __forceinline__ v4u ld_sc1(__amdgpu_buffer_rsrc_t r, int off) { return __builtin_amdgcn_raw_buffer_load_b128(r, off, 0, 16); }
__device__ __forceinline__ unsigned tag_bad(v4u d, unsigned tag) { return (d[1] ^ tag) | (d[3] ^ tag); }
#define PL4(a, b, c, d) "global_load_dwordx4 " a ", %[off], %[base] sc1\n\tglobal_load_dwordx4 " b ", %[off], %[base] offset:1024 sc1\n\t" \
                        "global_load_dwordx4 " c ", %[off], %[base] offset:2048 sc1\n\tglobal_load_dwordx4 " d ", %[off], %[base] offset:3072 sc1\n\t"
#define PL4A PL4("v[96:99]", "v[100:103]", "v[104:107]", "v[108:111]")
#define PL4B PL4("v[112:115]", "v[116:119]", "v[120:123]", "v[124:127]")
#define PCHK(a, b, c, d, e, f, g, h) "v_xor_b32 %[t0], %[tag], " a "\n\tv_xor_b32 %[t1], %[tag], " b "\n\tv_xor_b32 %[t2], %[tag], " c "\n\tv_or3_b32 %[t0], %[t0], %[t1], %[t2]\n\t" \
  "v_xor_b32 %[t1], %[tag], " d "\n\tv_xor_b32 %[t2], %[tag], " e "\n\tv_or3_b32 %[t0], %[t0], %[t1], %[t2]\n\t" \
  "v_xor_b32 %[t1], %[tag], " f "\n\tv_xor_b32 %[t2], %[tag], " g "\n\tv_or3_b32 %[t0], %[t0], %[t1], %[t2]\n\t" \
  "v_xor_b32 %[t1], %[tag], " h "\n\tv_or_b32 %[t0], %[t0], %[t1]\n\tv_cmp_ne_u32 vcc, 0, %[t0]\n\t"
template <int GAP> __device__ __forceinline__ void poll4(float (&x)[4], float (&y)[4], const unsigned char* base, unsigned off, unsigned tag) {
  unsigned t0, t1, t2, cnt = SPIN_CAP;
  asm volatile(PL4A "s_waitcnt vmcnt(0)\n\t" PCHK("v97", "v99", "v101", "v103", "v105", "v107", "v109", "v111") "s_cbranch_vccz 2f\n\t"
               PL4A "s_sleep %[gap]\n"
               "1:\n\t" PL4B "s_waitcnt vmcnt(4)\n\t" PCHK("v97", "v99", "v101", "v103", "v105", "v107", "v109", "v111") "s_cbranch_vccz 2f\n\t"
               PL4A "s_waitcnt vmcnt(4)\n\t" PCHK("v113", "v115", "v117", "v119", "v121", "v123", "v125", "v127") "s_cbranch_vccz 3f\n\t"
               "s_sub_u32 %[cnt], %[cnt], 1\n\ts_cmp_lg_u32 %[cnt], 0\n\ts_cbranch_scc1 1b\n"
               "2:\n\tv_mov_b32 %[x0], v96\n\tv_mov_b32 %[y0], v98\n\tv_mov_b32 %[x1], v100\n\tv_mov_b32 %[y1], v102\n\tv_mov_b32 %[x2], v104\n\tv_mov_b32 %[y2], v106\n\tv_mov_b32 %[x3], v108\n\tv_mov_b32 %[y3], v110\n\ts_branch 4f\n"
               "3:\n\tv_mov_b32 %[x0], v112\n\tv_mov_b32 %[y0], v114\n\tv_mov_b32 %[x1], v116\n\tv_mov_b32 %[y1], v118\n\tv_mov_b32 %[x2], v120\n\tv_mov_b32 %[y2], v122\n\tv_mov_b32 %[x3], v124\n\tv_mov_b32 %[y3], v126\n"
               "4:"
               : [x0] "=&v"(x[0]), [y0] "=&v"(y[0]), [x1] "=&v"(x[1]), [y1] "=&v"(y[1]), [x2] "=&v"(x[2]), [y2] "=&v"(y[2]), [x3] "=&v"(x[3]), [y3] "=&v"(y[3]),
                 [t0] "=&v"(t0), [t1] "=&v"(t1), [t2] "=&v"(t2), [cnt] "+&s"(cnt)
               : [off] "v"(off), [base] "s"(base), [tag] "s"(tag), [gap] "n"(GAP)
               : "memory", "vcc", "scc", "v96", "v97", "v98", "v99", "v100", "v101", "v102", "v103", "v104", "v105", "v106", "v107", "v108", "v109", "v110", "v111",
                 "v112", "v113", "v114", "v115", "v116", "v117", "v118", "v119", "v120", "v121", "v122", "v123", "v124", "v125", "v126", "v127");
}
#define PL5(a, b, c, d, e) PL4(a, b, c, d) "global_load_dwordx4 " e ", %[off5], %[base] sc1\n\t"
#define PL5A PL5("v[88:91]", "v[92:95]", "v[96:99]", "v[100:103]", "v[104:107]")
#define PL5B PL5("v[108:111]", "v[112:115]", "v[116:119]", "v[120:123]", "v[124:127]")
#define PCHK5(a, b, c, d, e, f, g, h, i, j) "v_xor_b32 %[t0], %[tag], " a "\n\tv_xor_b32 %[t1], %[tag], " b "\n\tv_xor_b32 %[t2], %[tag], " c "\n\tv_or3_b32 %[t0], %[t0], %[t1], %[t2]\n\t" \
  "v_xor_b32 %[t1], %[tag], " d "\n\tv_xor_b32 %[t2], %[tag], " e "\n\tv_or3_b32 %[t0], %[t0], %[t1], %[t2]\n\t" \
  "v_xor_b32 %[t1], %[tag], " f "\n\tv_xor_b32 %[t2], %[tag], " g "\n\tv_or3_b32 %[t0], %[t0], %[t1], %[t2]\n\t" \
  "v_xor_b32 %[t1], %[tag], " h "\n\tv_xor_b32 %[t2], %[tag], " i "\n\tv_or3_b32 %[t0], %[t0], %[t1], %[t2]\n\t" \
  "v_xor_b32 %[t1], %[tag], " j "\n\tv_or_b32 %[t0], %[t0], %[t1]\n\tv_cmp_ne_u32 vcc, 0, %[t0]\n\t"
template <int GAP> __device__ __forceinline__ void poll5(float (&x)[4], float (&y)[4], float& m, float& l, const unsigned char* base, unsigned off, unsigned off5, unsigned tag) {
  unsigned t0, t1, t2, cnt = SPIN_CAP;
  asm volatile(PL5A "s_sleep %[gap]\n"
               "1:\n\t" PL5B "s_waitcnt vmcnt(5)\n\t" PCHK5("v89", "v91", "v93", "v95", "v97", "v99", "v101", "v103", "v105", "v107") "s_cbranch_vccz 2f\n\t"
               PL5A "s_waitcnt vmcnt(5)\n\t" PCHK5("v109", "v111", "v113", "v115", "v117", "v119", "v121", "v123", "v125", "v127") "s_cbranch_vccz 3f\n\t"
               "s_sub_u32 %[cnt], %[cnt], 1\n\ts_cmp_lg_u32 %[cnt], 0\n\ts_cbranch_scc1 1b\n"
               "2:\n\tv_mov_b32 %[m], v104\n\tv_mov_b32 %[l], v106\n\tv_mov_b32 %[x0], v88\n\tv_mov_b32 %[y0], v90\n\tv_mov_b32 %[x1], v92\n\tv_mov_b32 %[y1], v94\n\tv_mov_b32 %[x2], v96\n\tv_mov_b32 %[y2], v98\n\tv_mov_b32 %[x3], v100\n\tv_mov_b32 %[y3], v102\n\ts_branch 4f\n"
               "3:\n\tv_mov_b32 %[m], v124\n\tv_mov_b32 %[l], v126\n\tv_mov_b32 %[x0], v108\n\tv_mov_b32 %[y0], v110\n\tv_mov_b32 %[x1], v112\n\tv_mov_b32 %[y1], v114\n\tv_mov_b32 %[x2], v116\n\tv_mov_b32 %[y2], v118\n\tv_mov_b32 %[x3], v120\n\tv_mov_b32 %[y3], v122\n"
               "4:"
               : [x0] "=&v"(x[0]), [y0] "=&v"(y[0]), [x1] "=&v"(x[1]), [y1] "=&v"(y[1]), [x2] "=&v"(x[2]), [y2] "=&v"(y[2]), [x3] "=&v"(x[3]), [y3] "=&v"(y[3]), [m] "=&v"(m), [l] "=&v"(l),
                 [t0] "=&v"(t0), [t1] "=&v"(t1), [t2] "=&v"(t2), [cnt] "+&s"(cnt)
               : [off] "v"(off), [off5] "v"(off5), [base] "s"(base), [tag] "s"(tag), [gap] "n"(GAP)
               : "memory", "vcc", "scc", "v88", "v89", "v90", "v91", "v92", "v93", "v94", "v95", "v96", "v97", "v98", "v99", "v100", "v101", "v102", "v103", "v104", "v105", "v106", "v107", "v108", "v109", "v110", "v111",
                 "v112", "v113", "v114", "v115", "v116", "v117", "v118", "v119", "v120", "v121", "v122", "v123", "v124", "v125", "v126", "v127");
}
template <int CTRL> __device__ __forceinline__ float dppx(float x) { return __builtin_bit_cast(float, __builtin_amdgcn_update_dpp(0, __builtin_bit_cast(int, x), CTRL, 0xf, 0xf, true)); }
__device__ __forceinline__ bool none(unsigned bad) { return __builtin_amdgcn_ballot_w64(bad != 0u) == 0ull; }

__global__ void __launch_bounds__(NTHR, 4) fused_decode(Params p) {
  __shared__ __attribute__((aligned(16))) unsigned char lds_raw[L_END];
  LAS unsigned char* L = (LAS unsigned char*)lds_raw;
  unsigned long long pc0, tt0;
  asm volatile("s_memrealtime %0" : "=s"(tt0));
  asm volatile("s_getpc_b64 %0" : "=s"(pc0));
  const int tid = threadIdx.x, lane = tid & 63, wave = __builtin_amdgcn_readfirstlane(tid >> 6);
  const int b = blockIdx.x, g = b & 15, s = b >> 4, br = g >> 3, kvh = g & 7;
  const int go = g ^ XG, kvo = kvh ^ XG;
  const int lr = lane >> 4, lc = lane & 15;
  const size_t kvoff = ((size_t)kvh * SK + (SK - WIN) + s * 256) * DH;
  const u16 *px = p.x, *pgw = p.gw, *pwq = p.wq[br], *pk = p.k[br], *pv = p.v[br], *pwo = p.wo[br];
  u16* pout = p.out; unsigned char* pws = p.ws;
  asm volatile("s_waitcnt lgkmcnt(0)" : "+s"(px), "+s"(pgw), "+s"(pwq), "+s"(pk), "+s"(pv), "+s"(pwo), "+s"(pout), "+s"(pws), "+s"(tt0));
  const unsigned t0 = (unsigned)tt0;

  if (wave >= NCW && wave < NCW + NLW) {
    const int j = wave - NCW;
    __builtin_amdgcn_s_setprio(3);
    if (j == 0) {
      *(LAS unsigned*)(L + L_CNT + 4 * lane) = (L_CNT + 4 * lane == C_TG || L_CNT + 4 * lane == C_TQ) ? t0 : 0u;
      if (lane < 32) { *(LAS unsigned*)(L + L_MX + 4 * lane) = EMPTY; *(LAS unsigned*)(L + L_O + 4 * lane) = lane < 16 ? EMPTY : 0u; }
    }
    const unsigned char* kb = (const unsigned char*)(pk + kvoff) + j * 1024;
    const unsigned char* vb = (const unsigned char*)(pv + kvoff) + j * 1024;
    const GAS v4u* wb = (const GAS v4u*)(pwo + ((size_t)kvo * 512 + s * 32 + j * 8) * DM) + lane;
    const unsigned ko = lr * 256 + ((lc ^ (4 * j + lr)) << 4);
    const unsigned vo = lr * 256 + ((lc ^ ((lr | ((j & 2) << 1)) << 1)) << 4);
    const unsigned ldum = L_DUM + 8 * lane;
    BARRIER();
    __builtin_amdgcn_s_sleep(KDELAY);
    const unsigned rot = 2 * j + 8 * s + ((7 * g) >> 1);
    dma_run<KWND, DK>(kb, ko, L_K + j * 1024, 0, 16, rot, C_TG, TK0);
    dma_run<VWND, DV>(vb, vo, L_V + j * 1024, 0, VWND - 1, rot, C_TG, TV0);
    wait_vm<VWND - 1>();
    *(volatile LAS unsigned*)(L + (lane == 0 ? C_KL + 4 * j : ldum)) = 1u;
    dma_run<VWND, DV>(vb, vo, L_V + j * 1024, VWND - 1, 8, rot, C_TG, TV0 + (VWND - 1) * DV);
    wait_vm<VWND - 1>();
    *(volatile LAS unsigned*)(L + (lane == 0 ? C_VH + 4 * j : ldum)) = 1u;
    dma_run<VWND, DV>(vb, vo, L_V + j * 1024, VWND + 7, 9 - VWND, rot, C_TG, TV0 + (VWND + 7) * DV);
    v4u w[8];
#pragma unroll
    for (int i = 0; i < WOA; ++i) {
      if (i == 0) wait_until(C_TG, TW0);
      wait_vm<WWND - 1>();
      if (i) wait_until(C_TG, TW0 + i * DW);
      w[i] = __builtin_nontemporal_load(wb + ((i + rot) & 7) * 64);
      RELOAD();
    }
    wait_vm<WOA>();
    BARRIER();
#pragma clang loop unroll(disable)
    for (unsigned i = 0; i < SPIN_CAP; ++i) if (none(*(volatile LAS unsigned*)(L + C_OP + 4 * (lane & 7)) == 0u)) break;
    RELOAD();
#pragma unroll
    for (int i = WOA; i < 8; ++i) w[i] = __builtin_nontemporal_load(wb + ((i + rot) & 7) * 64);
    RELOAD();
#pragma unroll
    for (int i = 0; i < 8; ++i) { const unsigned r = (i + rot) & 7; *(LAS v4u*)(L + L_WO + (8 * j + r) * 1024 + ((lane ^ (2 * r)) << 4)) = w[i]; }
    *(volatile LAS unsigned*)(L + (lane == 0 ? C_WO + 4 * j : ldum)) = 1u;
    return;
  }

  const __amdgpu_buffer_rsrc_t wsr = __builtin_amdgcn_make_buffer_rsrc(pws, 0, W_END, 0x00020000);
  if (wave == NCW + NLW) {
    unsigned c0 = 0, c1 = 0;
    if (b < CODEPF) {
      const GAS unsigned char* cp = (const GAS unsigned char*)(pc0 & ~127ull) + lane * 128;
      c0 = *(const GAS unsigned*)cp; if (lane < CODEPF_TAIL) c1 = *(const GAS unsigned*)(cp + 8192);
    }
    asm volatile("s_barrier" ::: "memory");
    unsigned ep;
    asm volatile("s_load_dword %0, %1, 0x0\n\ts_waitcnt lgkmcnt(0)" : "=s"(ep) : "s"(pws + W_E) : "memory");
    const unsigned tag = ep + 1u;
    __builtin_amdgcn_raw_buffer_store_b64(v2u{t0, tag}, wsr, (b < 8 && lane == 0) ? W_T + 8 * b : W_END, 0, 16);
    asm volatile("" :: "v"(c0), "v"(c1));
    __builtin_amdgcn_s_sleep(TSLEEP);
    v2u ts;
#pragma clang loop unroll(disable)
    for (unsigned i = 0; i < SPIN_CAP; ++i) { RELOAD(); ts = __builtin_amdgcn_raw_buffer_load_b64(wsr, W_T + 8 * (lane & 7), 0, 16); if (none(ts[1] ^ tag)) break; }
    int d = min(max((int)(ts[0] - t0), -400), 400);
    const unsigned tq = t0 + (unsigned)__builtin_amdgcn_readlane(d, kvo);
    d += __builtin_amdgcn_mov_dpp(d, 0xB1, 0xf, 0xf, true); d += __builtin_amdgcn_mov_dpp(d, 0x4E, 0xf, 0xf, true); d += __builtin_amdgcn_mov_dpp(d, 0x124, 0xf, 0xf, true);
    const unsigned tg = t0 + (unsigned)(__builtin_amdgcn_readfirstlane(d) >> 3);
    *(volatile LAS unsigned*)(L + (lane == 0 ? C_TG : lane == 1 ? C_TQ : L_DUM + 8 * lane)) = lane == 1 ? tq : tg;
    unsigned moff = W_ML + ((go * 16 + ((lc & 12) | ((lc + lr) & 3))) * 4 + (s >> 2)) * 16;
    unsigned oaddr = lane < 16 ? L_O + 16 * ((lane >> 1) & 3) + 8 * (lane >> 3) + 4 * (lane & 1) : L_DUM + 8 * lane;
    unsigned ooff = W_O + (go * 16 + s) * 4096 + lane * 16;
    asm volatile("" : "+v"(moff), "+v"(oaddr), "+v"(ooff));
    BARRIER();
    __builtin_amdgcn_s_setprio(3);
    float x[4], y[4], m, l;
    __builtin_amdgcn_s_sleep(TUNE1);
    poll5<GAP1>(x, y, m, l, pws, ooff, moff, tag);
    float M = m;
    M = fmaxf(M, dppx<0x128>(M)); M = fmaxf(M, dppx<0x124>(M)); M = fmaxf(M, dppx<0x122>(M)); M = fmaxf(M, dppx<0x121>(M));
    const float e = __builtin_amdgcn_exp2f(m - M);
    float Ls = e * l;
    Ls += dppx<0x128>(Ls); Ls += dppx<0x124>(Ls); Ls += dppx<0x122>(Ls); Ls += dppx<0x121>(Ls);
    const float wn = e * __builtin_amdgcn_rcpf(Ls);
    const float wgt[4] = {dppx<0x150>(wn), dppx<0x154>(wn), dppx<0x158>(wn), dppx<0x15C>(wn)};
    float ov = rs32(fmaf(wgt[0], x[0], wgt[1] * x[1]) + fmaf(wgt[2], x[2], wgt[3] * x[3]), fmaf(wgt[0], y[0], wgt[1] * y[1]) + fmaf(wgt[2], y[2], wgt[3] * y[3]));
    ov = rs16(ov, ov);
    const auto sw = __builtin_amdgcn_permlane32_swap(f2u(ov), f2u(ov), false, false);
    *(LAS unsigned*)(L + oaddr) = cvtpk(u2f(sw[0]), u2f(sw[1]));
    return;
  }

  const int cw = wave;
  __builtin_amdgcn_s_setprio(2);
  unsigned tag;
  float gate = 0.f;
  const int q4 = lc >> 2, p4 = lane & 3;
  const bool qb0 = (lane & 4) != 0, qb1 = (lane & 8) != 0;
  const unsigned dum = L_DUM + 8 * lane;
  {
    const int kq = s >> 2, hq = s & 3, row0 = 128 * kq + 16 * cw;
    const GAS v4u* wp = (const GAS v4u*)(pwq + (size_t)(row0 + lr) * NQ + kvo * 512 + 128 * hq + 8 * lc);
    v4u w[4];
#pragma unroll
    for (int i = 0; i < 4; ++i) w[i] = __builtin_nontemporal_load(wp + (size_t)i * (NQ / 2));
    typedef unsigned s8u __attribute__((ext_vector_type(8)));
    s8u xs;
    asm volatile("s_load_dwordx8 %0, %1, 0x0" : "=s"(xs) : "s"(px + row0) : "memory");
    const bool gw = wave == 0 && b < 32;
    const v4u gx = *(gw ? (const GAS v4u*)(px + 8 * lane) : wp), gg = *(gw ? (const GAS v4u*)(pgw + 8 * lane) : wp);
    RELOAD();
    unsigned tlb = 0;
    {
      const unsigned char *a0 = (const unsigned char*)(pk + kvoff), *a1 = (const unsigned char*)(pv + kvoff), *a2 = (const unsigned char*)(pwo + ((size_t)kvo * 512 + s * 32) * DM), *a3 = pws + W_F + b * 4096;
      asm volatile("" : "+s"(a0), "+s"(a1), "+s"(a2), "+s"(a3));
      const unsigned char* a = (wave & 2) ? ((wave & 1) ? a3 : a2) : ((wave & 1) ? a1 : a0);
      asm volatile("s_load_dword %0, %1, 0x0" : "=s"(tlb) : "s"(a) : "memory");
    }
    asm volatile("s_barrier" ::: "memory");
    unsigned ep;
    asm volatile("s_load_dword %0, %1, 0x0" : "=s"(ep) : "s"(pws + W_E) : "memory");
    asm volatile("s_waitcnt lgkmcnt(0)" : "+s"(xs), "+s"(ep), "+s"(tlb), "+v"(w[0]), "+v"(w[1]) :: "memory");
    tag = ep + 1u;
    const unsigned xd[4] = {lr & 2 ? xs[1] : xs[0], lr & 2 ? xs[3] : xs[2], lr & 2 ? xs[5] : xs[4], lr & 2 ? xs[7] : xs[6]};
    const unsigned xsel = lr & 1 ? 0x07060302u : 0x05040100u;
    float acc[8];
#pragma unroll
    for (int k = 0; k < 8; ++k) acc[k] = 0.f;
#pragma unroll
    for (int i = 0; i < 4; i += 2) {
      const unsigned xp = __builtin_amdgcn_perm(xd[i + 1], xd[i], xsel);
#pragma unroll
      for (int d = 0; d < 4; ++d) {
        acc[2 * d] = dot2(__builtin_amdgcn_perm(w[i + 1][d], w[i][d], 0x05040100u), xp, acc[2 * d]);
        acc[2 * d + 1] = dot2(__builtin_amdgcn_perm(w[i + 1][d], w[i][d], 0x07060302u), xp, acc[2 * d + 1]);
      }
    }
    const float r0 = rs32(acc[0], acc[4]), r1 = rs32(acc[1], acc[5]), r2 = rs32(acc[2], acc[6]), r3 = rs32(acc[3], acc[7]);
    const float u0 = rs16(r0, r2), u1 = rs16(r1, r3);
    *(LAS f32x2*)(L + L_QP + (cw * 128 + 8 * lc + 4 * (lane >> 5) + 2 * (lr & 1)) * 4) = f32x2{u0, u1};
    if (__builtin_expect(lds_add(C_QP) == NCW - 1, 1)) {
      f32x2 qs = {0.f, 0.f};
#pragma unroll
      for (int i = 0; i < 8; i += 2) qs += *(const LAS f32x2*)(L + L_QP + (i * 128 + 2 * lane) * 4) + *(const LAS f32x2*)(L + L_QP + ((i + 1) * 128 + 2 * lane) * 4);
      __builtin_amdgcn_raw_buffer_store_b128(v4u{f2u(qs[0]), tag, f2u(qs[1]), tag}, wsr, W_Q + (go * 4 + kq) * 4096 + (128 * hq + 2 * lane) * 8, 0, 16);
    }
    v4u gx_ = gx, gg_ = gg;
    asm volatile("" : "+v"(gx_), "+v"(gg_));
    if (__builtin_expect(gw, 1)) {
      float z = 0.f;
#pragma unroll
      for (int d = 0; d < 4; ++d) z += rbf(bflo(gx_[d]) * bflo(gg_[d])) + rbf(bfhi(gx_[d]) * bfhi(gg_[d]));
      z += dpp<0xB1>(z); z += dpp<0x4E>(z); z += dpp<0x124>(z); z += dpp<0x128>(z); z = xrow_sum(z);
      gate = rbf(__builtin_amdgcn_rcpf(1.f + __expf(-rbf(z))));
    }
  }
  {
    const int off = W_Q + g * 16384 + (lane >> 5) * 4096 + (64 * cw + 2 * (lane & 31)) * 8;
    v4u a, a2;
    __builtin_amdgcn_s_sleep(TUNEQ);
    wait_until<3>(C_TQ, TQ);
#pragma clang loop unroll(disable)
    for (unsigned i = 0; i < SPIN_CAP; ++i) {
      RELOAD(); a = ld_sc1(wsr, off); a2 = ld_sc1(wsr, off + 8192);
      if (none(tag_bad(a, tag) | tag_bad(a2, tag))) break;
    }
    const float qv = rs32(u2f(a[0]) + u2f(a2[0]), u2f(a[2]) + u2f(a2[2]));
    *(LAS u16*)(L + L_Q + (cw >> 1) * Q_STR + ((cw & 1) * 64 + 2 * (lane & 31) + (lane >> 5)) * 2) = (u16)cvtpk(qv, qv);
    *(volatile LAS unsigned*)(L + (lane == 0 ? C_FLAG + 4 * cw : dum)) = 1u;
  }
  unsigned ka = L_K + (32 * cw + lc) * 256 + ((lr ^ lc) << 4), qa = L_Q + p4 * Q_STR + 16 * lr, pa = L_P + p4 * P_STR + 16 * lr;
  unsigned pw = (lane & 12) == 0 ? L_P + p4 * P_STR + (32 * cw + 4 * lr) * 2 : dum, pw2 = (lane & 12) == 0 ? pw + 32 : dum;
  unsigned va = L_V + (8 * lr + q4) * 256 + (((2 * cw + (p4 >> 1)) ^ ((q4 | ((lr & 1) << 2)) << 1)) << 4) + 8 * (p4 & 1);
  unsigned wa = L_WO + (4 * lr + q4) * 1024 + (((8 * cw + (p4 >> 1)) ^ ((q4 | ((lr & 1) << 2)) << 1)) << 4) + 8 * (p4 & 1);
  unsigned oo = W_O + ((g * 16 + 4 * p4 + (cw >> 1)) * 16 + s) * 256 + (16 * (cw & 1) + 4 * lr + q4) * 8;
  unsigned fo = W_F + ((4 * cw + p4) * 256 + go * 16 + s) * 128 + (4 * lr + q4) * 8;
  unsigned opa = lane == 0 ? C_OP + 4 * cw : dum;
  asm volatile("" : "+v"(opa));
  unsigned ba0 = p4 == 0 ? L_O + 16 * lr : L_Z, ba1 = p4 == 1 ? L_O + 16 * lr : L_Z, ba2 = p4 == 2 ? L_O + 16 * lr : L_Z, ba3 = p4 == 3 ? L_O + 16 * lr : L_Z;
  asm volatile("" : "+v"(ka), "+v"(qa), "+v"(pw), "+v"(pw2), "+v"(pa), "+v"(va), "+v"(wa), "+v"(oo), "+v"(fo), "+v"(ba0), "+v"(ba1), "+v"(ba2), "+v"(ba3));
#pragma clang loop unroll(disable)
  for (unsigned i = 0; i < SPIN_CAP; ++i) if (none(*(volatile LAS unsigned*)(L + C_FLAG + 4 * (lane < 12 ? lane : 0)) == 0u)) break;
  RELOAD();
  float mc;
  {
    v4u qf[4], kf[2][4];
#pragma unroll
    for (int ks = 0; ks < 4; ++ks) { kf[0][ks] = *(const LAS v4u*)(L + (ka ^ (64 * ks))); qf[ks] = *(const LAS v4u*)(L + qa + 64 * ks); kf[1][ks] = *(const LAS v4u*)(L + (ka ^ (64 * ks)) + 4096); }
    __builtin_amdgcn_sched_barrier(0);
    f32x4 sc[2] = {{0.f, 0.f, 0.f, 0.f}, {0.f, 0.f, 0.f, 0.f}};
#pragma unroll
    for (int ks = 0; ks < 4; ++ks) { sc[0] = mfma(kf[0][ks], qf[ks], sc[0]); sc[1] = mfma(kf[1][ks], qf[ks], sc[1]); }
    float m = fmaxf(fmaxf(fmaxf(sc[0][0], sc[0][1]), fmaxf(sc[0][2], sc[0][3])), fmaxf(fmaxf(sc[1][0], sc[1][1]), fmaxf(sc[1][2], sc[1][3])));
    m = xrow_max(m);
    *(volatile LAS float*)(L + (lane < 4 ? L_MX + (lane * 8 + cw) * 4 : dum)) = m;
    constexpr float C2 = 0.08838834764831845f * 1.4426950408889634f;
    v4u mx0, mx1;
#pragma clang loop unroll(disable)
    for (unsigned i = 0; i < SPIN_CAP; ++i) {
      mx0 = *(volatile LAS v4u*)(L + L_MX + 32 * p4); mx1 = *(volatile LAS v4u*)(L + L_MX + 32 * p4 + 16);
      const unsigned top = max(max(max(mx0[0], mx0[1]), max(mx0[2], mx0[3])), max(max(mx1[0], mx1[1]), max(mx1[2], mx1[3])));
      if (none(top == EMPTY)) break;
    }
    mc = fmaxf(fmaxf(fmaxf(u2f(mx0[0]), u2f(mx0[1])), fmaxf(u2f(mx0[2]), u2f(mx0[3]))), fmaxf(fmaxf(u2f(mx1[0]), u2f(mx1[1])), fmaxf(u2f(mx1[2]), u2f(mx1[3])))) * C2;
    float ls = 0.f;
    v2u pk[2];
#pragma unroll
    for (int t = 0; t < 2; ++t) {
      float e[4];
#pragma unroll
      for (int r = 0; r < 4; ++r) { e[r] = __builtin_amdgcn_exp2f(fmaf(sc[t][r], C2, -mc)); ls += e[r]; }
      pk[t] = v2u{cvtpk(e[0], e[1]), cvtpk(e[2], e[3])};
    }
    ls = xrow_sum(ls);
    *(LAS v2u*)(L + pw) = pk[0]; *(LAS v2u*)(L + pw2) = pk[1];
    *(LAS float*)(L + (lane < 4 ? L_LS + (lane * 8 + cw) * 4 : dum)) = ls;
    *(volatile LAS unsigned*)(L + (lane == 0 ? C_PF + 4 * cw : dum)) = 1u;
  }
  f32x4 o = {0.f, 0.f, 0.f, 0.f};
  v4u pf[8];
  {
#pragma clang loop unroll(disable)
    for (unsigned i = 0; i < SPIN_CAP; ++i) if (none(*(volatile LAS unsigned*)(L + C_PF + 4 * (lane < 12 ? lane : 0)) == 0u)) break;
    RELOAD();
    v4u vf[4];
#pragma unroll
    for (int ks = 0; ks < 4; ++ks) { vf[ks] = tr_pair(L + va + 8192 * ks, 1024); pf[ks] = *(const LAS v4u*)(L + pa + 64 * ks); }
#pragma unroll
    for (int ks = 4; ks < 8; ++ks) pf[ks] = *(const LAS v4u*)(L + pa + 64 * ks);
    if (__builtin_expect(cw == NCW - 1, 1)) {
      const f32x4 l0 = *(const LAS f32x4*)(L + L_LS + 32 * p4), l1 = *(const LAS f32x4*)(L + L_LS + 32 * p4 + 16);
      const v4u ml = {f2u(mc), tag, f2u(((l0[0] + l0[1]) + (l0[2] + l0[3])) + ((l1[0] + l1[1]) + (l1[2] + l1[3]))), tag};
      __builtin_amdgcn_raw_buffer_store_b128(ml, wsr, lane < 4 ? W_ML + ((g * 16 + s) * 4 + lane) * 16 : W_END, 0, 16);
    }
#pragma unroll
    for (int ks = 0; ks < 4; ++ks) o = mfma(vf[ks], pf[ks], o);
    asm volatile("" : "+v"(o), "+v"(pf[4]), "+v"(pf[5]), "+v"(pf[6]), "+v"(pf[7]));
  }
  BARRIER();
  {
    v4u vf[4];
#pragma unroll
    for (int ks = 0; ks < 4; ++ks) vf[ks] = tr_pair(L + va + 8192 * (ks + 4), 1024);
    __builtin_amdgcn_sched_barrier(0);
#pragma unroll
    for (int ks = 0; ks < 4; ++ks) o = mfma(vf[ks], pf[ks + 4], o);
    st_granule(pws, oo, pick4(o, qb0, qb1), tag);
    RELOAD(); *(volatile LAS unsigned*)(L + opa) = 1u;
  }
  {
#pragma clang loop unroll(disable)
    for (unsigned i = 0; i < SPIN_CAP; ++i) if (none(*(volatile LAS unsigned*)(L + C_WO + 4 * (lane & 3)) == 0u)) break;
    RELOAD();
    v4u wf[4];
#pragma unroll
    for (int t = 0; t < 4; ++t) wf[t] = tr_pair(L + (wa ^ (32 * t)), 16384);
    asm volatile("" : "+v"(wf[0]), "+v"(wf[1]), "+v"(wf[2]), "+v"(wf[3]));
    v4u b0, b1, b2, b3;
#pragma clang loop unroll(disable)
    for (unsigned i = 0; i < SPIN_CAP; ++i) {
      b0 = *(volatile LAS v4u*)(L + ba0); b1 = *(volatile LAS v4u*)(L + ba1); b2 = *(volatile LAS v4u*)(L + ba2); b3 = *(volatile LAS v4u*)(L + ba3);
      if (none(((b0[0] | b1[0]) | (b2[0] | b3[0])) == EMPTY)) break;
    }
    f32x4 r = {0.f, 0.f, 0.f, 0.f};
    r = mfma(wf[0], b0, r); r = mfma(wf[1], b1, r); r = mfma(wf[2], b2, r); r = mfma(wf[3], b3, r);
    st_granule(pws, fo, pick4(r, qb0, qb1), tag);
  }
  if (__builtin_expect(b < 32, 1)) {
    unsigned ra_ = (lane & 24) == 0 ? L_RED + ((2 * (lane & 7) + (lane >> 5)) * 8 + cw) * 4 : dum;
    float g1 = rbf(1.f - gate);
    asm volatile("" : "+v"(ra_), "+v"(g1));
    __builtin_amdgcn_s_sleep(TUNE2);
    wait_until(C_TG, TF);
    float x[4], y[4];
    poll4<GAP2>(x, y, pws, W_F + b * 32768 + cw * 4096 + lane * 16, tag);
    float t = rs32((x[0] + x[1]) + (x[2] + x[3]), (y[0] + y[1]) + (y[2] + y[3]));
    t = rs16(t, t); t += dpp<0x128>(t);
    *(LAS float*)(L + ra_) = t;
    BARRIER();
    if (__builtin_expect(cw == 0, 1)) {
      const f32x4 ra = *(const LAS f32x4*)(L + L_RED + 32 * lc), rb = *(const LAS f32x4*)(L + L_RED + 32 * lc + 16);
      const float A = rbf((ra[0] + ra[1]) + (ra[2] + ra[3])), B = rbf((rb[0] + rb[1]) + (rb[2] + rb[3]));
      const u16 res = (u16)cvtpk(rbf(gate * A) + rbf(g1 * B), 0.f);
      __builtin_amdgcn_raw_buffer_store_b16(res, __builtin_amdgcn_make_buffer_rsrc(pout, 0, 2 * DM, 0x00020000), lane < 16 ? (16 * b + lane) * 2 : 2 * DM, 0, 16);
      __builtin_amdgcn_raw_buffer_store_b32(tag, wsr, (b | lane) == 0 ? W_E : W_END, 0, 16);
    }
  }
}

extern "C" void kernel_launch(void* const* d_in, const int* in_sizes, int n_in, void* d_out, int out_size, void* d_ws, size_t ws_size, hipStream_t stream) {
  Params p;
  p.x = (const u16*)d_in[0]; p.gw = (const u16*)d_in[10];
  p.wq[0] = (const u16*)d_in[3]; p.wq[1] = (const u16*)d_in[8];
  p.k[0] = (const u16*)d_in[1]; p.k[1] = (const u16*)d_in[6];
  p.v[0] = (const u16*)d_in[2]; p.v[1] = (const u16*)d_in[7];
  p.wo[0] = (const u16*)d_in[4]; p.wo[1] = (const u16*)d_in[9];
  p.out = (u16*)d_out; p.ws = (unsigned char*)d_ws;
  fused_decode<<<NWG, NTHR, 0, stream>>>(p);
}
```
